```python
import torch

def _softmax(x, axis=-1):
    return torch.softmax(x, dim=axis)
def _gelu(x):
    return torch.nn.functional.gelu(x, approximate="tanh")
def _silu(x):
    return torch.nn.functional.silu(x)
def _sigmoid(x):
    return torch.sigmoid(x)
def _layernorm(x, axis=-1, eps=1e-5):
    m = x.mean(dim=axis, keepdim=True)
    v = x.var(dim=axis, keepdim=True, unbiased=False)
    return (x - m) / torch.sqrt(v + eps)
def _scatter_add(tgt, idx, src, axis=0):
    return tgt.clone().index_add_(axis, idx, src)
def _segment_sum(src, seg, n):
    out = torch.zeros((n,) + src.shape[1:], dtype=src.dtype)
    return out.index_add_(0, seg, src)
def _pad(x, pw):
    flat = tuple(v for pair in reversed(pw) for v in pair)
    return torch.nn.functional.pad(x, flat)

kernel_name = 'recurrent_scan__linear_kernel_hedgehog__glu_gated_bilinear__prompt'

def setup_inputs():

    _w = (lambda a: (lambda t: t.to(torch.bfloat16) if t.is_floating_point() else t)(torch.as_tensor(a)))
    def _A_setup():
        B, S, D, H = 1, 2048, 512, 32
        Dh = D // H
        return {
            "x": _w((lambda *s: torch.randn(*s, dtype=torch.bfloat16) * 0.05)(B, S, D)),
            "wqkv": _w((lambda *s: torch.randn(*s, dtype=torch.bfloat16) * 0.8)(D, 3 * D)),
            "w_phi": _w((lambda *s: torch.randn(*s, dtype=torch.bfloat16) * 0.8)(H, Dh, Dh) * (Dh ** -0.5)),
            "wo": _w((lambda *s: torch.randn(*s, dtype=torch.bfloat16) * 0.8)(D, D)),
        }
    _A_in = _A_setup()

    def _B_setup():
        B, S, D, F = 1, 2048, 512, 1792
        return {
            "x": _w((lambda *s: torch.randn(*s, dtype=torch.bfloat16) * 0.05)(B, S, D)),
            "w_gate": _w((lambda *s: torch.randn(*s, dtype=torch.bfloat16) * 0.2)(D, F) * (D ** -0.5)),
            "w_up": _w((lambda *s: torch.randn(*s, dtype=torch.bfloat16) * 0.2)(D, F) * (D ** -0.5)),
            "w_down": _w((lambda *s: torch.randn(*s, dtype=torch.bfloat16) * 0.2)(F, D) * (F ** -0.5)),
        }
    _B_in = _B_setup()

    return {
        "A_x": _A_in["x"],
        "A_wqkv": _A_in["wqkv"],
        "A_w_phi": _A_in["w_phi"],
        "A_wo": _A_in["wo"],
        "B_x": _B_in["x"],
        "B_w_gate": _B_in["w_gate"],
        "B_w_up": _B_in["w_up"],
        "B_w_down": _B_in["w_down"],
    }

def reference(A_x, A_wqkv, A_w_phi, A_wo, B_x, B_w_gate, B_w_up, B_w_down):
    def _A_ref(x, wqkv, w_phi, wo):
        B, S, D = x.shape
        H = 32; Dh = D // H
        qkv = (lambda x, *d: x.permute(*d))((x @ wqkv).reshape(B, S, 3, H, Dh), 2, 0, 3, 1, 4)
        q, k, v = qkv[0], qkv[1], qkv[2]
        pq = torch.einsum("bhsd,hdf->bhsf", q, w_phi)
        pk = torch.einsum("bhsd,hdf->bhsf", k, w_phi)
        phi_q = torch.concatenate([_softmax(pq, axis=-1), _softmax(-pq, axis=-1)], axis=-1)
        phi_k = torch.concatenate([_softmax(pk, axis=-1), _softmax(-pk, axis=-1)], axis=-1)
        kv = torch.einsum("bhsf,bhsd->bhfd", phi_k, v)
        z  = torch.einsum("bhsf->bhf", phi_k)
        num = torch.einsum("bhqf,bhfd->bhqd", phi_q, kv)
        den = torch.einsum("bhqf,bhf->bhq", phi_q, z)[..., None] + 1e-6
        o = num / den
        return (lambda x, *d: x.permute(*d))(o, 0, 2, 1, 3).reshape(B, S, D) @ wo

    def _B_ref(x, w_gate, w_up, w_down):
        g = torch.einsum("bsd,df->bsf", x, w_gate)
        u = torch.einsum("bsd,df->bsf", x, w_up)
        h = g * u
        return torch.einsum("bsf,fd->bsd", h, w_down)

    x = A_x
    xn = _layernorm(x, axis=-1)
    s = _A_ref(xn, A_wqkv, A_w_phi, A_wo)
    g = _silu(_B_ref(xn, B_w_gate, B_w_up, B_w_down))
    return x + s * g
```

```cpp
#include <hip/hip_runtime.h>
#include <hip/hip_bf16.h>
typedef __hip_bfloat16 bf16;
constexpr int S = 2048, D = 512, H = 32, DH = 16, F = 1792;
__device__ __forceinline__ float b2f(bf16 v) { return __bfloat162float(v); }
__device__ __forceinline__ bf16 f2b(float v) { return __float2bfloat16(v); }

__global__ void k_ln(const bf16* x, bf16* xn) {
  int r = blockIdx.x, t = threadIdx.x;
  __shared__ float red[64];
  float v[8], s = 0;
  for (int i = 0; i < 8; ++i) { v[i] = b2f(x[r * D + t * 8 + i]); s += v[i]; }
  for (int o = 32; o; o >>= 1) s += __shfl_xor(s, o);
  float m = s / D, q = 0;
  for (int i = 0; i < 8; ++i) { float d = v[i] - m; q += d * d; }
  for (int o = 32; o; o >>= 1) q += __shfl_xor(q, o);
  float rs = rsqrtf(q / D + 1e-5f);
  for (int i = 0; i < 8; ++i) xn[r * D + t * 8 + i] = f2b((v[i] - m) * rs);
}
__global__ void k_gemm(const bf16* A, const bf16* B, float* C, int M, int N, int K) {
  int n = blockIdx.x * 64 + threadIdx.x, m0 = blockIdx.y * 8;
  float acc[8] = {0};
  for (int k = 0; k < K; ++k) { float b = b2f(B[k * N + n]);
    for (int i = 0; i < 8; ++i) acc[i] += b2f(A[(m0 + i) * K + k]) * b; }
  for (int i = 0; i < 8; ++i) C[(m0 + i) * N + n] = acc[i];
}
__global__ void k_phi(const float* qkv, const bf16* wphi, float* phiq, float* phik) {
  int idx = blockIdx.x * blockDim.x + threadIdx.x;
  int which = idx / (S * H), rem = idx % (S * H), s = rem / H, h = rem % H;
  float q[16], p[16];
  for (int d = 0; d < 16; ++d) q[d] = b2f(f2b(qkv[s * 1536 + which * 512 + h * 16 + d]));
  float mx = -1e30f, mn = 1e30f;
  for (int f = 0; f < 16; ++f) { float a = 0; for (int d = 0; d < 16; ++d) a += q[d] * b2f(wphi[h * 256 + d * 16 + f]); p[f] = a; mx = fmaxf(mx, a); mn = fminf(mn, a); }
  float s1 = 0, s2 = 0;
  for (int f = 0; f < 16; ++f) { s1 += __expf(p[f] - mx); s2 += __expf(mn - p[f]); }
  float* o = (which ? phik : phiq) + (s * H + h) * 32;
  for (int f = 0; f < 16; ++f) { o[f] = __expf(p[f] - mx) / s1; o[16 + f] = __expf(mn - p[f]) / s2; }
}
__global__ void k_kv(const float* qkv, const float* phik, float* kv) {
  int h = blockIdx.x, f = threadIdx.x / 17, d = threadIdx.x % 17;
  float a = 0;
  for (int s = 0; s < S; ++s) a += b2f(f2b(phik[(s * H + h) * 32 + f])) * (d < 16 ? b2f(f2b(qkv[s * 1536 + 1024 + h * 16 + d])) : 1.f);
  kv[(h * 32 + f) * 17 + d] = a;
}
__global__ void k_o(const float* phiq, const float* kv, bf16* o) {
  int idx = blockIdx.x * blockDim.x + threadIdx.x;
  int s = idx / D, c = idx % D, h = c / 16, d = c % 16;
  float num = 0, den = 0;
  for (int f = 0; f < 32; ++f) { float p = phiq[(s * H + h) * 32 + f]; num += p * kv[(h * 32 + f) * 17 + d]; den += p * kv[(h * 32 + f) * 17 + 16]; }
  o[idx] = f2b(num / (den + 1e-6f));
}
__global__ void k_h(const float* g, const float* u, bf16* h) { int i = blockIdx.x * blockDim.x + threadIdx.x; h[i] = f2b(g[i] * u[i]); }
__global__ void k_fin(const bf16* x, const float* s, const float* y, bf16* out) {
  int i = blockIdx.x * blockDim.x + threadIdx.x; float yy = y[i];
  out[i] = f2b(b2f(x[i]) + s[i] * (yy / (1.f + __expf(-yy))));
}
extern "C" void kernel_launch(void* const* d_in, const int* in_sizes, int n_in, void* d_out, int out_size, void* d_ws, size_t ws_size, hipStream_t stream) {
  const bf16 *x = (const bf16*)d_in[0], *wqkv = (const bf16*)d_in[1], *wphi = (const bf16*)d_in[2], *wo = (const bf16*)d_in[3];
  const bf16 *wg = (const bf16*)d_in[5], *wu = (const bf16*)d_in[6], *wd = (const bf16*)d_in[7];
  char* ws = (char*)d_ws; size_t MB = 1 << 20;
  bf16* xn = (bf16*)ws; float* qkv = (float*)(ws + 4 * MB); float* g = (float*)(ws + 20 * MB); float* u = (float*)(ws + 40 * MB);
  float* phiq = (float*)(ws + 60 * MB); float* phik = (float*)(ws + 70 * MB); float* kv = (float*)(ws + 80 * MB);
  bf16* o = (bf16*)(ws + 82 * MB); bf16* h = (bf16*)(ws + 86 * MB); float* s = (float*)(ws + 100 * MB); float* y = (float*)(ws + 110 * MB);
  k_ln<<<S, 64, 0, stream>>>(x, xn);
  k_gemm<<<dim3(1536 / 64, S / 8), 64, 0, stream>>>(xn, wqkv, qkv, S, 1536, D);
  k_gemm<<<dim3(F / 64, S / 8), 64, 0, stream>>>(xn, wg, g, S, F, D);
  k_gemm<<<dim3(F / 64, S / 8), 64, 0, stream>>>(xn, wu, u, S, F, D);
  k_phi<<<2 * S * H / 256, 256, 0, stream>>>(qkv, wphi, phiq, phik);
  k_kv<<<H, 32 * 17, 0, stream>>>(qkv, phik, kv);
  k_o<<<S * D / 256, 256, 0, stream>>>(phiq, kv, o);
  k_h<<<S * F / 256, 256, 0, stream>>>(g, u, h);
  k_gemm<<<dim3(D / 64, S / 8), 64, 0, stream>>>(o, wo, s, S, D, D);
  k_gemm<<<dim3(D / 64, S / 8), 64, 0, stream>>>(h, wd, y, S, D, F);
  k_fin<<<S * D / 256, 256, 0, stream>>>(x, s, y, (bf16*)d_out);
}
```

```cpp
#include <hip/hip_runtime.h>

__global__ void __launch_bounds__(256, 1)
hedgehog_glu_block(const void* x, const void* wqkv, const void* wphi, const void* wo, const void* wg, const void* wu, const void* wd, void* out, void* ws) {
  __shared__ __attribute__((aligned(16))) char lds[163840];
  const unsigned wave = __builtin_amdgcn_readfirstlane(threadIdx.x >> 6);
  const unsigned blk = blockIdx.x;
  asm volatile(
    "s_load_dwordx16 s[12:27], %0, 0x0\n"
    "s_load_dwordx2 s[28:29], %0, 0x40\n"
    "s_mov_b32 s68, %1\n"
    "s_mov_b32 s69, %2\n"
    "v_mbcnt_lo_u32_b32 v0, -1, 0\n"
    "v_mbcnt_hi_u32_b32 v0, -1, v0\n"
    "s_lshr_b32 s85, s68, 3\n"
    "s_and_b32 s86, s85, 7\n"
    "s_bfe_u32 s70, s68, 0x10002\n"
    "s_lshl_b32 s70, s70, 3\n"
    "s_add_u32 s70, s70, s86\n"
    "s_and_b32 s71, s68, 3\n"
    "s_lshl_b32 s71, s71, 1\n"
    "s_bfe_u32 s87, s68, 0x10006\n"
    "s_add_u32 s71, s71, s87\n"
    "s_lshl_b32 s80, s69, 12\n"
    "s_lshl_b32 s81, s69, 11\n"
    "s_lshr_b32 s75, s69, 1\n"
    "s_and_b32 s76, s69, 1\n"
    "s_add_u32 s60, s86, 0\n"
    "s_and_b32 s60, s60, 7\n"
    "s_lshl_b32 s60, s60, 7\n"
    "s_add_u32 s61, s86, 1\n"
    "s_and_b32 s61, s61, 7\n"
    "s_lshl_b32 s61, s61, 7\n"
    "s_add_u32 s62, s86, 2\n"
    "s_and_b32 s62, s62, 7\n"
    "s_lshl_b32 s62, s62, 7\n"
    "s_add_u32 s63, s86, 3\n"
    "s_and_b32 s63, s63, 7\n"
    "s_lshl_b32 s63, s63, 7\n"
    "s_add_u32 s64, s86, 4\n"
    "s_and_b32 s64, s64, 7\n"
    "s_lshl_b32 s64, s64, 7\n"
    "s_add_u32 s65, s86, 5\n"
    "s_and_b32 s65, s65, 7\n"
    "s_lshl_b32 s65, s65, 7\n"
    "s_add_u32 s66, s86, 6\n"
    "s_and_b32 s66, s66, 7\n"
    "s_lshl_b32 s66, s66, 7\n"
    "s_add_u32 s67, s86, 7\n"
    "s_and_b32 s67, s67, 7\n"
    "s_lshl_b32 s67, s67, 7\n"
    "v_lshrrev_b32 v186, 3, v0\n"
    "s_lshl_b32 s30, s69, 15\n"
    "v_lshl_add_u32 v186, v186, 10, s30\n"
    "v_lshrrev_b32 v187, 4, v0\n"
    "v_bitop3_b32 v187, v0, 7, v187 bitop3:0x6a\n"
    "v_lshl_add_u32 v6, v187, 4, v186\n"
    "v_xor_b32 v7, 64, v6\n"
    "v_add_u32 v8, 0x3800, v6\n"
    "v_add_u32 v7, 0x1c00, v7\n"
    "v_add_u32 v9, 0x3800, v7\n"
    "s_bfe_u32 s87, s68, 0x10007\n"
    "s_cmp_lg_u32 s87, 0\n"
    "s_cbranch_scc1 L_B%=\n"
    "v_lshrrev_b32 v10, 3, v0\n"
    "s_mul_i32 s30, s69, 0xe000\n"
    "s_mov_b32 s31, 0xe00\n"
    "v_mul_lo_u32 v10, v10, s31\n"
    "v_bfe_u32 v251, v0, 4, 1\n"
    "v_lshlrev_b32 v251, 2, v251\n"
    "v_bitop3_b32 v251, v0, 7, v251 bitop3:0x6a\n"
    "v_lshl_add_u32 v10, v251, 4, v10\n"
    "v_add_u32 v10, s30, v10\n"
    "v_add_u32 v11, 0x6c00, v10\n"
    "s_waitcnt lgkmcnt(0)\n"
    "s_lshl_b32 s30, s70, 17\n"
    "s_add_u32 s36, s12, s30\n"
    "s_addc_u32 s37, s13, 0\n"
    "s_sub_u32 s38, 0x200000, s30\n"
    "s_mov_b32 s39, 0x20000\n"
    "s_mov_b32 s48, s28\n"
    "s_mov_b32 s49, s29\n"
    "s_mov_b32 s50, 0xe98900\n"
    "s_mov_b32 s51, 0x20000\n"
    "s_add_u32 s82, s71, 0\n"
    "s_lshl_b32 s85, s82, 13\n"
    "s_add_u32 s85, s85, s81\n"
    "v_lshlrev_b32 v232, 4, v0\n"
    "v_mov_b32 v233, 0\n"
    "s_mov_b32 s86, 0xa58800\n"
    "buffer_load_dword v234, v233, s[48:51], s86 offen sc1\n"
    "buffer_load_dwordx4 v[192:195], v232, s[36:39], s85 offen\n"
    "buffer_load_dwordx4 v[196:199], v232, s[36:39], s85 offen offset:1024\n"
    "s_mul_i32 s30, s71, 0x100\n"
    "s_add_u32 s30, s30, 0\n"
    "s_add_u32 s40, s20, s30\n"
    "s_addc_u32 s41, s21, 0\n"
    "s_sub_u32 s42, 0x1c0000, s30\n"
    "s_mov_b32 s43, 0x20000\n"
    "s_add_u32 s44, s22, s30\n"
    "s_addc_u32 s45, s23, 0\n"
    "s_sub_u32 s44, s44, 0x800\n"
    "s_subb_u32 s45, s45, 0\n"
    "s_sub_u32 s46, 0x1c0800, s30\n"
    "s_mov_b32 s47, 0x20000\n"
    "s_add_u32 m0, s80, 0\n"
    "s_nop 0\n"
    "buffer_load_dwordx4 v6, s[36:39], s60 offen lds\n"
    "buffer_load_dwordx4 v7, s[36:39], s60 offen offset:1024 lds\n"
    "buffer_load_dwordx4 v8, s[36:39], s60 offen offset:2048 lds\n"
    "buffer_load_dwordx4 v9, s[36:39], s60 offen offset:3072 lds\n"
    "s_mul_i32 s74, s60, 0x700\n"
    "s_add_u32 m0, s80, 0x4000\n"
    "s_nop 0\n"
    "buffer_load_dwordx4 v10, s[40:43], s74 offen lds\n"
    "buffer_load_dwordx4 v11, s[40:43], s74 offen offset:1024 lds\n"
    "buffer_load_dwordx4 v10, s[44:47], s74 offen offset:2048 lds\n"
    "buffer_load_dwordx4 v11, s[44:47], s74 offen offset:3072 lds\n"
    "s_getpc_b64 s[88:89]\n"
    "LA_pf%=:\n"
    "s_bfe_u32 s85, s68, 0x40003\n"
    "s_lshl_b32 s85, s85, 2\n"
    "s_add_u32 s85, s85, s69\n"
    "s_lshl_b32 s85, s85, 9\n"
    "v_lshl_add_u32 v235, v0, 3, s85\n"
    "s_mov_b32 s85, L_B%=-LA_pf%=-8\n"
    "v_min_u32 v235, s85, v235\n"
    "global_load_dwordx2 v[236:237], v235, s[88:89]\n"
    "s_add_u32 m0, s80, 0x8000\n"
    "s_nop 0\n"
    "buffer_load_dwordx4 v6, s[36:39], s61 offen lds\n"
    "buffer_load_dwordx4 v7, s[36:39], s61 offen offset:1024 lds\n"
    "buffer_load_dwordx4 v8, s[36:39], s61 offen offset:2048 lds\n"
    "buffer_load_dwordx4 v9, s[36:39], s61 offen offset:3072 lds\n"
    "s_mul_i32 s74, s61, 0x700\n"
    "s_add_u32 m0, s80, 0xc000\n"
    "s_nop 0\n"
    "buffer_load_dwordx4 v10, s[40:43], s74 offen lds\n"
    "buffer_load_dwordx4 v11, s[40:43], s74 offen offset:1024 lds\n"
    "buffer_load_dwordx4 v10, s[44:47], s74 offen offset:2048 lds\n"
    "buffer_load_dwordx4 v11, s[44:47], s74 offen offset:3072 lds\n"
    "s_add_u32 m0, s80, 0x10000\n"
    "s_nop 0\n"
    "buffer_load_dwordx4 v6, s[36:39], s62 offen lds\n"
    "buffer_load_dwordx4 v7, s[36:39], s62 offen offset:1024 lds\n"
    "buffer_load_dwordx4 v8, s[36:39], s62 offen offset:2048 lds\n"
    "buffer_load_dwordx4 v9, s[36:39], s62 offen offset:3072 lds\n"
    "s_mul_i32 s74, s62, 0x700\n"
    "s_add_u32 m0, s80, 0x14000\n"
    "s_nop 0\n"
    "buffer_load_dwordx4 v10, s[40:43], s74 offen lds\n"
    "buffer_load_dwordx4 v11, s[40:43], s74 offen offset:1024 lds\n"
    "buffer_load_dwordx4 v10, s[44:47], s74 offen offset:2048 lds\n"
    "buffer_load_dwordx4 v11, s[44:47], s74 offen offset:3072 lds\n"
    "s_add_u32 m0, s80, 0x18000\n"
    "s_nop 0\n"
    "buffer_load_dwordx4 v6, s[36:39], s63 offen lds\n"
    "buffer_load_dwordx4 v7, s[36:39], s63 offen offset:1024 lds\n"
    "buffer_load_dwordx4 v8, s[36:39], s63 offen offset:2048 lds\n"
    "buffer_load_dwordx4 v9, s[36:39], s63 offen offset:3072 lds\n"
    "s_mul_i32 s74, s63, 0x700\n"
    "s_add_u32 m0, s80, 0x1c000\n"
    "s_nop 0\n"
    "buffer_load_dwordx4 v10, s[40:43], s74 offen lds\n"
    "buffer_load_dwordx4 v11, s[40:43], s74 offen offset:1024 lds\n"
    "buffer_load_dwordx4 v10, s[44:47], s74 offen offset:2048 lds\n"
    "buffer_load_dwordx4 v11, s[44:47], s74 offen offset:3072 lds\n"
    "s_add_u32 m0, s80, 0x20000\n"
    "s_nop 0\n"
    "buffer_load_dwordx4 v6, s[36:39], s64 offen lds\n"
    "buffer_load_dwordx4 v7, s[36:39], s64 offen offset:1024 lds\n"
    "buffer_load_dwordx4 v8, s[36:39], s64 offen offset:2048 lds\n"
    "buffer_load_dwordx4 v9, s[36:39], s64 offen offset:3072 lds\n"
    "s_lshl_b32 s85, s71, 11\n"
    "s_lshl_b32 s86, s76, 10\n"
    "s_add_u32 s85, s85, s86\n"
    "s_add_u32 s56, s16, 0\n"
    "s_addc_u32 s57, s17, 0\n"
    "s_sub_u32 s58, 0x4000, 0\n"
    "s_mov_b32 s59, 0x20000\n"
    "s_mul_i32 s86, s69, 0xc00\n"
    "s_add_u32 m0, s86, 0x24000\n"
    "s_nop 0\n"
    "buffer_load_dwordx4 v232, s[56:59], s85 offen lds\n"
    "s_and_b32 s77, s68, 1\n"
    "s_bfe_u32 s78, s68, 0x20001\n"
    "s_lshl_b32 s78, s78, 2\n"
    "s_bfe_u32 s85, s68, 0x20003\n"
    "s_add_u32 s78, s78, s85\n"
    "s_lshr_b32 s79, s68, 5\n"
    "s_mov_b32 s73, 0x20000\n"
    "s_mov_b32 s92, 0xffff0000\n"
    "s_mov_b32 s93, 0xffff0000\n"
    "v_lshrrev_b32 v12, 3, v0\n"
    "s_mul_i32 s30, s69, 0xc000\n"
    "s_mov_b32 s31, 0xc00\n"
    "v_mul_lo_u32 v12, v12, s31\n"
    "v_bfe_u32 v251, v0, 4, 1\n"
    "v_lshlrev_b32 v251, 2, v251\n"
    "v_bitop3_b32 v251, v0, 7, v251 bitop3:0x6a\n"
    "v_lshl_add_u32 v12, v251, 4, v12\n"
    "v_add_u32 v12, s30, v12\n"
    "v_add_u32 v13, 0x5c00, v12\n"
    "v_bfe_u32 v1, v0, 3, 1\n"
    "v_xor_b32 v1, s76, v1\n"
    "v_lshlrev_b32 v1, 6, v1\n"
    "v_bfe_u32 v251, v0, 4, 1\n"
    "v_lshl_add_u32 v1, v251, 5, v1\n"
    "v_and_b32 v251, 3, v0\n"
    "v_lshl_add_u32 v1, v251, 3, v1\n"
    "v_bfe_u32 v251, v0, 2, 2\n"
    "v_lshl_add_u32 v1, v251, 7, v1\n"
    "v_lshrrev_b32 v251, 5, v0\n"
    "v_lshl_add_u32 v1, v251, 10, v1\n"
    "v_and_b32 v168, 31, v0\n"
    "v_lshrrev_b32 v169, 5, v0\n"
    "v_bfe_u32 v170, v0, 1, 3\n"
    "v_xor_b32 v170, v169, v170\n"
    "s_lshl_b32 s85, s75, 13\n"
    "v_lshl_add_u32 v171, v168, 7, s85\n"
    "v_xor_b32 v172, 0, v170\n"
    "v_lshl_add_u32 v2, v172, 4, v171\n"
    "v_xor_b32 v172, 2, v170\n"
    "v_lshl_add_u32 v3, v172, 4, v171\n"
    "v_xor_b32 v172, 4, v170\n"
    "v_lshl_add_u32 v4, v172, 4, v171\n"
    "v_xor_b32 v172, 6, v170\n"
    "v_lshl_add_u32 v5, v172, 4, v171\n"
    "v_bfe_u32 v170, v0, 4, 1\n"
    "s_mov_b32 s85, 0x12000\n"
    "v_mul_lo_u32 v170, v170, s85\n"
    "v_lshl_add_u32 v170, v169, 8, v170\n"
    "v_and_b32 v171, 15, v0\n"
    "v_lshl_add_u32 v14, v171, 4, v170\n"
    "s_lshl_b32 s85, s70, 1\n"
    "s_add_u32 s85, s85, s75\n"
    "s_mul_i32 s83, s85, 0x48000\n"
    "s_lshl_b32 s85, s85, 9\n"
    "s_lshl_b32 s86, s82, 5\n"
    "s_add_u32 s85, s85, s86\n"
    "s_lshl_b32 s86, s76, 2\n"
    "s_add_u32 s85, s85, s86\n"
    "s_add_u32 s84, s85, 0xa54000\n"
    "s_waitcnt vmcnt(40)\n"
    "v_readfirstlane_b32 s72, v234\n"
    "s_add_u32 s72, s72, 1\n"
    "s_waitcnt vmcnt(39)\n"
    "v_lshlrev_b32 v200, 16, v192\n"
    "v_and_b32 v201, 0xffff0000, v192\n"
    "v_lshlrev_b32 v202, 16, v193\n"
    "v_and_b32 v203, 0xffff0000, v193\n"
    "v_lshlrev_b32 v204, 16, v194\n"
    "v_and_b32 v205, 0xffff0000, v194\n"
    "v_lshlrev_b32 v206, 16, v195\n"
    "v_and_b32 v207, 0xffff0000, v195\n"
    "s_waitcnt vmcnt(38)\n"
    "v_lshlrev_b32 v208, 16, v196\n"
    "v_and_b32 v209, 0xffff0000, v196\n"
    "v_lshlrev_b32 v210, 16, v197\n"
    "v_and_b32 v211, 0xffff0000, v197\n"
    "v_lshlrev_b32 v212, 16, v198\n"
    "v_and_b32 v213, 0xffff0000, v198\n"
    "v_lshlrev_b32 v214, 16, v199\n"
    "v_and_b32 v215, 0xffff0000, v199\n"
    "v_add_f32 v216, v200, v201\n"
    "v_add_f32 v216, v216, v202\n"
    "v_add_f32 v216, v216, v203\n"
    "v_add_f32 v216, v216, v204\n"
    "v_add_f32 v216, v216, v205\n"
    "v_add_f32 v216, v216, v206\n"
    "v_add_f32 v216, v216, v207\n"
    "v_add_f32 v217, v208, v209\n"
    "v_add_f32 v217, v217, v210\n"
    "v_add_f32 v217, v217, v211\n"
    "v_add_f32 v217, v217, v212\n"
    "v_add_f32 v217, v217, v213\n"
    "v_add_f32 v217, v217, v214\n"
    "v_add_f32 v217, v217, v215\n"
    "s_nop 1\n"
    "v_permlane32_swap_b32 v216, v217\n"
    "v_add_f32 v216, v216, v217\n"
    "s_nop 1\n"
    "v_add_f32_dpp v216, v216, v216 row_ror:8 row_mask:0xf bank_mask:0xf\n"
    "s_nop 1\n"
    "v_add_f32_dpp v216, v216, v216 row_ror:4 row_mask:0xf bank_mask:0xf\n"
    "s_nop 1\n"
    "v_add_f32_dpp v216, v216, v216 row_ror:2 row_mask:0xf bank_mask:0xf\n"
    "s_nop 1\n"
    "v_add_f32_dpp v216, v216, v216 row_ror:1 row_mask:0xf bank_mask:0xf\n"
    "v_mov_b32 v218, v216\n"
    "s_nop 1\n"
    "v_permlane16_swap_b32 v216, v218\n"
    "v_add_f32 v216, v216, v218\n"
    "v_mul_f32 v216, 0x3b000000, v216\n"
    "v_mov_b32 v219, v216\n"
    "v_mov_b32 v217, v216\n"
    "s_nop 1\n"
    "v_permlane32_swap_b32 v216, v217\n"
    "v_sub_f32 v200, v200, v216\n"
    "v_sub_f32 v201, v201, v216\n"
    "v_sub_f32 v202, v202, v216\n"
    "v_sub_f32 v203, v203, v216\n"
    "v_sub_f32 v204, v204, v216\n"
    "v_sub_f32 v205, v205, v216\n"
    "v_sub_f32 v206, v206, v216\n"
    "v_sub_f32 v207, v207, v216\n"
    "v_sub_f32 v208, v208, v217\n"
    "v_sub_f32 v209, v209, v217\n"
    "v_sub_f32 v210, v210, v217\n"
    "v_sub_f32 v211, v211, v217\n"
    "v_sub_f32 v212, v212, v217\n"
    "v_sub_f32 v213, v213, v217\n"
    "v_sub_f32 v214, v214, v217\n"
    "v_sub_f32 v215, v215, v217\n"
    "v_mul_f32 v216, v200, v200\n"
    "v_fmac_f32 v216, v201, v201\n"
    "v_fmac_f32 v216, v202, v202\n"
    "v_fmac_f32 v216, v203, v203\n"
    "v_fmac_f32 v216, v204, v204\n"
    "v_fmac_f32 v216, v205, v205\n"
    "v_fmac_f32 v216, v206, v206\n"
    "v_fmac_f32 v216, v207, v207\n"
    "v_mul_f32 v217, v208, v208\n"
    "v_fmac_f32 v217, v209, v209\n"
    "v_fmac_f32 v217, v210, v210\n"
    "v_fmac_f32 v217, v211, v211\n"
    "v_fmac_f32 v217, v212, v212\n"
    "v_fmac_f32 v217, v213, v213\n"
    "v_fmac_f32 v217, v214, v214\n"
    "v_fmac_f32 v217, v215, v215\n"
    "v_mov_b32 v221, s72\n"
    "v_lshrrev_b32 v222, 2, v0\n"
    "v_permlane32_swap_b32 v216, v217\n"
    "v_add_f32 v216, v216, v217\n"
    "v_and_b32 v223, 31, v0\n"
    "v_cmp_eq_u32 vcc, 0, v223\n"
    "v_add_f32_dpp v216, v216, v216 row_ror:8 row_mask:0xf bank_mask:0xf\n"
    "v_mov_b32 v223, 0x7ff00000\n"
    "v_cndmask_b32 v222, v223, v222, vcc\n"
    "v_add_f32_dpp v216, v216, v216 row_ror:4 row_mask:0xf bank_mask:0xf\n"
    "s_lshl_b32 s85, s70, 10\n"
    "s_lshl_b32 s86, s82, 6\n"
    "v_add_f32_dpp v216, v216, v216 row_ror:2 row_mask:0xf bank_mask:0xf\n"
    "s_add_u32 s85, s85, s86\n"
    "s_lshl_b32 s86, s69, 4\n"
    "v_add_f32_dpp v216, v216, v216 row_ror:1 row_mask:0xf bank_mask:0xf\n"
    "v_mov_b32 v218, v216\n"
    "s_add_u32 s85, s85, s86\n"
    "s_add_u32 s85, s85, 0xa50000\n"
    "v_permlane16_swap_b32 v216, v218\n"
    "v_add_f32 v216, v216, v218\n"
    "v_mul_f32 v216, 0x3b000000, v216\n"
    "v_cvt_pk_bf16_f32 v216, v216, v216\n"
    "v_and_b32 v216, 0xffff0000, v216\n"
    "v_add_f32 v216, 0x3727c5ac, v216\n"
    "v_cvt_pk_bf16_f32 v216, v216, v216\n"
    "v_and_b32 v216, 0xffff0000, v216\n"
    "v_sqrt_f32 v216, v216\n"
    "s_nop 0\n"
    "v_cvt_pk_bf16_f32 v220, v219, v216\n"
    "buffer_store_dwordx2 v[220:221], v222, s[48:51], s85 offen sc1\n"
    "s_waitcnt vmcnt(31)\n"
    "s_barrier\n"
    "ds_read_b64_tr_b16 v[16:17], v1 offset:16384\n"
    "ds_read_b128 a[0:3], v2\n"
    "ds_read_b64_tr_b16 v[18:19], v1 offset:16896\n"
    "ds_read_b128 a[4:7], v2 offset:4096\n"
    "ds_read_b64_tr_b16 v[20:21], v1 offset:18432\n"
    "ds_read_b64_tr_b16 v[22:23], v1 offset:18944\n"
    "ds_read_b64_tr_b16 v[24:25], v1 offset:20480\n"
    "ds_read_b128 a[8:11], v3\n"
    "s_waitcnt lgkmcnt(5)\n"
    "v_mfma_f32_32x32x16_bf16 v[32:47], v[16:19], a[0:3], 0\n"
    "ds_read_b64_tr_b16 v[26:27], v1 offset:20992\n"
    "ds_read_b128 a[12:15], v3 offset:4096\n"
    "v_bfe_u32 v180, v0, 2, 2\n"
    "v_and_b32 v181, 3, v0\n"
    "v_bfe_u32 v182, v0, 4, 1\n"
    "s_waitcnt lgkmcnt(6)\n"
    "v_mfma_f32_32x32x16_bf16 v[48:63], v[16:19], a[4:7], 0\n"
    "ds_read_b64_tr_b16 v[28:29], v1 offset:22528\n"
    "v_lshrrev_b32 v183, 5, v0\n"
    "s_lshr_b32 s34, s70, 3\n"
    "s_mul_i32 s34, s34, 0x2000\n"
    "s_add_u32 s34, s34, 0x4000\n"
    "s_waitcnt lgkmcnt(5)\n"
    "v_mfma_f32_32x32x16_bf16 v[80:95], v[20:23], a[4:7], 0\n"
    "ds_read_b64_tr_b16 v[30:31], v1 offset:23040\n"
    "s_lshl_b32 s35, s75, 11\n"
    "s_add_u32 s34, s34, s35\n"
    "v_lshlrev_b32 v183, 12, v183\n"
    "v_lshl_add_u32 v183, v181, 3, v183\n"
    "v_mfma_f32_32x32x16_bf16 v[64:79], v[20:23], a[0:3], 0\n"
    "ds_read_b64_tr_b16 v[16:17], v1 offset:24576\n"
    "ds_read_b128 a[16:19], v4\n"
    "v_lshl_add_u32 v182, v182, 3, v180\n"
    "v_lshl_add_u32 v183, v182, 7, v183\n"
    "v_lshrrev_b32 v180, 1, v180\n"
    "s_waitcnt lgkmcnt(5)\n"
    "v_mfma_f32_32x32x16_bf16 v[32:47], v[24:27], a[8:11], v[32:47]\n"
    "ds_read_b64_tr_b16 v[18:19], v1 offset:25088\n"
    "ds_read_b128 a[20:23], v4 offset:4096\n"
    "v_xor_b32 v180, s76, v180\n"
    "v_lshl_add_u32 v183, v180, 6, v183\n"
    "v_add_u32 v15, s34, v183\n"
    "s_waitcnt lgkmcnt(6)\n"
    "v_mfma_f32_32x32x16_bf16 v[48:63], v[24:27], a[12:15], v[48:63]\n"
    "ds_read_b64_tr_b16 v[20:21], v1 offset:26624\n"
    "ds_read_b64_tr_b16 v[168:169], v15\n"
    "ds_read_b64_tr_b16 v[170:171], v15 offset:512\n"
    "ds_read_b64_tr_b16 v[172:173], v15 offset:32\n"
    "ds_read_b64_tr_b16 v[174:175], v15 offset:544\n"
    "s_waitcnt lgkmcnt(9)\n"
    "v_mfma_f32_32x32x16_bf16 v[80:95], v[28:31], a[12:15], v[80:95]\n"
    "ds_read_b64_tr_b16 v[22:23], v1 offset:27136\n"
    "v_mov_b32 v176, 0x3f803f80\n"
    "v_mov_b32 v177, 0x3f803f80\n"
    "v_mov_b32 v178, 0x3f803f80\n"
    "v_mov_b32 v179, 0x3f803f80\n"
    "v_mfma_f32_32x32x16_bf16 v[64:79], v[28:31], a[8:11], v[64:79]\n"
    "ds_read_b64_tr_b16 v[24:25], v1 offset:28672\n"
    "ds_read_b128 a[24:27], v5\n"
    "s_mul_i32 s34, s82, 12\n"
    "s_lshl_b32 s35, s76, 1\n"
    "s_add_u32 s34, s34, s35\n"
    "s_waitcnt lgkmcnt(9)\n"
    "v_mfma_f32_32x32x16_bf16 v[32:47], v[16:19], a[16:19], v[32:47]\n"
    "ds_read_b64_tr_b16 v[26:27], v1 offset:29184\n"
    "ds_read_b128 a[28:31], v5 offset:4096\n"
    "s_add_u32 s34, s34, s75\n"
    "s_add_u32 s34, s34, 0\n"
    "s_lshl_b32 s34, s34, 12\n"
    "s_waitcnt lgkmcnt(10)\n"
    "v_mfma_f32_32x32x16_bf16 v[48:63], v[16:19], a[20:23], v[48:63]\n"
    "ds_read_b64_tr_b16 v[28:29], v1 offset:30720\n"
    "s_add_u32 s34, s34, 0x990000\n"
    "s_and_b32 s35, s70, 7\n"
    "s_lshl_b32 s35, s35, 9\n"
    "s_waitcnt lgkmcnt(8)\n"
    "v_mfma_f32_16x16x32_bf16 v[180:183], v[176:179], v[168:171], 0\n"
    "s_waitcnt lgkmcnt(6)\n"
    "v_mfma_f32_16x16x32_bf16 v[184:187], v[176:179], v[172:175], 0\n"
    "s_add_u32 s34, s34, s35\n"
    "s_waitcnt lgkmcnt(5)\n"
    "v_mfma_f32_32x32x16_bf16 v[80:95], v[20:23], a[20:23], v[80:95]\n"
    "ds_read_b64_tr_b16 v[30:31], v1 offset:31232\n"
    "s_lshr_b32 s35, s70, 3\n"
    "s_lshl_b32 s35, s35, 8\n"
    "s_add_u32 s34, s34, s35\n"
    "v_and_b32 v15, 31, v0\n"
    "v_mfma_f32_32x32x16_bf16 v[64:79], v[20:23], a[16:19], v[64:79]\n"
    "s_waitcnt vmcnt(22)\n"
    "s_barrier\n"
    "ds_read_b64_tr_b16 v[16:17], v1 offset:49152\n"
    "ds_read_b128 a[32:35], v2 offset:32768\n"
    "v_lshlrev_b32 v15, 3, v15\n"
    "v_cndmask_b32 v180, v180, v184, s[92:93]\n"
    "s_waitcnt lgkmcnt(5)\n"
    "v_mfma_f32_32x32x16_bf16 v[32:47], v[24:27], a[24:27], v[32:47]\n"
    "ds_read_b64_tr_b16 v[18:19], v1 offset:49664\n"
    "ds_read_b128 a[36:39], v2 offset:36864\n"
    "v_mov_b32 v181, s72\n"
    "buffer_store_dwordx2 v[180:181], v15, s[48:51], s34 offen sc1\n"
    "s_waitcnt lgkmcnt(6)\n"
    "v_mfma_f32_32x32x16_bf16 v[48:63], v[24:27], a[28:31], v[48:63]\n"
    "ds_read_b64_tr_b16 v[20:21], v1 offset:51200\n"
    "s_waitcnt lgkmcnt(5)\n"
    "v_mfma_f32_32x32x16_bf16 v[80:95], v[28:31], a[28:31], v[80:95]\n"
    "ds_read_b64_tr_b16 v[22:23], v1 offset:51712\n"
    "v_mfma_f32_32x32x16_bf16 v[64:79], v[28:31], a[24:27], v[64:79]\n"
    "ds_read_b64_tr_b16 v[24:25], v1 offset:53248\n"
    "ds_read_b128 a[40:43], v3 offset:32768\n"
    "s_waitcnt lgkmcnt(5)\n"
    "v_mfma_f32_32x32x16_bf16 v[32:47], v[16:19], a[32:35], v[32:47]\n"
    "ds_read_b64_tr_b16 v[26:27], v1 offset:53760\n"
    "ds_read_b128 a[44:47], v3 offset:36864\n"
    "s_waitcnt lgkmcnt(6)\n"
    "v_mfma_f32_32x32x16_bf16 v[48:63], v[16:19], a[36:39], v[48:63]\n"
    "ds_read_b64_tr_b16 v[28:29], v1 offset:55296\n"
    "s_waitcnt lgkmcnt(5)\n"
    "v_mfma_f32_32x32x16_bf16 v[80:95], v[20:23], a[36:39], v[80:95]\n"
    "ds_read_b64_tr_b16 v[30:31], v1 offset:55808\n"
    "v_mfma_f32_32x32x16_bf16 v[64:79], v[20:23], a[32:35], v[64:79]\n"
    "ds_read_b64_tr_b16 v[16:17], v1 offset:57344\n"
    "ds_read_b128 a[48:51], v4 offset:32768\n"
    "s_waitcnt lgkmcnt(5)\n"
    "v_mfma_f32_32x32x16_bf16 v[32:47], v[24:27], a[40:43], v[32:47]\n"
    "ds_read_b64_tr_b16 v[18:19], v1 offset:57856\n"
    "ds_read_b128 a[52:55], v4 offset:36864\n"
    "s_waitcnt lgkmcnt(6)\n"
    "v_mfma_f32_32x32x16_bf16 v[48:63], v[24:27], a[44:47], v[48:63]\n"
    "ds_read_b64_tr_b16 v[20:21], v1 offset:59392\n"
    "s_waitcnt lgkmcnt(5)\n"
    "v_mfma_f32_32x32x16_bf16 v[80:95], v[28:31], a[44:47], v[80:95]\n"
    "ds_read_b64_tr_b16 v[22:23], v1 offset:59904\n"
    "v_mfma_f32_32x32x16_bf16 v[64:79], v[28:31], a[40:43], v[64:79]\n"
    "ds_read_b64_tr_b16 v[24:25], v1 offset:61440\n"
    "ds_read_b128 a[56:59], v5 offset:32768\n"
    "s_waitcnt lgkmcnt(5)\n"
    "v_mfma_f32_32x32x16_bf16 v[32:47], v[16:19], a[48:51], v[32:47]\n"
    "ds_read_b64_tr_b16 v[26:27], v1 offset:61952\n"
    "ds_read_b128 a[60:63], v5 offset:36864\n"
    "s_waitcnt lgkmcnt(6)\n"
    "v_mfma_f32_32x32x16_bf16 v[48:63], v[16:19], a[52:55], v[48:63]\n"
    "ds_read_b64_tr_b16 v[28:29], v1 offset:63488\n"
    "s_waitcnt lgkmcnt(5)\n"
    "v_mfma_f32_32x32x16_bf16 v[80:95], v[20:23], a[52:55], v[80:95]\n"
    "ds_read_b64_tr_b16 v[30:31], v1 offset:64000\n"
    "v_mfma_f32_32x32x16_bf16 v[64:79], v[20:23], a[48:51], v[64:79]\n"
    "s_waitcnt vmcnt(15)\n"
    "s_barrier\n"
    "v_add_u32 v1, 0x14000, v1\n"
    "ds_read_b64_tr_b16 v[16:17], v1\n"
    "v_add_u32 v2, 0x10000, v2\n"
    "v_add_u32 v3, 0x10000, v3\n"
    "v_add_u32 v4, 0x10000, v4\n"
    "v_add_u32 v5, 0x10000, v5\n"
    "ds_read_b128 a[64:67], v2\n"
    "s_mul_i32 s74, s64, 0x700\n"
    "s_add_u32 m0, s80, 0\n"
    "s_nop 0\n"
    "buffer_load_dwordx4 v10, s[40:43], s74 offen lds\n"
    "s_waitcnt lgkmcnt(5)\n"
    "v_mfma_f32_32x32x16_bf16 v[32:47], v[24:27], a[56:59], v[32:47]\n"
    "ds_read_b64_tr_b16 v[18:19], v1 offset:512\n"
    "ds_read_b128 a[68:71], v2 offset:4096\n"
    "buffer_load_dwordx4 v11, s[40:43], s74 offen offset:1024 lds\n"
    "s_waitcnt lgkmcnt(6)\n"
    "v_mfma_f32_32x32x16_bf16 v[48:63], v[24:27], a[60:63], v[48:63]\n"
    "ds_read_b64_tr_b16 v[20:21], v1 offset:2048\n"
    "buffer_load_dwordx4 v10, s[44:47], s74 offen offset:2048 lds\n"
    "s_waitcnt lgkmcnt(5)\n"
    "v_mfma_f32_32x32x16_bf16 v[80:95], v[28:31], a[60:63], v[80:95]\n"
    "ds_read_b64_tr_b16 v[22:23], v1 offset:2560\n"
    "buffer_load_dwordx4 v11, s[44:47], s74 offen offset:3072 lds\n"
    "v_mfma_f32_32x32x16_bf16 v[64:79], v[28:31], a[56:59], v[64:79]\n"
    "ds_read_b64_tr_b16 v[24:25], v1 offset:4096\n"
    "ds_read_b128 a[72:75], v3\n"
    "s_add_u32 m0, s80, 0x4000\n"
    "s_nop 0\n"
    "buffer_load_dwordx4 v6, s[36:39], s65 offen lds\n"
    "s_waitcnt lgkmcnt(5)\n"
    "v_mfma_f32_32x32x16_bf16 v[32:47], v[16:19], a[64:67], v[32:47]\n"
    "ds_read_b64_tr_b16 v[26:27], v1 offset:4608\n"
    "ds_read_b128 a[76:79], v3 offset:4096\n"
    "buffer_load_dwordx4 v7, s[36:39], s65 offen offset:1024 lds\n"
    "s_waitcnt lgkmcnt(6)\n"
    "v_mfma_f32_32x32x16_bf16 v[48:63], v[16:19], a[68:71], v[48:63]\n"
    "ds_read_b64_tr_b16 v[28:29], v1 offset:6144\n"
    "buffer_load_dwordx4 v8, s[36:39], s65 offen offset:2048 lds\n"
    "s_waitcnt lgkmcnt(5)\n"
    "v_mfma_f32_32x32x16_bf16 v[80:95], v[20:23], a[68:71], v[80:95]\n"
    "ds_read_b64_tr_b16 v[30:31], v1 offset:6656\n"
    "buffer_load_dwordx4 v9, s[36:39], s65 offen offset:3072 lds\n"
    "v_mfma_f32_32x32x16_bf16 v[64:79], v[20:23], a[64:67], v[64:79]\n"
    "ds_read_b64_tr_b16 v[16:17], v1 offset:8192\n"
    "ds_read_b128 a[80:83], v4\n"
    "s_waitcnt lgkmcnt(5)\n"
    "v_mfma_f32_32x32x16_bf16 v[32:47], v[24:27], a[72:75], v[32:47]\n"
    "ds_read_b64_tr_b16 v[18:19], v1 offset:8704\n"
    "ds_read_b128 a[84:87], v4 offset:4096\n"
    "s_waitcnt lgkmcnt(6)\n"
    "v_mfma_f32_32x32x16_bf16 v[48:63], v[24:27], a[76:79], v[48:63]\n"
    "ds_read_b64_tr_b16 v[20:21], v1 offset:10240\n"
    "s_waitcnt lgkmcnt(5)\n"
    "v_mfma_f32_32x32x16_bf16 v[80:95], v[28:31], a[76:79], v[80:95]\n"
    "ds_read_b64_tr_b16 v[22:23], v1 offset:10752\n"
    "v_mfma_f32_32x32x16_bf16 v[64:79], v[28:31], a[72:75], v[64:79]\n"
    "ds_read_b64_tr_b16 v[24:25], v1 offset:12288\n"
    "ds_read_b128 a[88:91], v5\n"
    "s_waitcnt lgkmcnt(5)\n"
    "v_mfma_f32_32x32x16_bf16 v[32:47], v[16:19], a[80:83], v[32:47]\n"
    "ds_read_b64_tr_b16 v[26:27], v1 offset:12800\n"
    "ds_read_b128 a[92:95], v5 offset:4096\n"
    "s_waitcnt lgkmcnt(6)\n"
    "v_mfma_f32_32x32x16_bf16 v[48:63], v[16:19], a[84:87], v[48:63]\n"
    "ds_read_b64_tr_b16 v[28:29], v1 offset:14336\n"
    "s_waitcnt lgkmcnt(5)\n"
    "v_mfma_f32_32x32x16_bf16 v[80:95], v[20:23], a[84:87], v[80:95]\n"
    "ds_read_b64_tr_b16 v[30:31], v1 offset:14848\n"
    "v_mfma_f32_32x32x16_bf16 v[64:79], v[20:23], a[80:83], v[64:79]\n"
    "s_waitcnt vmcnt(15)\n"
    "s_barrier\n"
    "ds_read_b64_tr_b16 v[16:17], v1 offset:32768\n"
    "ds_read_b128 a[96:99], v2 offset:32768\n"
    "s_mul_i32 s74, s65, 0x700\n"
    "s_add_u32 m0, s80, 0x8000\n"
    "s_nop 0\n"
    "buffer_load_dwordx4 v10, s[40:43], s74 offen lds\n"
    "s_waitcnt lgkmcnt(5)\n"
    "v_mfma_f32_32x32x16_bf16 v[32:47], v[24:27], a[88:91], v[32:47]\n"
    "ds_read_b64_tr_b16 v[18:19], v1 offset:33280\n"
    "ds_read_b128 a[100:103], v2 offset:36864\n"
    "buffer_load_dwordx4 v11, s[40:43], s74 offen offset:1024 lds\n"
    "s_waitcnt lgkmcnt(6)\n"
    "v_mfma_f32_32x32x16_bf16 v[48:63], v[24:27], a[92:95], v[48:63]\n"
    "ds_read_b64_tr_b16 v[20:21], v1 offset:34816\n"
    "buffer_load_dwordx4 v10, s[44:47], s74 offen offset:2048 lds\n"
    "s_waitcnt lgkmcnt(5)\n"
    "v_mfma_f32_32x32x16_bf16 v[80:95], v[28:31], a[92:95], v[80:95]\n"
    "ds_read_b64_tr_b16 v[22:23], v1 offset:35328\n"
    "buffer_load_dwordx4 v11, s[44:47], s74 offen offset:3072 lds\n"
    "v_mfma_f32_32x32x16_bf16 v[64:79], v[28:31], a[88:91], v[64:79]\n"
    "ds_read_b64_tr_b16 v[24:25], v1 offset:36864\n"
    "ds_read_b128 a[104:107], v3 offset:32768\n"
    "s_add_u32 m0, s80, 0xc000\n"
    "s_nop 0\n"
    "buffer_load_dwordx4 v6, s[36:39], s66 offen lds\n"
    "s_waitcnt lgkmcnt(5)\n"
    "v_mfma_f32_32x32x16_bf16 v[32:47], v[16:19], a[96:99], v[32:47]\n"
    "ds_read_b64_tr_b16 v[26:27], v1 offset:37376\n"
    "ds_read_b128 a[108:111], v3 offset:36864\n"
    "buffer_load_dwordx4 v7, s[36:39], s66 offen offset:1024 lds\n"
    "s_waitcnt lgkmcnt(6)\n"
    "v_mfma_f32_32x32x16_bf16 v[48:63], v[16:19], a[100:103], v[48:63]\n"
    "ds_read_b64_tr_b16 v[28:29], v1 offset:38912\n"
    "buffer_load_dwordx4 v8, s[36:39], s66 offen offset:2048 lds\n"
    "s_waitcnt lgkmcnt(5)\n"
    "v_mfma_f32_32x32x16_bf16 v[80:95], v[20:23], a[100:103], v[80:95]\n"
    "ds_read_b64_tr_b16 v[30:31], v1 offset:39424\n"
    "buffer_load_dwordx4 v9, s[36:39], s66 offen offset:3072 lds\n"
    "v_mfma_f32_32x32x16_bf16 v[64:79], v[20:23], a[96:99], v[64:79]\n"
    "ds_read_b64_tr_b16 v[16:17], v1 offset:40960\n"
    "ds_read_b128 a[112:115], v4 offset:32768\n"
    "s_waitcnt lgkmcnt(5)\n"
    "v_mfma_f32_32x32x16_bf16 v[32:47], v[24:27], a[104:107], v[32:47]\n"
    "ds_read_b64_tr_b16 v[18:19], v1 offset:41472\n"
    "ds_read_b128 a[116:119], v4 offset:36864\n"
    "s_waitcnt lgkmcnt(6)\n"
    "v_mfma_f32_32x32x16_bf16 v[48:63], v[24:27], a[108:111], v[48:63]\n"
    "ds_read_b64_tr_b16 v[20:21], v1 offset:43008\n"
    "s_waitcnt lgkmcnt(5)\n"
    "v_mfma_f32_32x32x16_bf16 v[80:95], v[28:31], a[108:111], v[80:95]\n"
    "ds_read_b64_tr_b16 v[22:23], v1 offset:43520\n"
    "v_mfma_f32_32x32x16_bf16 v[64:79], v[28:31], a[104:107], v[64:79]\n"
    "ds_read_b64_tr_b16 v[24:25], v1 offset:45056\n"
    "ds_read_b128 a[120:123], v5 offset:32768\n"
    "s_waitcnt lgkmcnt(5)\n"
    "v_mfma_f32_32x32x16_bf16 v[32:47], v[16:19], a[112:115], v[32:47]\n"
    "ds_read_b64_tr_b16 v[26:27], v1 offset:45568\n"
    "ds_read_b128 a[124:127], v5 offset:36864\n"
    "s_waitcnt lgkmcnt(6)\n"
    "v_mfma_f32_32x32x16_bf16 v[48:63], v[16:19], a[116:119], v[48:63]\n"
    "ds_read_b64_tr_b16 v[28:29], v1 offset:47104\n"
    "s_waitcnt lgkmcnt(5)\n"
    "v_mfma_f32_32x32x16_bf16 v[80:95], v[20:23], a[116:119], v[80:95]\n"
    "ds_read_b64_tr_b16 v[30:31], v1 offset:47616\n"
    "v_mfma_f32_32x32x16_bf16 v[64:79], v[20:23], a[112:115], v[64:79]\n"
    "s_waitcnt vmcnt(12)\n"
    "s_barrier\n"
    "v_add_u32 v1, 0xfffec000, v1\n"
    "ds_read_b64_tr_b16 v[16:17], v1\n"
    "v_add_u32 v2, 0x8000, v2\n"
    "v_add_u32 v3, 0x8000, v3\n"
    "v_add_u32 v4, 0x8000, v4\n"
    "v_add_u32 v5, 0x8000, v5\n"
    "ds_read_b128 a[128:131], v2 offset:32768\n"
    "s_mul_i32 s74, s66, 0x700\n"
    "s_add_u32 m0, s80, 0x10000\n"
    "s_nop 0\n"
    "buffer_load_dwordx4 v10, s[40:43], s74 offen lds\n"
    "s_waitcnt lgkmcnt(5)\n"
    "v_mfma_f32_32x32x16_bf16 v[32:47], v[24:27], a[120:123], v[32:47]\n"
    "ds_read_b64_tr_b16 v[18:19], v1 offset:512\n"
    "ds_read_b128 a[132:135], v2 offset:36864\n"
    "buffer_load_dwordx4 v11, s[40:43], s74 offen offset:1024 lds\n"
    "s_waitcnt lgkmcnt(6)\n"
    "v_mfma_f32_32x32x16_bf16 v[48:63], v[24:27], a[124:127], v[48:63]\n"
    "ds_read_b64_tr_b16 v[20:21], v1 offset:2048\n"
    "buffer_load_dwordx4 v10, s[44:47], s74 offen offset:2048 lds\n"
    "s_waitcnt lgkmcnt(5)\n"
    "v_mfma_f32_32x32x16_bf16 v[80:95], v[28:31], a[124:127], v[80:95]\n"
    "ds_read_b64_tr_b16 v[22:23], v1 offset:2560\n"
    "buffer_load_dwordx4 v11, s[44:47], s74 offen offset:3072 lds\n"
    "v_mfma_f32_32x32x16_bf16 v[64:79], v[28:31], a[120:123], v[64:79]\n"
    "ds_read_b64_tr_b16 v[24:25], v1 offset:4096\n"
    "ds_read_b128 a[136:139], v3 offset:32768\n"
    "s_add_u32 m0, s80, 0x14000\n"
    "s_nop 0\n"
    "buffer_load_dwordx4 v6, s[36:39], s67 offen lds\n"
    "s_waitcnt lgkmcnt(5)\n"
    "v_mfma_f32_32x32x16_bf16 v[32:47], v[16:19], a[128:131], v[32:47]\n"
    "ds_read_b64_tr_b16 v[26:27], v1 offset:4608\n"
    "ds_read_b128 a[140:143], v3 offset:36864\n"
    "buffer_load_dwordx4 v7, s[36:39], s67 offen offset:1024 lds\n"
    "s_waitcnt lgkmcnt(6)\n"
    "v_mfma_f32_32x32x16_bf16 v[48:63], v[16:19], a[132:135], v[48:63]\n"
    "ds_read_b64_tr_b16 v[28:29], v1 offset:6144\n"
    "buffer_load_dwordx4 v8, s[36:39], s67 offen offset:2048 lds\n"
    "s_waitcnt lgkmcnt(5)\n"
    "v_mfma_f32_32x32x16_bf16 v[80:95], v[20:23], a[132:135], v[80:95]\n"
    "ds_read_b64_tr_b16 v[30:31], v1 offset:6656\n"
    "buffer_load_dwordx4 v9, s[36:39], s67 offen offset:3072 lds\n"
    "v_mfma_f32_32x32x16_bf16 v[64:79], v[20:23], a[128:131], v[64:79]\n"
    "ds_read_b64_tr_b16 v[16:17], v1 offset:8192\n"
    "ds_read_b128 a[144:147], v4 offset:32768\n"
    "s_waitcnt lgkmcnt(5)\n"
    "v_mfma_f32_32x32x16_bf16 v[32:47], v[24:27], a[136:139], v[32:47]\n"
    "ds_read_b64_tr_b16 v[18:19], v1 offset:8704\n"
    "ds_read_b128 a[148:151], v4 offset:36864\n"
    "s_waitcnt lgkmcnt(6)\n"
    "v_mfma_f32_32x32x16_bf16 v[48:63], v[24:27], a[140:143], v[48:63]\n"
    "ds_read_b64_tr_b16 v[20:21], v1 offset:10240\n"
    "v_and_b32 v196, 31, v0\n"
    "v_lshlrev_b32 v196, 3, v196\n"
    "s_lshl_b32 s34, s70, 10\n"
    "s_lshl_b32 s35, s75, 9\n"
    "s_waitcnt lgkmcnt(5)\n"
    "v_mfma_f32_32x32x16_bf16 v[80:95], v[28:31], a[140:143], v[80:95]\n"
    "ds_read_b64_tr_b16 v[22:23], v1 offset:10752\n"
    "s_add_u32 s34, s34, s35\n"
    "s_add_u32 s34, s34, 0xa50000\n"
    "buffer_load_dwordx2 v[192:193], v196, s[48:51], s34 offen sc1\n"
    "buffer_load_dwordx2 v[194:195], v196, s[48:51], s34 offen offset:256 sc1\n"
    "v_mfma_f32_32x32x16_bf16 v[64:79], v[28:31], a[136:139], v[64:79]\n"
    "ds_read_b64_tr_b16 v[24:25], v1 offset:12288\n"
    "ds_read_b128 a[152:155], v5 offset:32768\n"
    "s_waitcnt lgkmcnt(5)\n"
    "v_mfma_f32_32x32x16_bf16 v[32:47], v[16:19], a[144:147], v[32:47]\n"
    "ds_read_b64_tr_b16 v[26:27], v1 offset:12800\n"
    "ds_read_b128 a[156:159], v5 offset:36864\n"
    "s_waitcnt lgkmcnt(6)\n"
    "v_mfma_f32_32x32x16_bf16 v[48:63], v[16:19], a[148:151], v[48:63]\n"
    "ds_read_b64_tr_b16 v[28:29], v1 offset:14336\n"
    "s_waitcnt lgkmcnt(5)\n"
    "v_mfma_f32_32x32x16_bf16 v[80:95], v[20:23], a[148:151], v[80:95]\n"
    "ds_read_b64_tr_b16 v[30:31], v1 offset:14848\n"
    "v_mfma_f32_32x32x16_bf16 v[64:79], v[20:23], a[144:147], v[64:79]\n"
    "s_waitcnt vmcnt(14)\n"
    "s_barrier\n"
    "ds_read_b64_tr_b16 v[16:17], v1 offset:32768\n"
    "v_add_u32 v2, 0xfffec000, v2\n"
    "v_add_u32 v3, 0xfffec000, v3\n"
    "v_add_u32 v4, 0xfffec000, v4\n"
    "v_add_u32 v5, 0xfffec000, v5\n"
    "ds_read_b128 a[160:163], v2\n"
    "s_mul_i32 s74, s67, 0x700\n"
    "s_add_u32 m0, s80, 0x18000\n"
    "s_nop 0\n"
    "buffer_load_dwordx4 v10, s[40:43], s74 offen lds\n"
    "s_waitcnt lgkmcnt(5)\n"
    "v_mfma_f32_32x32x16_bf16 v[32:47], v[24:27], a[152:155], v[32:47]\n"
    "ds_read_b64_tr_b16 v[18:19], v1 offset:33280\n"
    "ds_read_b128 a[164:167], v2 offset:4096\n"
    "buffer_load_dwordx4 v11, s[40:43], s74 offen offset:1024 lds\n"
    "s_waitcnt lgkmcnt(6)\n"
    "v_mfma_f32_32x32x16_bf16 v[48:63], v[24:27], a[156:159], v[48:63]\n"
    "ds_read_b64_tr_b16 v[20:21], v1 offset:34816\n"
    "buffer_load_dwordx4 v10, s[44:47], s74 offen offset:2048 lds\n"
    "s_waitcnt lgkmcnt(5)\n"
    "v_mfma_f32_32x32x16_bf16 v[80:95], v[28:31], a[156:159], v[80:95]\n"
    "ds_read_b64_tr_b16 v[22:23], v1 offset:35328\n"
    "buffer_load_dwordx4 v11, s[44:47], s74 offen offset:3072 lds\n"
    "v_mfma_f32_32x32x16_bf16 v[64:79], v[28:31], a[152:155], v[64:79]\n"
    "ds_read_b64_tr_b16 v[24:25], v1 offset:36864\n"
    "ds_read_b128 a[168:171], v3\n"
    "s_lshl_b32 s30, s71, 7\n"
    "s_add_u32 s40, s14, s30\n"
    "s_addc_u32 s41, s15, 0\n"
    "s_sub_u32 s42, 0x180000, s30\n"
    "s_mov_b32 s43, 0x20000\n"
    "s_mul_i32 s74, s60, 0x600\n"
    "s_add_u32 m0, s81, 0x1c000\n"
    "s_nop 0\n"
    "buffer_load_dwordx4 v12, s[40:43], s74 offen lds\n"
    "s_waitcnt lgkmcnt(5)\n"
    "v_mfma_f32_32x32x16_bf16 v[32:47], v[16:19], a[160:163], v[32:47]\n"
    "ds_read_b64_tr_b16 v[26:27], v1 offset:37376\n"
    "ds_read_b128 a[172:175], v3 offset:4096\n"
    "buffer_load_dwordx4 v13, s[40:43], s74 offen offset:1024 lds\n"
    "s_waitcnt lgkmcnt(6)\n"
    "v_mfma_f32_32x32x16_bf16 v[48:63], v[16:19], a[164:167], v[48:63]\n"
    "ds_read_b64_tr_b16 v[28:29], v1 offset:38912\n"
    "s_mul_i32 s74, s61, 0x600\n"
    "s_add_u32 m0, s81, 0x1e000\n"
    "s_nop 0\n"
    "buffer_load_dwordx4 v12, s[40:43], s74 offen lds\n"
    "s_waitcnt lgkmcnt(5)\n"
    "v_mfma_f32_32x32x16_bf16 v[80:95], v[20:23], a[164:167], v[80:95]\n"
    "ds_read_b64_tr_b16 v[30:31], v1 offset:39424\n"
    "buffer_load_dwordx4 v13, s[40:43], s74 offen offset:1024 lds\n"
    "v_mfma_f32_32x32x16_bf16 v[64:79], v[20:23], a[160:163], v[64:79]\n"
    "ds_read_b64_tr_b16 v[16:17], v1 offset:40960\n"
    "ds_read_b128 a[176:179], v4\n"
    "s_waitcnt lgkmcnt(5)\n"
    "v_mfma_f32_32x32x16_bf16 v[32:47], v[24:27], a[168:171], v[32:47]\n"
    "ds_read_b64_tr_b16 v[18:19], v1 offset:41472\n"
    "ds_read_b128 a[180:183], v4 offset:4096\n"
    "s_waitcnt lgkmcnt(6)\n"
    "v_mfma_f32_32x32x16_bf16 v[48:63], v[24:27], a[172:175], v[48:63]\n"
    "ds_read_b64_tr_b16 v[20:21], v1 offset:43008\n"
    "s_waitcnt lgkmcnt(5)\n"
    "v_mfma_f32_32x32x16_bf16 v[80:95], v[28:31], a[172:175], v[80:95]\n"
    "ds_read_b64_tr_b16 v[22:23], v1 offset:43520\n"
    "v_mfma_f32_32x32x16_bf16 v[64:79], v[28:31], a[168:171], v[64:79]\n"
    "ds_read_b64_tr_b16 v[24:25], v1 offset:45056\n"
    "ds_read_b128 a[184:187], v5\n"
    "s_waitcnt lgkmcnt(5)\n"
    "v_mfma_f32_32x32x16_bf16 v[32:47], v[16:19], a[176:179], v[32:47]\n"
    "ds_read_b64_tr_b16 v[26:27], v1 offset:45568\n"
    "ds_read_b128 a[188:191], v5 offset:4096\n"
    "s_waitcnt lgkmcnt(6)\n"
    "v_mfma_f32_32x32x16_bf16 v[48:63], v[16:19], a[180:183], v[48:63]\n"
    "ds_read_b64_tr_b16 v[28:29], v1 offset:47104\n"
    "s_waitcnt lgkmcnt(5)\n"
    "v_mfma_f32_32x32x16_bf16 v[80:95], v[20:23], a[180:183], v[80:95]\n"
    "ds_read_b64_tr_b16 v[30:31], v1 offset:47616\n"
    "v_mfma_f32_32x32x16_bf16 v[64:79], v[20:23], a[176:179], v[64:79]\n"
    "s_waitcnt vmcnt(14)\n"
    "s_barrier\n"
    "v_add_u32 v1, 0x10000, v1\n"
    "ds_read_b64_tr_b16 v[16:17], v1\n"
    "ds_read_b128 a[192:195], v2 offset:32768\n"
    "s_mul_i32 s74, s62, 0x600\n"
    "s_add_u32 m0, s81, 0x20000\n"
    "s_nop 0\n"
    "buffer_load_dwordx4 v12, s[40:43], s74 offen lds\n"
    "s_waitcnt lgkmcnt(5)\n"
    "v_mfma_f32_32x32x16_bf16 v[32:47], v[24:27], a[184:187], v[32:47]\n"
    "ds_read_b64_tr_b16 v[18:19], v1 offset:512\n"
    "ds_read_b128 a[196:199], v2 offset:36864\n"
    "buffer_load_dwordx4 v13, s[40:43], s74 offen offset:1024 lds\n"
    "s_waitcnt lgkmcnt(6)\n"
    "v_mfma_f32_32x32x16_bf16 v[48:63], v[24:27], a[188:191], v[48:63]\n"
    "ds_read_b64_tr_b16 v[20:21], v1 offset:2048\n"
    "s_mul_i32 s74, s63, 0x600\n"
    "s_add_u32 m0, s81, 0x22000\n"
    "s_nop 0\n"
    "buffer_load_dwordx4 v12, s[40:43], s74 offen lds\n"
    "s_waitcnt lgkmcnt(5)\n"
    "v_mfma_f32_32x32x16_bf16 v[80:95], v[28:31], a[188:191], v[80:95]\n"
    "ds_read_b64_tr_b16 v[22:23], v1 offset:2560\n"
    "buffer_load_dwordx4 v13, s[40:43], s74 offen offset:1024 lds\n"
    "v_mfma_f32_32x32x16_bf16 v[64:79], v[28:31], a[184:187], v[64:79]\n"
    "ds_read_b64_tr_b16 v[24:25], v1 offset:4096\n"
    "ds_read_b128 a[200:203], v3 offset:32768\n"
    "s_mul_i32 s74, s64, 0x600\n"
    "s_add_u32 m0, s81, 0\n"
    "s_nop 0\n"
    "buffer_load_dwordx4 v12, s[40:43], s74 offen lds\n"
    "s_waitcnt lgkmcnt(5)\n"
    "v_mfma_f32_32x32x16_bf16 v[32:47], v[16:19], a[192:195], v[32:47]\n"
    "ds_read_b64_tr_b16 v[26:27], v1 offset:4608\n"
    "ds_read_b128 a[204:207], v3 offset:36864\n"
    "buffer_load_dwordx4 v13, s[40:43], s74 offen offset:1024 lds\n"
    "s_waitcnt lgkmcnt(6)\n"
    "v_mfma_f32_32x32x16_bf16 v[48:63], v[16:19], a[196:199], v[48:63]\n"
    "ds_read_b64_tr_b16 v[28:29], v1 offset:6144\n"
    "s_mul_i32 s74, s65, 0x600\n"
    "s_add_u32 m0, s81, 0x2000\n"
    "s_nop 0\n"
    "buffer_load_dwordx4 v12, s[40:43], s74 offen lds\n"
    "s_waitcnt lgkmcnt(5)\n"
    "v_mfma_f32_32x32x16_bf16 v[80:95], v[20:23], a[196:199], v[80:95]\n"
    "ds_read_b64_tr_b16 v[30:31], v1 offset:6656\n"
    "buffer_load_dwordx4 v13, s[40:43], s74 offen offset:1024 lds\n"
    "v_mfma_f32_32x32x16_bf16 v[64:79], v[20:23], a[192:195], v[64:79]\n"
    "ds_read_b64_tr_b16 v[16:17], v1 offset:8192\n"
    "ds_read_b128 a[208:211], v4 offset:32768\n"
    "s_waitcnt lgkmcnt(5)\n"
    "v_mfma_f32_32x32x16_bf16 v[32:47], v[24:27], a[200:203], v[32:47]\n"
    "ds_read_b64_tr_b16 v[18:19], v1 offset:8704\n"
    "ds_read_b128 a[212:215], v4 offset:36864\n"
    "s_waitcnt lgkmcnt(6)\n"
    "v_mfma_f32_32x32x16_bf16 v[48:63], v[24:27], a[204:207], v[48:63]\n"
    "ds_read_b64_tr_b16 v[20:21], v1 offset:10240\n"
    "s_waitcnt lgkmcnt(5)\n"
    "v_mfma_f32_32x32x16_bf16 v[80:95], v[28:31], a[204:207], v[80:95]\n"
    "ds_read_b64_tr_b16 v[22:23], v1 offset:10752\n"
    "v_mfma_f32_32x32x16_bf16 v[64:79], v[28:31], a[200:203], v[64:79]\n"
    "ds_read_b64_tr_b16 v[24:25], v1 offset:12288\n"
    "ds_read_b128 a[216:219], v5 offset:32768\n"
    "s_waitcnt lgkmcnt(5)\n"
    "v_mfma_f32_32x32x16_bf16 v[32:47], v[16:19], a[208:211], v[32:47]\n"
    "ds_read_b64_tr_b16 v[26:27], v1 offset:12800\n"
    "ds_read_b128 a[220:223], v5 offset:36864\n"
    "s_waitcnt lgkmcnt(6)\n"
    "v_mfma_f32_32x32x16_bf16 v[48:63], v[16:19], a[212:215], v[48:63]\n"
    "ds_read_b64_tr_b16 v[28:29], v1 offset:14336\n"
    "s_waitcnt lgkmcnt(5)\n"
    "v_mfma_f32_32x32x16_bf16 v[80:95], v[20:23], a[212:215], v[80:95]\n"
    "ds_read_b64_tr_b16 v[30:31], v1 offset:14848\n"
    "v_mfma_f32_32x32x16_bf16 v[64:79], v[20:23], a[208:211], v[64:79]\n"
    "s_waitcnt vmcnt(12)\n"
    "s_barrier\n"
    "ds_read_b64_tr_b16 v[16:17], v1 offset:32768\n"
    "v_add_u32 v2, 0x10000, v2\n"
    "v_add_u32 v3, 0x10000, v3\n"
    "v_add_u32 v4, 0x10000, v4\n"
    "v_add_u32 v5, 0x10000, v5\n"
    "ds_read_b128 a[224:227], v2\n"
    "s_mul_i32 s74, s66, 0x600\n"
    "s_add_u32 m0, s81, 0x4000\n"
    "s_nop 0\n"
    "buffer_load_dwordx4 v12, s[40:43], s74 offen lds\n"
    "s_waitcnt lgkmcnt(5)\n"
    "v_mfma_f32_32x32x16_bf16 v[32:47], v[24:27], a[216:219], v[32:47]\n"
    "ds_read_b64_tr_b16 v[18:19], v1 offset:33280\n"
    "ds_read_b128 a[228:231], v2 offset:4096\n"
    "buffer_load_dwordx4 v13, s[40:43], s74 offen offset:1024 lds\n"
    "s_waitcnt lgkmcnt(6)\n"
    "v_mfma_f32_32x32x16_bf16 v[48:63], v[24:27], a[220:223], v[48:63]\n"
    "ds_read_b64_tr_b16 v[20:21], v1 offset:34816\n"
    "s_mul_i32 s74, s67, 0x600\n"
    "s_add_u32 m0, s81, 0x6000\n"
    "s_nop 0\n"
    "buffer_load_dwordx4 v12, s[40:43], s74 offen lds\n"
    "s_waitcnt lgkmcnt(5)\n"
    "v_mfma_f32_32x32x16_bf16 v[80:95], v[28:31], a[220:223], v[80:95]\n"
    "ds_read_b64_tr_b16 v[22:23], v1 offset:35328\n"
    "buffer_load_dwordx4 v13, s[40:43], s74 offen offset:1024 lds\n"
    "v_mfma_f32_32x32x16_bf16 v[64:79], v[28:31], a[216:219], v[64:79]\n"
    "ds_read_b64_tr_b16 v[24:25], v1 offset:36864\n"
    "ds_read_b128 a[232:235], v3\n"
    "s_mul_i32 s30, s71, 0x100\n"
    "s_add_u32 s30, s30, 0x80\n"
    "s_add_u32 s40, s20, s30\n"
    "s_addc_u32 s41, s21, 0\n"
    "s_sub_u32 s42, 0x1c0000, s30\n"
    "s_mov_b32 s43, 0x20000\n"
    "s_add_u32 s44, s22, s30\n"
    "s_addc_u32 s45, s23, 0\n"
    "s_sub_u32 s44, s44, 0x800\n"
    "s_subb_u32 s45, s45, 0\n"
    "s_sub_u32 s46, 0x1c0800, s30\n"
    "s_mov_b32 s47, 0x20000\n"
    "s_mul_i32 s74, s60, 0x700\n"
    "s_add_u32 m0, s80, 0x8000\n"
    "s_nop 0\n"
    "buffer_load_dwordx4 v10, s[40:43], s74 offen lds\n"
    "s_waitcnt lgkmcnt(5)\n"
    "v_mfma_f32_32x32x16_bf16 v[32:47], v[16:19], a[224:227], v[32:47]\n"
    "ds_read_b64_tr_b16 v[26:27], v1 offset:37376\n"
    "ds_read_b128 a[236:239], v3 offset:4096\n"
    "buffer_load_dwordx4 v11, s[40:43], s74 offen offset:1024 lds\n"
    "s_waitcnt lgkmcnt(6)\n"
    "v_mfma_f32_32x32x16_bf16 v[48:63], v[16:19], a[228:231], v[48:63]\n"
    "ds_read_b64_tr_b16 v[28:29], v1 offset:38912\n"
    "buffer_load_dwordx4 v10, s[44:47], s74 offen offset:2048 lds\n"
    "s_waitcnt lgkmcnt(5)\n"
    "v_mfma_f32_32x32x16_bf16 v[80:95], v[20:23], a[228:231], v[80:95]\n"
    "ds_read_b64_tr_b16 v[30:31], v1 offset:39424\n"
    "buffer_load_dwordx4 v11, s[44:47], s74 offen offset:3072 lds\n"
    "v_mfma_f32_32x32x16_bf16 v[64:79], v[20:23], a[224:227], v[64:79]\n"
    "ds_read_b64_tr_b16 v[16:17], v1 offset:40960\n"
    "ds_read_b128 a[240:243], v4\n"
    "s_waitcnt lgkmcnt(5)\n"
    "v_mfma_f32_32x32x16_bf16 v[32:47], v[24:27], a[232:235], v[32:47]\n"
    "ds_read_b64_tr_b16 v[18:19], v1 offset:41472\n"
    "ds_read_b128 a[244:247], v4 offset:4096\n"
    "s_waitcnt lgkmcnt(6)\n"
    "v_mfma_f32_32x32x16_bf16 v[48:63], v[24:27], a[236:239], v[48:63]\n"
    "ds_read_b64_tr_b16 v[20:21], v1 offset:43008\n"
    "s_waitcnt lgkmcnt(5)\n"
    "v_mfma_f32_32x32x16_bf16 v[80:95], v[28:31], a[236:239], v[80:95]\n"
    "ds_read_b64_tr_b16 v[22:23], v1 offset:43520\n"
    "v_mfma_f32_32x32x16_bf16 v[64:79], v[28:31], a[232:235], v[64:79]\n"
    "ds_read_b64_tr_b16 v[24:25], v1 offset:45056\n"
    "ds_read_b128 a[248:251], v5\n"
    "s_waitcnt lgkmcnt(5)\n"
    "v_mfma_f32_32x32x16_bf16 v[32:47], v[16:19], a[240:243], v[32:47]\n"
    "ds_read_b64_tr_b16 v[26:27], v1 offset:45568\n"
    "ds_read_b128 a[252:255], v5 offset:4096\n"
    "s_waitcnt lgkmcnt(6)\n"
    "v_mfma_f32_32x32x16_bf16 v[48:63], v[16:19], a[244:247], v[48:63]\n"
    "ds_read_b64_tr_b16 v[28:29], v1 offset:47104\n"
    "s_waitcnt lgkmcnt(5)\n"
    "v_mfma_f32_32x32x16_bf16 v[80:95], v[20:23], a[244:247], v[80:95]\n"
    "ds_read_b64_tr_b16 v[30:31], v1 offset:47616\n"
    "v_mfma_f32_32x32x16_bf16 v[64:79], v[20:23], a[240:243], v[64:79]\n"
    "s_waitcnt vmcnt(16)\n"
    "s_barrier\n"
    "ds_read_b64_tr_b16 v[16:17], v1 offset:49152\n"
    "s_mul_i32 s74, s61, 0x700\n"
    "s_add_u32 m0, s80, 0xc000\n"
    "s_nop 0\n"
    "buffer_load_dwordx4 v10, s[40:43], s74 offen lds\n"
    "s_waitcnt lgkmcnt(4)\n"
    "v_mfma_f32_32x32x16_bf16 v[32:47], v[24:27], a[248:251], v[32:47]\n"
    "ds_read_b64_tr_b16 v[18:19], v1 offset:49664\n"
    "buffer_load_dwordx4 v11, s[40:43], s74 offen offset:1024 lds\n"
    "s_waitcnt lgkmcnt(4)\n"
    "v_mfma_f32_32x32x16_bf16 v[48:63], v[24:27], a[252:255], v[48:63]\n"
    "ds_read_b64_tr_b16 v[20:21], v1 offset:51200\n"
    "buffer_load_dwordx4 v10, s[44:47], s74 offen offset:2048 lds\n"
    "s_waitcnt lgkmcnt(3)\n"
    "v_mfma_f32_32x32x16_bf16 v[80:95], v[28:31], a[252:255], v[80:95]\n"
    "ds_read_b64_tr_b16 v[22:23], v1 offset:51712\n"
    "buffer_load_dwordx4 v11, s[44:47], s74 offen offset:3072 lds\n"
    "v_mfma_f32_32x32x16_bf16 v[64:79], v[28:31], a[248:251], v[64:79]\n"
    "ds_read_b64_tr_b16 v[24:25], v1 offset:53248\n"
    "s_mul_i32 s74, s62, 0x700\n"
    "s_add_u32 m0, s80, 0x10000\n"
    "s_nop 0\n"
    "buffer_load_dwordx4 v10, s[40:43], s74 offen lds\n"
    "v_xor_b32 v197, s72, v193\n"
    "v_bitop3_b32 v197, v197, v195, s72 bitop3:0xf6\n"
    "v_cmp_ne_u32 s[88:89], 0, v197\n"
    "s_cmp_lg_u64 s[88:89], 0\n"
    "s_cbranch_scc1 LA_slow_1%=\n"
    "LA_back_2%=:\n"
    "v_and_b32 v166, 0xffff0000, v192\n"
    "v_and_b32 v192, 0xffff, v192\n"
    "v_rcp_f32 v166, v166\n"
    "v_lshl_or_b32 v192, v192, 16, v192\n"
    "v_xor_b32 v160, 0x80008000, v192\n"
    "v_mov_b32 v161, 0\n"
    "v_mul_f32 v164, 0x3fb8aa3b, v166\n"
    "v_and_b32 v167, 0xffff0000, v194\n"
    "v_and_b32 v194, 0xffff, v194\n"
    "v_rcp_f32 v167, v167\n"
    "v_lshl_or_b32 v194, v194, 16, v194\n"
    "v_xor_b32 v162, 0x80008000, v194\n"
    "v_mov_b32 v163, 0\n"
    "v_mul_f32 v165, 0x3fb8aa3b, v167\n"
    "s_mul_i32 s34, s82, 12\n"
    "s_lshl_b32 s35, s76, 1\n"
    "s_add_u32 s34, s34, s35\n"
    "s_add_u32 s34, s34, s75\n"
    "s_add_u32 s34, s34, 0\n"
    "s_lshl_b32 s34, s34, 12\n"
    "s_add_u32 s34, s34, 0x990000\n"
    "v_and_b32 v184, 31, v0\n"
    "v_lshlrev_b32 v184, 3, v184\n"
    "v_lshrrev_b32 v185, 5, v0\n"
    "v_lshl_add_u32 v15, v185, 11, v184\n"
    "buffer_load_dwordx2 v[168:169], v15, s[48:51], s34 offen sc1\n"
    "buffer_load_dwordx2 v[170:171], v15, s[48:51], s34 offen offset:256 sc1\n"
    "buffer_load_dwordx2 v[172:173], v15, s[48:51], s34 offen offset:512 sc1\n"
    "buffer_load_dwordx2 v[174:175], v15, s[48:51], s34 offen offset:768 sc1\n"
    "buffer_load_dwordx2 v[176:177], v15, s[48:51], s34 offen offset:1024 sc1\n"
    "buffer_load_dwordx2 v[178:179], v15, s[48:51], s34 offen offset:1280 sc1\n"
    "buffer_load_dwordx2 v[180:181], v15, s[48:51], s34 offen offset:1536 sc1\n"
    "buffer_load_dwordx2 v[182:183], v15, s[48:51], s34 offen offset:1792 sc1\n"
    "s_waitcnt vmcnt(7)\n"
    "v_xor_b32 v185, s72, v169\n"
    "s_waitcnt vmcnt(6)\n"
    "v_bitop3_b32 v185, v185, v171, s72 bitop3:0xf6\n"
    "s_waitcnt vmcnt(5)\n"
    "v_bitop3_b32 v185, v185, v173, s72 bitop3:0xf6\n"
    "s_waitcnt vmcnt(4)\n"
    "v_bitop3_b32 v185, v185, v175, s72 bitop3:0xf6\n"
    "s_waitcnt vmcnt(3)\n"
    "v_bitop3_b32 v185, v185, v177, s72 bitop3:0xf6\n"
    "s_waitcnt vmcnt(2)\n"
    "v_bitop3_b32 v185, v185, v179, s72 bitop3:0xf6\n"
    "s_waitcnt vmcnt(1)\n"
    "v_bitop3_b32 v185, v185, v181, s72 bitop3:0xf6\n"
    "s_waitcnt vmcnt(0)\n"
    "v_bitop3_b32 v185, v185, v183, s72 bitop3:0xf6\n"
    "v_cmp_ne_u32 s[88:89], 0, v185\n"
    "s_cmp_lg_u64 s[88:89], 0\n"
    "s_cbranch_scc1 LA_slow_3%=\n"
    "LA_back_4%=:\n"
    "v_add_f32 v168, v168, v170\n"
    "v_add_f32 v172, v172, v174\n"
    "v_add_f32 v176, v176, v178\n"
    "v_add_f32 v180, v180, v182\n"
    "v_add_f32 v168, v168, v172\n"
    "v_add_f32 v176, v176, v180\n"
    "v_add_f32 v168, v168, v176\n"
    "v_cvt_pk_bf16_f32 v185, v168, v168\n"
    "v_and_b32 v185, 0xffff0000, v185\n"
    "v_sub_f32 v185, v168, v185\n"
    "v_cvt_pk_bf16_f32 v185, v168, v185\n"
    "s_lshl_b32 s35, s76, 9\n"
    "s_add_u32 s35, s35, 0x27000\n"
    "v_lshl_add_u32 v184, v0, 2, s35\n"
    "s_lshl_b32 s35, s75, 8\n"
    "v_add_u32 v15, s35, v184\n"
    "ds_write_b32 v15, v185\n"
    "s_waitcnt lgkmcnt(0)\n"
    "s_barrier\n"
    "ds_read_b32 v188, v184\n"
    "ds_read_b32 v190, v184 offset:256\n"
    "v_mov_b32 v189, 0\n"
    "v_mov_b32 v191, 0\n"
    "v_mfma_f32_32x32x16_bf16 v[96:111], v[16:19], a[0:3], 0\n"
    "ds_read_b64_tr_b16 v[26:27], v1 offset:53760\n"
    "buffer_load_dwordx4 v11, s[40:43], s74 offen offset:1024 lds\n"
    "v_bfe_u32 v180, v0, 2, 2\n"
    "v_and_b32 v181, 3, v0\n"
    "v_bfe_u32 v182, v0, 4, 1\n"
    "v_mfma_f32_32x32x16_bf16 v[112:127], v[16:19], a[4:7], 0\n"
    "ds_read_b64_tr_b16 v[28:29], v1 offset:55296\n"
    "buffer_load_dwordx4 v10, s[44:47], s74 offen offset:2048 lds\n"
    "v_lshrrev_b32 v183, 5, v0\n"
    "s_lshr_b32 s34, s70, 3\n"
    "s_mul_i32 s34, s34, 0x1000\n"
    "v_mfma_f32_32x32x16_bf16 v[96:111], v[20:23], a[8:11], v[96:111]\n"
    "ds_read_b64_tr_b16 v[30:31], v1 offset:55808\n"
    "buffer_load_dwordx4 v11, s[44:47], s74 offen offset:3072 lds\n"
    "s_add_u32 s34, s34, 0x1c000\n"
    "v_lshlrev_b32 v183, 11, v183\n"
    "v_lshl_add_u32 v183, v181, 3, v183\n"
    "v_mfma_f32_32x32x16_bf16 v[112:127], v[20:23], a[12:15], v[112:127]\n"
    "ds_read_b64_tr_b16 v[16:17], v1 offset:57344\n"
    "v_lshl_add_u32 v182, v182, 3, v180\n"
    "v_lshl_add_u32 v183, v182, 7, v183\n"
    "v_lshrrev_b32 v180, 1, v180\n"
    "v_xor_b32 v180, s76, v180\n"
    "s_waitcnt lgkmcnt(5)\n"
    "v_mfma_f32_32x32x8_bf16 v[32:47], v[188:189], v[160:161], v[32:47]\n"
    "v_lshl_add_u32 v183, v180, 6, v183\n"
    "v_add_u32 v15, s34, v183\n"
    "ds_read_b64_tr_b16 v[168:169], v15\n"
    "ds_read_b64_tr_b16 v[170:171], v15 offset:512\n"
    "ds_read_b64_tr_b16 v[172:173], v15 offset:32\n"
    "v_mfma_f32_32x32x8_bf16 v[48:63], v[188:189], v[162:163], v[48:63]\n"
    "ds_read_b64_tr_b16 v[174:175], v15 offset:544\n"
    "v_mov_b32 v176, 0x3f803f80\n"
    "v_mov_b32 v177, 0x3f803f80\n"
    "v_mov_b32 v178, 0x3f803f80\n"
    "v_mov_b32 v179, 0x3f803f80\n"
    "s_waitcnt lgkmcnt(8)\n"
    "v_mfma_f32_32x32x8_bf16 v[64:79], v[190:191], v[160:161], v[64:79]\n"
    "s_mul_i32 s34, s82, 12\n"
    "s_lshl_b32 s35, s76, 1\n"
    "s_add_u32 s34, s34, s35\n"
    "s_add_u32 s34, s34, 4\n"
    "s_lshl_b32 s34, s34, 12\n"
    "v_mfma_f32_32x32x8_bf16 v[80:95], v[190:191], v[162:163], v[80:95]\n"
    "s_add_u32 s34, s34, 0x990000\n"
    "s_and_b32 s35, s70, 7\n"
    "s_lshl_b32 s35, s35, 9\n"
    "s_add_u32 s34, s34, s35\n"
    "s_lshr_b32 s35, s70, 3\n"
    "s_waitcnt lgkmcnt(7)\n"
    "v_mfma_f32_32x32x16_bf16 v[96:111], v[24:27], a[16:19], v[96:111]\n"
    "ds_read_b64_tr_b16 v[18:19], v1 offset:57856\n"
    "s_lshl_b32 s35, s35, 8\n"
    "s_add_u32 s34, s34, s35\n"
    "v_and_b32 v15, 31, v0\n"
    "s_waitcnt lgkmcnt(3)\n"
    "v_mfma_f32_16x16x32_bf16 v[180:183], v[176:179], v[168:171], 0\n"
    "s_waitcnt lgkmcnt(1)\n"
    "v_mfma_f32_16x16x32_bf16 v[184:187], v[176:179], v[172:175], 0\n"
    "v_lshlrev_b32 v15, 3, v15\n"
    "v_mfma_f32_32x32x16_bf16 v[112:127], v[24:27], a[20:23], v[112:127]\n"
    "ds_read_b64_tr_b16 v[20:21], v1 offset:59392\n"
    "s_mul_i32 s10, s71, 0x1000\n"
    "s_lshl_b32 s11, s76, 10\n"
    "s_add_u32 s10, s10, s11\n"
    "s_add_u32 s10, s10, s83\n"
    "v_mfma_f32_32x32x16_bf16 v[96:111], v[28:31], a[24:27], v[96:111]\n"
    "ds_read_b64_tr_b16 v[22:23], v1 offset:59904\n"
    "v_cndmask_b32 v180, v180, v184, s[92:93]\n"
    "v_mov_b32 v181, s72\n"
    "buffer_store_dwordx2 v[180:181], v15, s[48:51], s34 offen sc1\n"
    "s_add_u32 s10, s10, 0\n"
    "v_mfma_f32_32x32x16_bf16 v[112:127], v[28:31], a[28:31], v[112:127]\n"
    "ds_read_b64_tr_b16 v[24:25], v1 offset:61440\n"
    "s_add_u32 s11, s10, 0x24000\n"
    "v_mul_f32 v32, v32, v64\n"
    "v_mul_f32 v33, v33, v65\n"
    "v_mul_f32 v34, v34, v66\n"
    "s_waitcnt lgkmcnt(3)\n"
    "v_mfma_f32_32x32x16_bf16 v[96:111], v[16:19], a[32:35], v[96:111]\n"
    "ds_read_b64_tr_b16 v[26:27], v1 offset:61952\n"
    "v_mul_f32 v35, v35, v67\n"
    "v_mul_f32 v36, v36, v68\n"
    "v_mul_f32 v37, v37, v69\n"
    "v_mul_f32 v38, v38, v70\n"
    "v_mfma_f32_32x32x16_bf16 v[112:127], v[16:19], a[36:39], v[112:127]\n"
    "ds_read_b64_tr_b16 v[28:29], v1 offset:63488\n"
    "v_mul_f32 v39, v39, v71\n"
    "v_mul_f32 v40, v40, v72\n"
    "v_mul_f32 v41, v41, v73\n"
    "v_mul_f32 v42, v42, v74\n"
    "s_waitcnt lgkmcnt(3)\n"
    "v_mfma_f32_32x32x16_bf16 v[96:111], v[20:23], a[40:43], v[96:111]\n"
    "ds_read_b64_tr_b16 v[30:31], v1 offset:64000\n"
    "v_mul_f32 v43, v43, v75\n"
    "v_mul_f32 v44, v44, v76\n"
    "v_mul_f32 v45, v45, v77\n"
    "v_mul_f32 v46, v46, v78\n"
    "v_mfma_f32_32x32x16_bf16 v[112:127], v[20:23], a[44:47], v[112:127]\n"
    "s_barrier\n"
    "v_add_u32 v1, 0x8000, v1\n"
    "ds_read_b64_tr_b16 v[16:17], v1 offset:32768\n"
    "s_mul_i32 s74, s63, 0x700\n"
    "s_add_u32 m0, s80, 0x14000\n"
    "s_nop 0\n"
    "buffer_load_dwordx4 v10, s[40:43], s74 offen lds\n"
    "s_waitcnt lgkmcnt(3)\n"
    "v_mfma_f32_32x32x16_bf16 v[96:111], v[24:27], a[48:51], v[96:111]\n"
    "ds_read_b64_tr_b16 v[18:19], v1 offset:33280\n"
    "buffer_load_dwordx4 v11, s[40:43], s74 offen offset:1024 lds\n"
    "v_mul_f32 v47, v47, v79\n"
    "v_cvt_pk_bf16_f32 v64, v32, v33\n"
    "v_cvt_pk_bf16_f32 v65, v34, v35\n"
    "v_mfma_f32_32x32x16_bf16 v[112:127], v[24:27], a[52:55], v[112:127]\n"
    "ds_read_b64_tr_b16 v[20:21], v1 offset:34816\n"
    "buffer_load_dwordx4 v10, s[44:47], s74 offen offset:2048 lds\n"
    "v_cvt_pk_bf16_f32 v66, v36, v37\n"
    "v_cvt_pk_bf16_f32 v67, v38, v39\n"
    "buffer_store_dwordx4 v[64:67], v14, s[48:51], s10 offen sc1\n"
    "s_waitcnt lgkmcnt(3)\n"
    "v_mfma_f32_32x32x16_bf16 v[96:111], v[28:31], a[56:59], v[96:111]\n"
    "ds_read_b64_tr_b16 v[22:23], v1 offset:35328\n"
    "buffer_load_dwordx4 v11, s[44:47], s74 offen offset:3072 lds\n"
    "v_cvt_pk_bf16_f32 v68, v40, v41\n"
    "v_cvt_pk_bf16_f32 v69, v42, v43\n"
    "v_cvt_pk_bf16_f32 v70, v44, v45\n"
    "v_mfma_f32_32x32x16_bf16 v[112:127], v[28:31], a[60:63], v[112:127]\n"
    "ds_read_b64_tr_b16 v[24:25], v1 offset:36864\n"
    "s_mul_i32 s74, s64, 0x700\n"
    "s_add_u32 m0, s80, 0x18000\n"
    "s_nop 0\n"
    "buffer_load_dwordx4 v10, s[40:43], s74 offen lds\n"
    "v_cvt_pk_bf16_f32 v71, v46, v47\n"
    "s_waitcnt lgkmcnt(3)\n"
    "v_mfma_f32_32x32x16_bf16 v[96:111], v[16:19], a[64:67], v[96:111]\n"
    "ds_read_b64_tr_b16 v[26:27], v1 offset:37376\n"
    "buffer_load_dwordx4 v11, s[40:43], s74 offen offset:1024 lds\n"
    "buffer_store_dwordx4 v[68:71], v14, s[48:51], s10 offen offset:512 sc1\n"
    "v_mul_f32 v48, v48, v80\n"
    "v_mul_f32 v49, v49, v81\n"
    "v_mfma_f32_32x32x16_bf16 v[112:127], v[16:19], a[68:71], v[112:127]\n"
    "ds_read_b64_tr_b16 v[28:29], v1 offset:38912\n"
    "buffer_load_dwordx4 v10, s[44:47], s74 offen offset:2048 lds\n"
    "v_mul_f32 v50, v50, v82\n"
    "v_mul_f32 v51, v51, v83\n"
    "v_mul_f32 v52, v52, v84\n"
    "s_waitcnt lgkmcnt(3)\n"
    "v_mfma_f32_32x32x16_bf16 v[96:111], v[20:23], a[72:75], v[96:111]\n"
    "ds_read_b64_tr_b16 v[30:31], v1 offset:39424\n"
    "buffer_load_dwordx4 v11, s[44:47], s74 offen offset:3072 lds\n"
    "v_mul_f32 v53, v53, v85\n"
    "v_mul_f32 v54, v54, v86\n"
    "v_mul_f32 v55, v55, v87\n"
    "v_mfma_f32_32x32x16_bf16 v[112:127], v[20:23], a[76:79], v[112:127]\n"
    "ds_read_b64_tr_b16 v[16:17], v1 offset:40960\n"
    "v_mul_f32 v56, v56, v88\n"
    "v_mul_f32 v57, v57, v89\n"
    "v_mul_f32 v58, v58, v90\n"
    "v_mul_f32 v59, v59, v91\n"
    "s_waitcnt lgkmcnt(3)\n"
    "v_mfma_f32_32x32x16_bf16 v[96:111], v[24:27], a[80:83], v[96:111]\n"
    "ds_read_b64_tr_b16 v[18:19], v1 offset:41472\n"
    "v_mul_f32 v60, v60, v92\n"
    "v_mul_f32 v61, v61, v93\n"
    "v_mul_f32 v62, v62, v94\n"
    "v_mul_f32 v63, v63, v95\n"
    "v_mfma_f32_32x32x16_bf16 v[112:127], v[24:27], a[84:87], v[112:127]\n"
    "ds_read_b64_tr_b16 v[20:21], v1 offset:43008\n"
    "v_cvt_pk_bf16_f32 v80, v48, v49\n"
    "v_cvt_pk_bf16_f32 v81, v50, v51\n"
    "v_cvt_pk_bf16_f32 v82, v52, v53\n"
    "v_cvt_pk_bf16_f32 v83, v54, v55\n"
    "s_waitcnt lgkmcnt(3)\n"
    "v_mfma_f32_32x32x16_bf16 v[96:111], v[28:31], a[88:91], v[96:111]\n"
    "ds_read_b64_tr_b16 v[22:23], v1 offset:43520\n"
    "buffer_store_dwordx4 v[80:83], v14, s[48:51], s11 offen sc1\n"
    "v_cvt_pk_bf16_f32 v84, v56, v57\n"
    "v_cvt_pk_bf16_f32 v85, v58, v59\n"
    "v_cvt_pk_bf16_f32 v86, v60, v61\n"
    "v_mfma_f32_32x32x16_bf16 v[112:127], v[28:31], a[92:95], v[112:127]\n"
    "ds_read_b64_tr_b16 v[24:25], v1 offset:45056\n"
    "v_cvt_pk_bf16_f32 v87, v62, v63\n"
    "buffer_store_dwordx4 v[84:87], v14, s[48:51], s11 offen offset:512 sc1\n"
    "v_mov_b32 v249, 0\n"
    "v_mov_b32 v250, s72\n"
    "s_waitcnt lgkmcnt(3)\n"
    "v_mfma_f32_32x32x16_bf16 v[96:111], v[16:19], a[96:99], v[96:111]\n"
    "ds_read_b64_tr_b16 v[26:27], v1 offset:45568\n"
    "v_mfma_f32_32x32x16_bf16 v[112:127], v[16:19], a[100:103], v[112:127]\n"
    "ds_read_b64_tr_b16 v[28:29], v1 offset:47104\n"
    "s_waitcnt lgkmcnt(3)\n"
    "v_mfma_f32_32x32x16_bf16 v[96:111], v[20:23], a[104:107], v[96:111]\n"
    "ds_read_b64_tr_b16 v[30:31], v1 offset:47616\n"
    "v_mfma_f32_32x32x16_bf16 v[112:127], v[20:23], a[108:111], v[112:127]\n"
    "s_barrier\n"
    "v_add_u32 v1, 0xfffe8000, v1\n"
    "ds_read_b64_tr_b16 v[16:17], v1\n"
    "s_mul_i32 s74, s65, 0x700\n"
    "s_add_u32 m0, s80, 0x1c000\n"
    "s_nop 0\n"
    "buffer_load_dwordx4 v10, s[40:43], s74 offen lds\n"
    "s_waitcnt lgkmcnt(3)\n"
    "v_mfma_f32_32x32x16_bf16 v[96:111], v[24:27], a[112:115], v[96:111]\n"
    "ds_read_b64_tr_b16 v[18:19], v1 offset:512\n"
    "buffer_load_dwordx4 v11, s[40:43], s74 offen offset:1024 lds\n"
    "v_mfma_f32_32x32x16_bf16 v[112:127], v[24:27], a[116:119], v[112:127]\n"
    "ds_read_b64_tr_b16 v[20:21], v1 offset:2048\n"
    "buffer_load_dwordx4 v10, s[44:47], s74 offen offset:2048 lds\n"
    "s_waitcnt lgkmcnt(3)\n"
    "v_mfma_f32_32x32x16_bf16 v[96:111], v[28:31], a[120:123], v[96:111]\n"
    "ds_read_b64_tr_b16 v[22:23], v1 offset:2560\n"
    "buffer_load_dwordx4 v11, s[44:47], s74 offen offset:3072 lds\n"
    "v_mfma_f32_32x32x16_bf16 v[112:127], v[28:31], a[124:127], v[112:127]\n"
    "ds_read_b64_tr_b16 v[24:25], v1 offset:4096\n"
    "s_waitcnt lgkmcnt(3)\n"
    "v_mfma_f32_32x32x16_bf16 v[96:111], v[16:19], a[128:131], v[96:111]\n"
    "ds_read_b64_tr_b16 v[26:27], v1 offset:4608\n"
    "v_mfma_f32_32x32x16_bf16 v[112:127], v[16:19], a[132:135], v[112:127]\n"
    "ds_read_b64_tr_b16 v[28:29], v1 offset:6144\n"
    "s_waitcnt lgkmcnt(3)\n"
    "v_mfma_f32_32x32x16_bf16 v[96:111], v[20:23], a[136:139], v[96:111]\n"
    "ds_read_b64_tr_b16 v[30:31], v1 offset:6656\n"
    "s_mul_i32 s34, s82, 12\n"
    "s_lshl_b32 s35, s76, 1\n"
    "s_add_u32 s34, s34, s35\n"
    "s_add_u32 s34, s34, 4\n"
    "v_mfma_f32_32x32x16_bf16 v[112:127], v[20:23], a[140:143], v[112:127]\n"
    "ds_read_b64_tr_b16 v[16:17], v1 offset:8192\n"
    "s_lshl_b32 s34, s34, 12\n"
    "s_add_u32 s34, s34, 0x990000\n"
    "v_and_b32 v184, 31, v0\n"
    "v_lshlrev_b32 v184, 3, v184\n"
    "s_waitcnt lgkmcnt(3)\n"
    "v_mfma_f32_32x32x16_bf16 v[96:111], v[24:27], a[144:147], v[96:111]\n"
    "ds_read_b64_tr_b16 v[18:19], v1 offset:8704\n"
    "v_lshrrev_b32 v185, 5, v0\n"
    "v_lshl_add_u32 v15, v185, 11, v184\n"
    "buffer_load_dwordx2 v[168:169], v15, s[48:51], s34 offen sc1\n"
    "buffer_load_dwordx2 v[170:171], v15, s[48:51], s34 offen offset:256 sc1\n"
    "v_mfma_f32_32x32x16_bf16 v[112:127], v[24:27], a[148:151], v[112:127]\n"
    "ds_read_b64_tr_b16 v[20:21], v1 offset:10240\n"
    "buffer_load_dwordx2 v[172:173], v15, s[48:51], s34 offen offset:512 sc1\n"
    "buffer_load_dwordx2 v[174:175], v15, s[48:51], s34 offen offset:768 sc1\n"
    "buffer_load_dwordx2 v[176:177], v15, s[48:51], s34 offen offset:1024 sc1\n"
    "buffer_load_dwordx2 v[178:179], v15, s[48:51], s34 offen offset:1280 sc1\n"
    "s_waitcnt lgkmcnt(3)\n"
    "v_mfma_f32_32x32x16_bf16 v[96:111], v[28:31], a[152:155], v[96:111]\n"
    "ds_read_b64_tr_b16 v[22:23], v1 offset:10752\n"
    "buffer_load_dwordx2 v[180:181], v15, s[48:51], s34 offen offset:1536 sc1\n"
    "buffer_load_dwordx2 v[182:183], v15, s[48:51], s34 offen offset:1792 sc1\n"
    "v_mfma_f32_32x32x16_bf16 v[112:127], v[28:31], a[156:159], v[112:127]\n"
    "ds_read_b64_tr_b16 v[24:25], v1 offset:12288\n"
    "s_waitcnt lgkmcnt(3)\n"
    "v_mfma_f32_32x32x16_bf16 v[96:111], v[16:19], a[160:163], v[96:111]\n"
    "ds_read_b64_tr_b16 v[26:27], v1 offset:12800\n"
    "v_mfma_f32_32x32x16_bf16 v[112:127], v[16:19], a[164:167], v[112:127]\n"
    "ds_read_b64_tr_b16 v[28:29], v1 offset:14336\n"
    "s_waitcnt lgkmcnt(3)\n"
    "v_mfma_f32_32x32x16_bf16 v[96:111], v[20:23], a[168:171], v[96:111]\n"
    "ds_read_b64_tr_b16 v[30:31], v1 offset:14848\n"
    "v_mfma_f32_32x32x16_bf16 v[112:127], v[20:23], a[172:175], v[112:127]\n"
    "s_barrier\n"
    "ds_read_b64_tr_b16 v[16:17], v1 offset:16384\n"
    "s_mul_i32 s74, s66, 0x700\n"
    "s_add_u32 m0, s80, 0x20000\n"
    "s_nop 0\n"
    "buffer_load_dwordx4 v10, s[40:43], s74 offen lds\n"
    "s_waitcnt lgkmcnt(3)\n"
    "v_mfma_f32_32x32x16_bf16 v[96:111], v[24:27], a[176:179], v[96:111]\n"
    "ds_read_b64_tr_b16 v[18:19], v1 offset:16896\n"
    "buffer_load_dwordx4 v11, s[40:43], s74 offen offset:1024 lds\n"
    "v_mfma_f32_32x32x16_bf16 v[112:127], v[24:27], a[180:183], v[112:127]\n"
    "ds_read_b64_tr_b16 v[20:21], v1 offset:18432\n"
    "buffer_load_dwordx4 v10, s[44:47], s74 offen offset:2048 lds\n"
    "s_waitcnt vmcnt(15)\n"
    "buffer_store_dword v250, v249, s[48:51], s84 offen sc1\n"
    "s_waitcnt lgkmcnt(3)\n"
    "v_mfma_f32_32x32x16_bf16 v[96:111], v[28:31], a[184:187], v[96:111]\n"
    "ds_read_b64_tr_b16 v[22:23], v1 offset:18944\n"
    "buffer_load_dwordx4 v11, s[44:47], s74 offen offset:3072 lds\n"
    "v_mfma_f32_32x32x16_bf16 v[112:127], v[28:31], a[188:191], v[112:127]\n"
    "ds_read_b64_tr_b16 v[24:25], v1 offset:20480\n"
    "s_waitcnt lgkmcnt(3)\n"
    "v_mfma_f32_32x32x16_bf16 v[96:111], v[16:19], a[192:195], v[96:111]\n"
    "ds_read_b64_tr_b16 v[26:27], v1 offset:20992\n"
    "v_mfma_f32_32x32x16_bf16 v[112:127], v[16:19], a[196:199], v[112:127]\n"
    "ds_read_b64_tr_b16 v[28:29], v1 offset:22528\n"
    "s_waitcnt lgkmcnt(3)\n"
    "v_mfma_f32_32x32x16_bf16 v[96:111], v[20:23], a[200:203], v[96:111]\n"
    "ds_read_b64_tr_b16 v[30:31], v1 offset:23040\n"
    "v_mfma_f32_32x32x16_bf16 v[112:127], v[20:23], a[204:207], v[112:127]\n"
    "ds_read_b64_tr_b16 v[16:17], v1 offset:24576\n"
    "s_waitcnt lgkmcnt(3)\n"
    "v_mfma_f32_32x32x16_bf16 v[96:111], v[24:27], a[208:211], v[96:111]\n"
    "ds_read_b64_tr_b16 v[18:19], v1 offset:25088\n"
    "v_mfma_f32_32x32x16_bf16 v[112:127], v[24:27], a[212:215], v[112:127]\n"
    "ds_read_b64_tr_b16 v[20:21], v1 offset:26624\n"
    "s_waitcnt lgkmcnt(3)\n"
    "v_mfma_f32_32x32x16_bf16 v[96:111], v[28:31], a[216:219], v[96:111]\n"
    "ds_read_b64_tr_b16 v[22:23], v1 offset:27136\n"
    "v_mfma_f32_32x32x16_bf16 v[112:127], v[28:31], a[220:223], v[112:127]\n"
    "ds_read_b64_tr_b16 v[24:25], v1 offset:28672\n"
    "s_waitcnt lgkmcnt(3)\n"
    "v_mfma_f32_32x32x16_bf16 v[96:111], v[16:19], a[224:227], v[96:111]\n"
    "ds_read_b64_tr_b16 v[26:27], v1 offset:29184\n"
    "v_mfma_f32_32x32x16_bf16 v[112:127], v[16:19], a[228:231], v[112:127]\n"
    "ds_read_b64_tr_b16 v[28:29], v1 offset:30720\n"
    "s_waitcnt lgkmcnt(3)\n"
    "v_mfma_f32_32x32x16_bf16 v[96:111], v[20:23], a[232:235], v[96:111]\n"
    "ds_read_b64_tr_b16 v[30:31], v1 offset:31232\n"
    "v_mfma_f32_32x32x16_bf16 v[112:127], v[20:23], a[236:239], v[112:127]\n"
    "s_barrier\n"
    "ds_read_b64_tr_b16 v[16:17], v1 offset:32768\n"
    "s_mul_i32 s74, s67, 0x700\n"
    "s_add_u32 m0, s80, 0\n"
    "s_nop 0\n"
    "buffer_load_dwordx4 v10, s[40:43], s74 offen lds\n"
    "s_waitcnt lgkmcnt(3)\n"
    "v_mfma_f32_32x32x16_bf16 v[96:111], v[24:27], a[240:243], v[96:111]\n"
    "ds_read_b64_tr_b16 v[18:19], v1 offset:33280\n"
    "buffer_load_dwordx4 v11, s[40:43], s74 offen offset:1024 lds\n"
    "v_mfma_f32_32x32x16_bf16 v[112:127], v[24:27], a[244:247], v[112:127]\n"
    "ds_read_b64_tr_b16 v[20:21], v1 offset:34816\n"
    "buffer_load_dwordx4 v10, s[44:47], s74 offen offset:2048 lds\n"
    "s_waitcnt lgkmcnt(3)\n"
    "v_mfma_f32_32x32x16_bf16 v[96:111], v[28:31], a[248:251], v[96:111]\n"
    "ds_read_b64_tr_b16 v[22:23], v1 offset:35328\n"
    "buffer_load_dwordx4 v11, s[44:47], s74 offen offset:3072 lds\n"
    "v_mfma_f32_32x32x16_bf16 v[112:127], v[28:31], a[252:255], v[112:127]\n"
    "ds_read_b64_tr_b16 v[24:25], v1 offset:36864\n"
    "v_lshrrev_b32 v6, 3, v0\n"
    "s_mul_i32 s30, s69, 0x4000\n"
    "s_mov_b32 s31, 0x400\n"
    "v_mul_lo_u32 v6, v6, s31\n"
    "v_bfe_u32 v251, v0, 4, 2\n"
    "v_lshlrev_b32 v251, 1, v251\n"
    "v_bitop3_b32 v251, v0, 7, v251 bitop3:0x6a\n"
    "v_lshl_add_u32 v6, v251, 4, v6\n"
    "v_add_u32 v6, s30, v6\n"
    "v_add_u32 v7, 0x1c00, v6\n"
    "s_lshl_b32 s30, s79, 7\n"
    "s_add_u32 s52, s24, s30\n"
    "s_addc_u32 s53, s25, 0\n"
    "s_sub_u32 s54, 0x1c0000, s30\n"
    "s_mov_b32 s55, 0x20000\n"
    "s_mul_i32 s74, s77, 0x80000\n"
    "s_add_u32 s74, s74, 0\n"
    "s_add_u32 m0, s81, 0x4000\n"
    "s_nop 0\n"
    "buffer_load_dwordx4 v6, s[52:55], s74 offen lds\n"
    "s_waitcnt vmcnt(17)\n"
    "v_xor_b32 v185, s72, v169\n"
    "s_waitcnt vmcnt(16)\n"
    "v_bitop3_b32 v185, v185, v171, s72 bitop3:0xf6\n"
    "s_waitcnt vmcnt(15)\n"
    "v_bitop3_b32 v185, v185, v173, s72 bitop3:0xf6\n"
    "s_waitcnt vmcnt(14)\n"
    "v_bitop3_b32 v185, v185, v175, s72 bitop3:0xf6\n"
    "s_waitcnt vmcnt(13)\n"
    "v_bitop3_b32 v185, v185, v177, s72 bitop3:0xf6\n"
    "s_waitcnt vmcnt(12)\n"
    "v_bitop3_b32 v185, v185, v179, s72 bitop3:0xf6\n"
    "s_waitcnt vmcnt(11)\n"
    "v_bitop3_b32 v185, v185, v181, s72 bitop3:0xf6\n"
    "s_waitcnt vmcnt(10)\n"
    "v_bitop3_b32 v185, v185, v183, s72 bitop3:0xf6\n"
    "v_cmp_ne_u32 s[88:89], 0, v185\n"
    "s_cmp_lg_u64 s[88:89], 0\n"
    "s_cbranch_scc1 LA_slow_5%=\n"
    "LA_back_6%=:\n"
    "v_add_f32 v168, v168, v170\n"
    "v_add_f32 v172, v172, v174\n"
    "v_add_f32 v176, v176, v178\n"
    "v_add_f32 v180, v180, v182\n"
    "v_add_f32 v168, v168, v172\n"
    "v_add_f32 v176, v176, v180\n"
    "v_add_f32 v168, v168, v176\n"
    "v_cvt_pk_bf16_f32 v185, v168, v168\n"
    "v_and_b32 v185, 0xffff0000, v185\n"
    "v_sub_f32 v185, v168, v185\n"
    "v_cvt_pk_bf16_f32 v188, v168, v185\n"
    "v_mov_b32 v189, 0\n"
    "s_waitcnt lgkmcnt(3)\n"
    "v_mfma_f32_32x32x16_bf16 v[32:47], v[16:19], a[0:3], 0\n"
    "ds_read_b64_tr_b16 v[26:27], v1 offset:37376\n"
    "buffer_load_dwordx4 v7, s[52:55], s74 offen offset:1024 lds\n"
    "v_bfe_u32 v180, v0, 2, 2\n"
    "v_and_b32 v181, 3, v0\n"
    "v_bfe_u32 v182, v0, 4, 1\n"
    "v_mfma_f32_32x32x8_bf16 v[96:111], v[188:189], v[160:161], v[96:111]\n"
    "v_lshrrev_b32 v183, 5, v0\n"
    "s_lshr_b32 s34, s70, 3\n"
    "s_mul_i32 s34, s34, 0x2000\n"
    "s_add_u32 s34, s34, 0x8000\n"
    "s_lshl_b32 s35, s75, 11\n"
    "v_mfma_f32_32x32x8_bf16 v[112:127], v[188:189], v[162:163], v[112:127]\n"
    "s_add_u32 s34, s34, s35\n"
    "v_lshlrev_b32 v183, 12, v183\n"
    "v_lshl_add_u32 v183, v181, 3, v183\n"
    "v_lshl_add_u32 v182, v182, 3, v180\n"
    "v_lshl_add_u32 v183, v182, 7, v183\n"
    "v_mfma_f32_32x32x16_bf16 v[48:63], v[16:19], a[4:7], 0\n"
    "ds_read_b64_tr_b16 v[28:29], v1 offset:38912\n"
    "v_lshrrev_b32 v180, 1, v180\n"
    "v_xor_b32 v180, s76, v180\n"
    "v_lshl_add_u32 v183, v180, 6, v183\n"
    "v_add_u32 v15, s34, v183\n"
    "s_waitcnt lgkmcnt(3)\n"
    "v_mfma_f32_32x32x16_bf16 v[80:95], v[20:23], a[4:7], 0\n"
    "ds_read_b64_tr_b16 v[30:31], v1 offset:39424\n"
    "ds_read_b64_tr_b16 v[168:169], v15\n"
    "ds_read_b64_tr_b16 v[170:171], v15 offset:512\n"
    "ds_read_b64_tr_b16 v[172:173], v15 offset:32\n"
    "ds_read_b64_tr_b16 v[174:175], v15 offset:544\n"
    "v_mfma_f32_32x32x16_bf16 v[64:79], v[20:23], a[0:3], 0\n"
    "ds_read_b64_tr_b16 v[16:17], v1 offset:40960\n"
    "v_mov_b32 v176, 0x3f803f80\n"
    "v_mov_b32 v177, 0x3f803f80\n"
    "v_mov_b32 v178, 0x3f803f80\n"
    "v_mov_b32 v179, 0x3f803f80\n"
    "s_waitcnt lgkmcnt(7)\n"
    "v_mfma_f32_32x32x16_bf16 v[32:47], v[24:27], a[8:11], v[32:47]\n"
    "ds_read_b64_tr_b16 v[18:19], v1 offset:41472\n"
    "s_mul_i32 s34, s82, 12\n"
    "s_lshl_b32 s35, s76, 1\n"
    "s_add_u32 s34, s34, s35\n"
    "s_add_u32 s34, s34, s75\n"
    "v_mfma_f32_32x32x16_bf16 v[48:63], v[24:27], a[12:15], v[48:63]\n"
    "ds_read_b64_tr_b16 v[20:21], v1 offset:43008\n"
    "s_add_u32 s34, s34, 8\n"
    "s_lshl_b32 s34, s34, 12\n"
    "s_add_u32 s34, s34, 0x990000\n"
    "s_and_b32 s35, s70, 7\n"
    "s_waitcnt lgkmcnt(7)\n"
    "v_mfma_f32_32x32x16_bf16 v[80:95], v[28:31], a[12:15], v[80:95]\n"
    "ds_read_b64_tr_b16 v[22:23], v1 offset:43520\n"
    "s_lshl_b32 s35, s35, 9\n"
    "s_add_u32 s34, s34, s35\n"
    "s_lshr_b32 s35, s70, 3\n"
    "s_waitcnt lgkmcnt(6)\n"
    "v_mfma_f32_16x16x32_bf16 v[180:183], v[176:179], v[168:171], 0\n"
    "s_waitcnt lgkmcnt(4)\n"
    "v_mfma_f32_16x16x32_bf16 v[184:187], v[176:179], v[172:175], 0\n"
    "s_lshl_b32 s35, s35, 8\n"
    "v_mfma_f32_32x32x16_bf16 v[64:79], v[28:31], a[8:11], v[64:79]\n"
    "ds_read_b64_tr_b16 v[24:25], v1 offset:45056\n"
    "s_add_u32 s34, s34, s35\n"
    "v_and_b32 v15, 31, v0\n"
    "v_lshlrev_b32 v15, 3, v15\n"
    "v_bfe_u32 v232, v0, 2, 2\n"
    "s_waitcnt lgkmcnt(3)\n"
    "v_mfma_f32_32x32x16_bf16 v[32:47], v[16:19], a[16:19], v[32:47]\n"
    "ds_read_b64_tr_b16 v[26:27], v1 offset:45568\n"
    "v_cndmask_b32 v180, v180, v184, s[92:93]\n"
    "v_mov_b32 v181, s72\n"
    "buffer_store_dwordx2 v[180:181], v15, s[48:51], s34 offen sc1\n"
    "v_lshrrev_b32 v233, 5, v0\n"
    "v_mfma_f32_32x32x16_bf16 v[48:63], v[16:19], a[20:23], v[48:63]\n"
    "ds_read_b64_tr_b16 v[28:29], v1 offset:47104\n"
    "v_lshl_add_u32 v232, v233, 2, v232\n"
    "v_and_b32 v233, 3, v0\n"
    "v_lshlrev_b32 v233, 3, v233\n"
    "v_lshl_add_u32 v232, v232, 5, v233\n"
    "s_waitcnt lgkmcnt(3)\n"
    "v_mfma_f32_32x32x16_bf16 v[80:95], v[20:23], a[20:23], v[80:95]\n"
    "ds_read_b64_tr_b16 v[30:31], v1 offset:47616\n"
    "s_mul_i32 s11, s69, 0xc00\n"
    "s_add_u32 s11, s11, 0x24000\n"
    "v_add_u32 v232, s11, v232\n"
    "ds_read_b64_tr_b16 v[192:193], v232\n"
    "v_mfma_f32_32x32x16_bf16 v[64:79], v[20:23], a[16:19], v[64:79]\n"
    "s_barrier\n"
    "ds_read_b64_tr_b16 v[16:17], v1 offset:49152\n"
    "s_mul_i32 s74, s77, 0x80000\n"
    "s_add_u32 s74, s74, 0x20000\n"
    "s_add_u32 m0, s81, 0x6000\n"
    "s_nop 0\n"
    "buffer_load_dwordx4 v6, s[52:55], s74 offen lds\n"
    "s_waitcnt lgkmcnt(4)\n"
    "v_mfma_f32_32x32x16_bf16 v[32:47], v[24:27], a[24:27], v[32:47]\n"
    "ds_read_b64_tr_b16 v[18:19], v1 offset:49664\n"
    "buffer_load_dwordx4 v7, s[52:55], s74 offen offset:1024 lds\n"
    "ds_read_b64_tr_b16 v[194:195], v232 offset:256\n"
    "ds_read_b64_tr_b16 v[196:197], v232 offset:512\n"
    "ds_read_b64_tr_b16 v[198:199], v232 offset:768\n"
    "v_mfma_f32_32x32x16_bf16 v[48:63], v[24:27], a[28:31], v[48:63]\n"
    "ds_read_b64_tr_b16 v[20:21], v1 offset:51200\n"
    "v_mov_b32 v232, 0x80008000\n"
    "v_cndmask_b32 v233, 0, v232, s[92:93]\n"
    "v_cvt_pk_bf16_f32 v96, v96, v97\n"
    "v_cvt_pk_bf16_f32 v97, v98, v99\n"
    "s_waitcnt lgkmcnt(7)\n"
    "v_mfma_f32_32x32x16_bf16 v[80:95], v[28:31], a[28:31], v[80:95]\n"
    "ds_read_b64_tr_b16 v[22:23], v1 offset:51712\n"
    "v_cvt_pk_bf16_f32 v98, v100, v101\n"
    "v_cvt_pk_bf16_f32 v99, v102, v103\n"
    "v_cvt_pk_bf16_f32 v100, v104, v105\n"
    "s_waitcnt lgkmcnt(7)\n"
    "v_xor_b32 v192, v233, v192\n"
    "v_mfma_f32_32x32x16_bf16 v[64:79], v[28:31], a[24:27], v[64:79]\n"
    "ds_read_b64_tr_b16 v[24:25], v1 offset:53248\n"
    "v_xor_b32 v193, v233, v193\n"
    "v_cvt_pk_bf16_f32 v101, v106, v107\n"
    "v_cvt_pk_bf16_f32 v102, v108, v109\n"
    "v_cvt_pk_bf16_f32 v103, v110, v111\n"
    "s_waitcnt lgkmcnt(6)\n"
    "v_mfma_f32_32x32x16_bf16 v[32:47], v[16:19], a[32:35], v[32:47]\n"
    "ds_read_b64_tr_b16 v[26:27], v1 offset:53760\n"
    "v_cvt_pk_bf16_f32 v112, v112, v113\n"
    "s_waitcnt lgkmcnt(6)\n"
    "v_xor_b32 v194, v233, v194\n"
    "v_xor_b32 v195, v233, v195\n"
    "s_waitcnt lgkmcnt(5)\n"
    "v_xor_b32 v196, v233, v196\n"
    "v_mfma_f32_32x32x16_bf16 v[48:63], v[16:19], a[36:39], v[48:63]\n"
    "ds_read_b64_tr_b16 v[28:29], v1 offset:55296\n"
    "v_xor_b32 v197, v233, v197\n"
    "s_waitcnt lgkmcnt(5)\n"
    "v_xor_b32 v198, v233, v198\n"
    "v_xor_b32 v199, v233, v199\n"
    "v_cvt_pk_bf16_f32 v113, v114, v115\n"
    "v_mfma_f32_32x32x16_bf16 v[200:215], v[192:195], v[96:99], 0\n"
    "v_cvt_pk_bf16_f32 v114, v116, v117\n"
    "v_cvt_pk_bf16_f32 v115, v118, v119\n"
    "v_cvt_pk_bf16_f32 v116, v120, v121\n"
    "v_cvt_pk_bf16_f32 v117, v122, v123\n"
    "v_cvt_pk_bf16_f32 v118, v124, v125\n"
    "s_waitcnt lgkmcnt(3)\n"
    "v_mfma_f32_32x32x16_bf16 v[80:95], v[20:23], a[36:39], v[80:95]\n"
    "ds_read_b64_tr_b16 v[30:31], v1 offset:55808\n"
    "v_cvt_pk_bf16_f32 v119, v126, v127\n"
    "v_mfma_f32_32x32x16_bf16 v[64:79], v[20:23], a[32:35], v[64:79]\n"
    "ds_read_b64_tr_b16 v[16:17], v1 offset:57344\n"
    "s_waitcnt lgkmcnt(3)\n"
    "v_mfma_f32_32x32x16_bf16 v[32:47], v[24:27], a[40:43], v[32:47]\n"
    "ds_read_b64_tr_b16 v[18:19], v1 offset:57856\n"
    "v_max3_f32 v232, v200, v201, v202\n"
    "v_max3_f32 v232, v232, v203, v204\n"
    "v_max3_f32 v232, v232, v205, v206\n"
    "v_max_f32 v232, v232, v207\n"
    "v_mfma_f32_32x32x16_bf16 v[48:63], v[24:27], a[44:47], v[48:63]\n"
    "ds_read_b64_tr_b16 v[20:21], v1 offset:59392\n"
    "v_max3_f32 v233, v208, v209, v210\n"
    "v_max3_f32 v233, v233, v211, v212\n"
    "v_max3_f32 v233, v233, v213, v214\n"
    "v_max_f32 v233, v233, v215\n"
    "s_waitcnt lgkmcnt(3)\n"
    "v_mfma_f32_32x32x16_bf16 v[80:95], v[28:31], a[44:47], v[80:95]\n"
    "ds_read_b64_tr_b16 v[22:23], v1 offset:59904\n"
    "v_permlane32_swap_b32 v232, v233\n"
    "v_max_f32 v232, v232, v233\n"
    "v_mul_f32 v232, -v232, v164\n"
    "v_mov_b32 v233, v232\n"
    "v_mfma_f32_32x32x16_bf16 v[64:79], v[28:31], a[40:43], v[64:79]\n"
    "ds_read_b64_tr_b16 v[24:25], v1 offset:61440\n"
    "v_permlane32_swap_b32 v232, v233\n"
    "v_fma_f32 v200, v200, v164, v232\n"
    "v_fma_f32 v201, v201, v164, v232\n"
    "v_fma_f32 v202, v202, v164, v232\n"
    "s_waitcnt lgkmcnt(3)\n"
    "v_mfma_f32_32x32x16_bf16 v[32:47], v[16:19], a[48:51], v[32:47]\n"
    "ds_read_b64_tr_b16 v[26:27], v1 offset:61952\n"
    "v_fma_f32 v203, v203, v164, v232\n"
    "v_fma_f32 v204, v204, v164, v232\n"
    "v_fma_f32 v205, v205, v164, v232\n"
    "v_fma_f32 v206, v206, v164, v232\n"
    "v_mfma_f32_32x32x16_bf16 v[48:63], v[16:19], a[52:55], v[48:63]\n"
    "ds_read_b64_tr_b16 v[28:29], v1 offset:63488\n"
    "v_fma_f32 v207, v207, v164, v232\n"
    "v_fma_f32 v208, v208, v164, v233\n"
    "v_fma_f32 v209, v209, v164, v233\n"
    "v_fma_f32 v210, v210, v164, v233\n"
    "s_waitcnt lgkmcnt(3)\n"
    "v_mfma_f32_32x32x16_bf16 v[80:95], v[20:23], a[52:55], v[80:95]\n"
    "ds_read_b64_tr_b16 v[30:31], v1 offset:64000\n"
    "v_fma_f32 v211, v211, v164, v233\n"
    "v_fma_f32 v212, v212, v164, v233\n"
    "v_fma_f32 v213, v213, v164, v233\n"
    "v_fma_f32 v214, v214, v164, v233\n"
    "v_mfma_f32_32x32x16_bf16 v[64:79], v[20:23], a[48:51], v[64:79]\n"
    "s_barrier\n"
    "v_add_u32 v1, 0x10000, v1\n"
    "ds_read_b64_tr_b16 v[16:17], v1\n"
    "s_mul_i32 s74, s77, 0x80000\n"
    "s_add_u32 s74, s74, 0x40000\n"
    "s_add_u32 m0, s81, 0x8000\n"
    "s_nop 0\n"
    "buffer_load_dwordx4 v6, s[52:55], s74 offen lds\n"
    "s_waitcnt lgkmcnt(3)\n"
    "v_mfma_f32_32x32x16_bf16 v[32:47], v[24:27], a[56:59], v[32:47]\n"
    "ds_read_b64_tr_b16 v[18:19], v1 offset:512\n"
    "buffer_load_dwordx4 v7, s[52:55], s74 offen offset:1024 lds\n"
    "v_fma_f32 v215, v215, v164, v233\n"
    "v_exp_f32 v200, v200\n"
    "v_mfma_f32_32x32x16_bf16 v[48:63], v[24:27], a[60:63], v[48:63]\n"
    "ds_read_b64_tr_b16 v[20:21], v1 offset:2048\n"
    "s_mul_i32 s74, s77, 0x80000\n"
    "s_add_u32 s74, s74, 0x60000\n"
    "s_add_u32 m0, s81, 0xa000\n"
    "s_nop 0\n"
    "buffer_load_dwordx4 v6, s[52:55], s74 offen lds\n"
    "s_waitcnt lgkmcnt(3)\n"
    "v_mfma_f32_32x32x16_bf16 v[80:95], v[28:31], a[60:63], v[80:95]\n"
    "ds_read_b64_tr_b16 v[22:23], v1 offset:2560\n"
    "buffer_load_dwordx4 v7, s[52:55], s74 offen offset:1024 lds\n"
    "v_exp_f32 v201, v201\n"
    "v_mfma_f32_32x32x16_bf16 v[64:79], v[28:31], a[56:59], v[64:79]\n"
    "ds_read_b64_tr_b16 v[24:25], v1 offset:4096\n"
    "v_exp_f32 v202, v202\n"
    "v_exp_f32 v203, v203\n"
    "s_waitcnt lgkmcnt(3)\n"
    "v_mfma_f32_32x32x16_bf16 v[32:47], v[16:19], a[64:67], v[32:47]\n"
    "ds_read_b64_tr_b16 v[26:27], v1 offset:4608\n"
    "v_exp_f32 v204, v204\n"
    "v_exp_f32 v205, v205\n"
    "v_mfma_f32_32x32x16_bf16 v[48:63], v[16:19], a[68:71], v[48:63]\n"
    "ds_read_b64_tr_b16 v[28:29], v1 offset:6144\n"
    "v_exp_f32 v206, v206\n"
    "v_exp_f32 v207, v207\n"
    "s_waitcnt lgkmcnt(3)\n"
    "v_mfma_f32_32x32x16_bf16 v[80:95], v[20:23], a[68:71], v[80:95]\n"
    "ds_read_b64_tr_b16 v[30:31], v1 offset:6656\n"
    "v_exp_f32 v208, v208\n"
    "v_exp_f32 v209, v209\n"
    "v_mfma_f32_32x32x16_bf16 v[64:79], v[20:23], a[64:67], v[64:79]\n"
    "ds_read_b64_tr_b16 v[16:17], v1 offset:8192\n"
    "v_exp_f32 v210, v210\n"
    "v_exp_f32 v211, v211\n"
    "v_mfma_f32_32x32x16_bf16 v[216:231], v[196:199], v[100:103], 0\n"
    "v_exp_f32 v212, v212\n"
    "v_exp_f32 v213, v213\n"
    "v_add_f32 v232, v200, v201\n"
    "s_waitcnt lgkmcnt(3)\n"
    "v_mfma_f32_32x32x16_bf16 v[32:47], v[24:27], a[72:75], v[32:47]\n"
    "ds_read_b64_tr_b16 v[18:19], v1 offset:8704\n"
    "v_exp_f32 v214, v214\n"
    "v_exp_f32 v215, v215\n"
    "v_mfma_f32_32x32x16_bf16 v[48:63], v[24:27], a[76:79], v[48:63]\n"
    "ds_read_b64_tr_b16 v[20:21], v1 offset:10240\n"
    "v_add_f32 v232, v232, v202\n"
    "v_add_f32 v232, v232, v203\n"
    "v_add_f32 v232, v232, v204\n"
    "v_add_f32 v232, v232, v205\n"
    "s_waitcnt lgkmcnt(3)\n"
    "v_mfma_f32_32x32x16_bf16 v[80:95], v[28:31], a[76:79], v[80:95]\n"
    "ds_read_b64_tr_b16 v[22:23], v1 offset:10752\n"
    "v_add_f32 v232, v232, v206\n"
    "v_add_f32 v232, v232, v207\n"
    "v_add_f32 v233, v208, v209\n"
    "v_add_f32 v233, v233, v210\n"
    "v_mfma_f32_32x32x16_bf16 v[64:79], v[28:31], a[72:75], v[64:79]\n"
    "ds_read_b64_tr_b16 v[24:25], v1 offset:12288\n"
    "v_add_f32 v233, v233, v211\n"
    "v_add_f32 v233, v233, v212\n"
    "v_add_f32 v233, v233, v213\n"
    "v_add_f32 v233, v233, v214\n"
    "s_waitcnt lgkmcnt(3)\n"
    "v_mfma_f32_32x32x16_bf16 v[32:47], v[16:19], a[80:83], v[32:47]\n"
    "ds_read_b64_tr_b16 v[26:27], v1 offset:12800\n"
    "v_add_f32 v233, v233, v215\n"
    "v_max3_f32 v234, v216, v217, v218\n"
    "v_max3_f32 v234, v234, v219, v220\n"
    "v_permlane32_swap_b32 v232, v233\n"
    "v_mfma_f32_32x32x16_bf16 v[48:63], v[16:19], a[84:87], v[48:63]\n"
    "ds_read_b64_tr_b16 v[28:29], v1 offset:14336\n"
    "v_add_f32 v232, v232, v233\n"
    "v_rcp_f32 v232, v232\n"
    "v_max3_f32 v234, v234, v221, v222\n"
    "s_waitcnt lgkmcnt(3)\n"
    "v_mfma_f32_32x32x16_bf16 v[80:95], v[20:23], a[84:87], v[80:95]\n"
    "ds_read_b64_tr_b16 v[30:31], v1 offset:14848\n"
    "v_mov_b32 v233, v232\n"
    "v_max_f32 v234, v234, v223\n"
    "v_max3_f32 v235, v224, v225, v226\n"
    "v_permlane32_swap_b32 v232, v233\n"
    "v_mfma_f32_32x32x16_bf16 v[64:79], v[20:23], a[80:83], v[64:79]\n"
    "s_barrier\n"
    "ds_read_b64_tr_b16 v[16:17], v1 offset:16384\n"
    "s_mul_i32 s74, s77, 0x40000\n"
    "s_add_u32 s74, s74, 0x100000\n"
    "s_add_u32 m0, s81, 0xc000\n"
    "s_nop 0\n"
    "buffer_load_dwordx4 v6, s[52:55], s74 offen lds\n"
    "s_waitcnt lgkmcnt(3)\n"
    "v_mfma_f32_32x32x16_bf16 v[32:47], v[24:27], a[88:91], v[32:47]\n"
    "ds_read_b64_tr_b16 v[18:19], v1 offset:16896\n"
    "buffer_load_dwordx4 v7, s[52:55], s74 offen offset:1024 lds\n"
    "v_mul_f32 v200, v200, v232\n"
    "v_mul_f32 v201, v201, v232\n"
    "v_mul_f32 v202, v202, v232\n"
    "v_mfma_f32_32x32x16_bf16 v[48:63], v[24:27], a[92:95], v[48:63]\n"
    "ds_read_b64_tr_b16 v[20:21], v1 offset:18432\n"
    "s_mul_i32 s74, s77, 0x40000\n"
    "s_add_u32 s74, s74, 0x110000\n"
    "s_add_u32 m0, s81, 0xe000\n"
    "s_nop 0\n"
    "buffer_load_dwordx4 v6, s[52:55], s74 offen lds\n"
    "s_waitcnt lgkmcnt(3)\n"
    "v_mfma_f32_32x32x16_bf16 v[80:95], v[28:31], a[92:95], v[80:95]\n"
    "ds_read_b64_tr_b16 v[22:23], v1 offset:18944\n"
    "buffer_load_dwordx4 v7, s[52:55], s74 offen offset:1024 lds\n"
    "v_mul_f32 v203, v203, v232\n"
    "v_mul_f32 v204, v204, v232\n"
    "v_mul_f32 v205, v205, v232\n"
    "v_mfma_f32_32x32x16_bf16 v[64:79], v[28:31], a[88:91], v[64:79]\n"
    "ds_read_b64_tr_b16 v[24:25], v1 offset:20480\n"
    "v_mul_f32 v206, v206, v232\n"
    "v_mul_f32 v207, v207, v232\n"
    "v_mul_f32 v208, v208, v233\n"
    "v_mul_f32 v209, v209, v233\n"
    "s_waitcnt lgkmcnt(3)\n"
    "v_mfma_f32_32x32x16_bf16 v[32:47], v[16:19], a[96:99], v[32:47]\n"
    "ds_read_b64_tr_b16 v[26:27], v1 offset:20992\n"
    "v_mul_f32 v210, v210, v233\n"
    "v_mul_f32 v211, v211, v233\n"
    "v_mul_f32 v212, v212, v233\n"
    "v_mul_f32 v213, v213, v233\n"
    "v_mfma_f32_32x32x16_bf16 v[48:63], v[16:19], a[100:103], v[48:63]\n"
    "ds_read_b64_tr_b16 v[28:29], v1 offset:22528\n"
    "v_mul_f32 v214, v214, v233\n"
    "v_mul_f32 v215, v215, v233\n"
    "v_cvt_pk_bf16_f32 v128, v200, v201\n"
    "v_cvt_pk_bf16_f32 v129, v202, v203\n"
    "s_waitcnt lgkmcnt(3)\n"
    "v_mfma_f32_32x32x16_bf16 v[80:95], v[20:23], a[100:103], v[80:95]\n"
    "ds_read_b64_tr_b16 v[30:31], v1 offset:23040\n"
    "v_cvt_pk_bf16_f32 v130, v204, v205\n"
    "v_cvt_pk_bf16_f32 v131, v206, v207\n"
    "v_cvt_pk_bf16_f32 v132, v208, v209\n"
    "v_cvt_pk_bf16_f32 v133, v210, v211\n"
    "v_mfma_f32_32x32x16_bf16 v[64:79], v[20:23], a[96:99], v[64:79]\n"
    "ds_read_b64_tr_b16 v[16:17], v1 offset:24576\n"
    "v_cvt_pk_bf16_f32 v134, v212, v213\n"
    "v_cvt_pk_bf16_f32 v135, v214, v215\n"
    "v_max3_f32 v235, v235, v227, v228\n"
    "v_max3_f32 v235, v235, v229, v230\n"
    "s_waitcnt lgkmcnt(3)\n"
    "v_mfma_f32_32x32x16_bf16 v[32:47], v[24:27], a[104:107], v[32:47]\n"
    "ds_read_b64_tr_b16 v[18:19], v1 offset:25088\n"
    "v_max_f32 v235, v235, v231\n"
    "v_mfma_f32_32x32x16_bf16 v[48:63], v[24:27], a[108:111], v[48:63]\n"
    "ds_read_b64_tr_b16 v[20:21], v1 offset:26624\n"
    "v_permlane32_swap_b32 v234, v235\n"
    "v_max_f32 v234, v234, v235\n"
    "v_mul_f32 v234, -v234, v164\n"
    "v_mov_b32 v235, v234\n"
    "s_waitcnt lgkmcnt(3)\n"
    "v_mfma_f32_32x32x16_bf16 v[80:95], v[28:31], a[108:111], v[80:95]\n"
    "ds_read_b64_tr_b16 v[22:23], v1 offset:27136\n"
    "v_permlane32_swap_b32 v234, v235\n"
    "v_fma_f32 v216, v216, v164, v234\n"
    "v_fma_f32 v217, v217, v164, v234\n"
    "v_fma_f32 v218, v218, v164, v234\n"
    "v_mfma_f32_32x32x16_bf16 v[64:79], v[28:31], a[104:107], v[64:79]\n"
    "ds_read_b64_tr_b16 v[24:25], v1 offset:28672\n"
    "v_fma_f32 v219, v219, v164, v234\n"
    "v_fma_f32 v220, v220, v164, v234\n"
    "v_fma_f32 v221, v221, v164, v234\n"
    "v_fma_f32 v222, v222, v164, v234\n"
    "s_waitcnt lgkmcnt(3)\n"
    "v_mfma_f32_32x32x16_bf16 v[32:47], v[16:19], a[112:115], v[32:47]\n"
    "ds_read_b64_tr_b16 v[26:27], v1 offset:29184\n"
    "v_fma_f32 v223, v223, v164, v234\n"
    "v_fma_f32 v224, v224, v164, v235\n"
    "v_fma_f32 v225, v225, v164, v235\n"
    "v_fma_f32 v226, v226, v164, v235\n"
    "v_mfma_f32_32x32x16_bf16 v[48:63], v[16:19], a[116:119], v[48:63]\n"
    "ds_read_b64_tr_b16 v[28:29], v1 offset:30720\n"
    "v_fma_f32 v227, v227, v164, v235\n"
    "v_fma_f32 v228, v228, v164, v235\n"
    "v_fma_f32 v229, v229, v164, v235\n"
    "v_fma_f32 v230, v230, v164, v235\n"
    "s_waitcnt lgkmcnt(3)\n"
    "v_mfma_f32_32x32x16_bf16 v[80:95], v[20:23], a[116:119], v[80:95]\n"
    "ds_read_b64_tr_b16 v[30:31], v1 offset:31232\n"
    "v_fma_f32 v231, v231, v164, v235\n"
    "v_exp_f32 v216, v216\n"
    "v_mfma_f32_32x32x16_bf16 v[64:79], v[20:23], a[112:115], v[64:79]\n"
    "s_barrier\n"
    "ds_read_b64_tr_b16 v[16:17], v1 offset:32768\n"
    "s_mul_i32 s74, s77, 0x40000\n"
    "s_add_u32 s74, s74, 0x120000\n"
    "s_add_u32 m0, s81, 0x10000\n"
    "s_nop 0\n"
    "buffer_load_dwordx4 v6, s[52:55], s74 offen lds\n"
    "s_waitcnt lgkmcnt(3)\n"
    "v_mfma_f32_32x32x16_bf16 v[32:47], v[24:27], a[120:123], v[32:47]\n"
    "ds_read_b64_tr_b16 v[18:19], v1 offset:33280\n"
    "buffer_load_dwordx4 v7, s[52:55], s74 offen offset:1024 lds\n"
    "v_exp_f32 v217, v217\n"
    "v_mfma_f32_32x32x16_bf16 v[48:63], v[24:27], a[124:127], v[48:63]\n"
    "ds_read_b64_tr_b16 v[20:21], v1 offset:34816\n"
    "s_mul_i32 s74, s77, 0x40000\n"
    "s_add_u32 s74, s74, 0x130000\n"
    "s_add_u32 m0, s81, 0x12000\n"
    "s_nop 0\n"
    "buffer_load_dwordx4 v6, s[52:55], s74 offen lds\n"
    "s_waitcnt lgkmcnt(3)\n"
    "v_mfma_f32_32x32x16_bf16 v[80:95], v[28:31], a[124:127], v[80:95]\n"
    "ds_read_b64_tr_b16 v[22:23], v1 offset:35328\n"
    "buffer_load_dwordx4 v7, s[52:55], s74 offen offset:1024 lds\n"
    "v_exp_f32 v218, v218\n"
    "v_add_f32 v234, v216, v217\n"
    "v_mfma_f32_32x32x16_bf16 v[64:79], v[28:31], a[120:123], v[64:79]\n"
    "ds_read_b64_tr_b16 v[24:25], v1 offset:36864\n"
    "v_exp_f32 v219, v219\n"
    "v_exp_f32 v220, v220\n"
    "s_waitcnt lgkmcnt(3)\n"
    "v_mfma_f32_32x32x16_bf16 v[32:47], v[16:19], a[128:131], v[32:47]\n"
    "ds_read_b64_tr_b16 v[26:27], v1 offset:37376\n"
    "v_exp_f32 v221, v221\n"
    "v_exp_f32 v222, v222\n"
    "v_mfma_f32_32x32x16_bf16 v[48:63], v[16:19], a[132:135], v[48:63]\n"
    "ds_read_b64_tr_b16 v[28:29], v1 offset:38912\n"
    "v_exp_f32 v223, v223\n"
    "v_exp_f32 v224, v224\n"
    "s_waitcnt lgkmcnt(3)\n"
    "v_mfma_f32_32x32x16_bf16 v[80:95], v[20:23], a[132:135], v[80:95]\n"
    "ds_read_b64_tr_b16 v[30:31], v1 offset:39424\n"
    "v_exp_f32 v225, v225\n"
    "v_exp_f32 v226, v226\n"
    "v_mfma_f32_32x32x16_bf16 v[200:215], v[192:195], v[112:115], 0\n"
    "v_exp_f32 v227, v227\n"
    "v_exp_f32 v228, v228\n"
    "v_add_f32 v234, v234, v218\n"
    "v_mfma_f32_32x32x16_bf16 v[64:79], v[20:23], a[128:131], v[64:79]\n"
    "ds_read_b64_tr_b16 v[16:17], v1 offset:40960\n"
    "v_exp_f32 v229, v229\n"
    "v_exp_f32 v230, v230\n"
    "s_waitcnt lgkmcnt(3)\n"
    "v_mfma_f32_32x32x16_bf16 v[32:47], v[24:27], a[136:139], v[32:47]\n"
    "ds_read_b64_tr_b16 v[18:19], v1 offset:41472\n"
    "v_exp_f32 v231, v231\n"
    "v_add_f32 v234, v234, v219\n"
    "v_add_f32 v234, v234, v220\n"
    "v_mfma_f32_32x32x16_bf16 v[48:63], v[24:27], a[140:143], v[48:63]\n"
    "ds_read_b64_tr_b16 v[20:21], v1 offset:43008\n"
    "v_add_f32 v234, v234, v221\n"
    "v_add_f32 v234, v234, v222\n"
    "v_add_f32 v234, v234, v223\n"
    "v_add_f32 v235, v224, v225\n"
    "s_waitcnt lgkmcnt(3)\n"
    "v_mfma_f32_32x32x16_bf16 v[80:95], v[28:31], a[140:143], v[80:95]\n"
    "ds_read_b64_tr_b16 v[22:23], v1 offset:43520\n"
    "v_add_f32 v235, v235, v226\n"
    "v_add_f32 v235, v235, v227\n"
    "v_add_f32 v235, v235, v228\n"
    "v_add_f32 v235, v235, v229\n"
    "v_mfma_f32_32x32x16_bf16 v[64:79], v[28:31], a[136:139], v[64:79]\n"
    "ds_read_b64_tr_b16 v[24:25], v1 offset:45056\n"
    "v_add_f32 v235, v235, v230\n"
    "v_add_f32 v235, v235, v231\n"
    "v_max3_f32 v232, v200, v201, v202\n"
    "v_max3_f32 v232, v232, v203, v204\n"
    "s_waitcnt lgkmcnt(3)\n"
    "v_mfma_f32_32x32x16_bf16 v[32:47], v[16:19], a[144:147], v[32:47]\n"
    "ds_read_b64_tr_b16 v[26:27], v1 offset:45568\n"
    "v_permlane32_swap_b32 v234, v235\n"
    "v_add_f32 v234, v234, v235\n"
    "v_rcp_f32 v234, v234\n"
    "v_mfma_f32_32x32x16_bf16 v[48:63], v[16:19], a[148:151], v[48:63]\n"
    "ds_read_b64_tr_b16 v[28:29], v1 offset:47104\n"
    "v_mov_b32 v235, v234\n"
    "v_max3_f32 v232, v232, v205, v206\n"
    "v_max_f32 v232, v232, v207\n"
    "v_permlane32_swap_b32 v234, v235\n"
    "s_waitcnt lgkmcnt(3)\n"
    "v_mfma_f32_32x32x16_bf16 v[80:95], v[20:23], a[148:151], v[80:95]\n"
    "ds_read_b64_tr_b16 v[30:31], v1 offset:47616\n"
    "v_mul_f32 v216, v216, v234\n"
    "v_mul_f32 v217, v217, v234\n"
    "v_mul_f32 v218, v218, v234\n"
    "v_mul_f32 v219, v219, v234\n"
    "v_mfma_f32_32x32x16_bf16 v[64:79], v[20:23], a[144:147], v[64:79]\n"
    "s_barrier\n"
    "ds_read_b64_tr_b16 v[16:17], v1 offset:49152\n"
    "s_mul_i32 s74, s77, 0x80000\n"
    "s_add_u32 s74, s74, 0x10000\n"
    "s_add_u32 m0, s81, 0x14000\n"
    "s_nop 0\n"
    "buffer_load_dwordx4 v6, s[52:55], s74 offen lds\n"
    "s_waitcnt lgkmcnt(3)\n"
    "v_mfma_f32_32x32x16_bf16 v[32:47], v[24:27], a[152:155], v[32:47]\n"
    "ds_read_b64_tr_b16 v[18:19], v1 offset:49664\n"
    "buffer_load_dwordx4 v7, s[52:55], s74 offen offset:1024 lds\n"
    "v_mul_f32 v220, v220, v234\n"
    "v_mul_f32 v221, v221, v234\n"
    "v_mul_f32 v222, v222, v234\n"
    "v_mfma_f32_32x32x16_bf16 v[48:63], v[24:27], a[156:159], v[48:63]\n"
    "ds_read_b64_tr_b16 v[20:21], v1 offset:51200\n"
    "s_mul_i32 s74, s77, 0x80000\n"
    "s_add_u32 s74, s74, 0x30000\n"
    "s_add_u32 m0, s81, 0x16000\n"
    "s_nop 0\n"
    "buffer_load_dwordx4 v6, s[52:55], s74 offen lds\n"
    "s_waitcnt lgkmcnt(3)\n"
    "v_mfma_f32_32x32x16_bf16 v[80:95], v[28:31], a[156:159], v[80:95]\n"
    "ds_read_b64_tr_b16 v[22:23], v1 offset:51712\n"
    "buffer_load_dwordx4 v7, s[52:55], s74 offen offset:1024 lds\n"
    "v_mul_f32 v223, v223, v234\n"
    "v_mul_f32 v224, v224, v235\n"
    "v_mul_f32 v225, v225, v235\n"
    "v_mfma_f32_32x32x16_bf16 v[64:79], v[28:31], a[152:155], v[64:79]\n"
    "ds_read_b64_tr_b16 v[24:25], v1 offset:53248\n"
    "v_mul_f32 v226, v226, v235\n"
    "v_mul_f32 v227, v227, v235\n"
    "v_mul_f32 v228, v228, v235\n"
    "v_mul_f32 v229, v229, v235\n"
    "s_waitcnt lgkmcnt(3)\n"
    "v_mfma_f32_32x32x16_bf16 v[32:47], v[16:19], a[160:163], v[32:47]\n"
    "ds_read_b64_tr_b16 v[26:27], v1 offset:53760\n"
    "v_mul_f32 v230, v230, v235\n"
    "v_mul_f32 v231, v231, v235\n"
    "v_cvt_pk_bf16_f32 v136, v216, v217\n"
    "v_cvt_pk_bf16_f32 v137, v218, v219\n"
    "v_mfma_f32_32x32x16_bf16 v[48:63], v[16:19], a[164:167], v[48:63]\n"
    "ds_read_b64_tr_b16 v[28:29], v1 offset:55296\n"
    "v_cvt_pk_bf16_f32 v138, v220, v221\n"
    "v_cvt_pk_bf16_f32 v139, v222, v223\n"
    "v_cvt_pk_bf16_f32 v140, v224, v225\n"
    "v_cvt_pk_bf16_f32 v141, v226, v227\n"
    "s_waitcnt lgkmcnt(3)\n"
    "v_mfma_f32_32x32x16_bf16 v[80:95], v[20:23], a[164:167], v[80:95]\n"
    "ds_read_b64_tr_b16 v[30:31], v1 offset:55808\n"
    "v_cvt_pk_bf16_f32 v142, v228, v229\n"
    "v_cvt_pk_bf16_f32 v143, v230, v231\n"
    "v_max3_f32 v233, v208, v209, v210\n"
    "v_max3_f32 v233, v233, v211, v212\n"
    "v_mfma_f32_32x32x16_bf16 v[64:79], v[20:23], a[160:163], v[64:79]\n"
    "ds_read_b64_tr_b16 v[16:17], v1 offset:57344\n"
    "v_max3_f32 v233, v233, v213, v214\n"
    "v_max_f32 v233, v233, v215\n"
    "s_mul_i32 s34, s82, 12\n"
    "s_lshl_b32 s35, s76, 1\n"
    "s_waitcnt lgkmcnt(3)\n"
    "v_mfma_f32_32x32x16_bf16 v[32:47], v[24:27], a[168:171], v[32:47]\n"
    "ds_read_b64_tr_b16 v[18:19], v1 offset:57856\n"
    "v_permlane32_swap_b32 v232, v233\n"
    "v_max_f32 v232, v232, v233\n"
    "v_mul_f32 v232, -v232, v165\n"
    "v_mov_b32 v233, v232\n"
    "v_mfma_f32_32x32x16_bf16 v[48:63], v[24:27], a[172:175], v[48:63]\n"
    "ds_read_b64_tr_b16 v[20:21], v1 offset:59392\n"
    "v_permlane32_swap_b32 v232, v233\n"
    "v_fma_f32 v200, v200, v165, v232\n"
    "v_fma_f32 v201, v201, v165, v232\n"
    "v_fma_f32 v202, v202, v165, v232\n"
    "s_waitcnt lgkmcnt(3)\n"
    "v_mfma_f32_32x32x16_bf16 v[80:95], v[28:31], a[172:175], v[80:95]\n"
    "ds_read_b64_tr_b16 v[22:23], v1 offset:59904\n"
    "v_fma_f32 v203, v203, v165, v232\n"
    "v_fma_f32 v204, v204, v165, v232\n"
    "v_fma_f32 v205, v205, v165, v232\n"
    "v_fma_f32 v206, v206, v165, v232\n"
    "v_mfma_f32_32x32x16_bf16 v[64:79], v[28:31], a[168:171], v[64:79]\n"
    "ds_read_b64_tr_b16 v[24:25], v1 offset:61440\n"
    "v_fma_f32 v207, v207, v165, v232\n"
    "v_fma_f32 v208, v208, v165, v233\n"
    "v_fma_f32 v209, v209, v165, v233\n"
    "v_fma_f32 v210, v210, v165, v233\n"
    "s_waitcnt lgkmcnt(3)\n"
    "v_mfma_f32_32x32x16_bf16 v[32:47], v[16:19], a[176:179], v[32:47]\n"
    "ds_read_b64_tr_b16 v[26:27], v1 offset:61952\n"
    "v_fma_f32 v211, v211, v165, v233\n"
    "v_fma_f32 v212, v212, v165, v233\n"
    "v_fma_f32 v213, v213, v165, v233\n"
    "v_fma_f32 v214, v214, v165, v233\n"
    "v_mfma_f32_32x32x16_bf16 v[48:63], v[16:19], a[180:183], v[48:63]\n"
    "ds_read_b64_tr_b16 v[28:29], v1 offset:63488\n"
    "v_fma_f32 v215, v215, v165, v233\n"
    "v_exp_f32 v200, v200\n"
    "s_add_u32 s34, s34, s35\n"
    "s_waitcnt lgkmcnt(3)\n"
    "v_mfma_f32_32x32x16_bf16 v[80:95], v[20:23], a[180:183], v[80:95]\n"
    "ds_read_b64_tr_b16 v[30:31], v1 offset:64000\n"
    "v_exp_f32 v201, v201\n"
    "v_exp_f32 v202, v202\n"
    "v_mfma_f32_32x32x16_bf16 v[64:79], v[20:23], a[176:179], v[64:79]\n"
    "s_waitcnt vmcnt(25)\n"
    "s_barrier\n"
    "v_add_u32 v1, 0x8000, v1\n"
    "ds_read_b64_tr_b16 v[16:17], v1 offset:32768\n"
    "s_mul_i32 s74, s77, 0x80000\n"
    "s_add_u32 s74, s74, 0x50000\n"
    "s_add_u32 m0, s81, 0x18000\n"
    "s_nop 0\n"
    "buffer_load_dwordx4 v6, s[52:55], s74 offen lds\n"
    "s_waitcnt lgkmcnt(3)\n"
    "v_mfma_f32_32x32x16_bf16 v[32:47], v[24:27], a[184:187], v[32:47]\n"
    "ds_read_b64_tr_b16 v[18:19], v1 offset:33280\n"
    "buffer_load_dwordx4 v7, s[52:55], s74 offen offset:1024 lds\n"
    "v_exp_f32 v203, v203\n"
    "v_add_f32 v232, v200, v201\n"
    "v_mfma_f32_32x32x16_bf16 v[48:63], v[24:27], a[188:191], v[48:63]\n"
    "ds_read_b64_tr_b16 v[20:21], v1 offset:34816\n"
    "s_mul_i32 s74, s77, 0x80000\n"
    "s_add_u32 s74, s74, 0x70000\n"
    "s_add_u32 m0, s81, 0x1a000\n"
    "s_nop 0\n"
    "buffer_load_dwordx4 v6, s[52:55], s74 offen lds\n"
    "s_waitcnt lgkmcnt(3)\n"
    "v_mfma_f32_32x32x16_bf16 v[80:95], v[28:31], a[188:191], v[80:95]\n"
    "ds_read_b64_tr_b16 v[22:23], v1 offset:35328\n"
    "buffer_load_dwordx4 v7, s[52:55], s74 offen offset:1024 lds\n"
    "v_exp_f32 v204, v204\n"
    "v_add_f32 v232, v232, v202\n"
    "v_mfma_f32_32x32x16_bf16 v[64:79], v[28:31], a[184:187], v[64:79]\n"
    "ds_read_b64_tr_b16 v[24:25], v1 offset:36864\n"
    "v_exp_f32 v205, v205\n"
    "v_exp_f32 v206, v206\n"
    "s_waitcnt lgkmcnt(3)\n"
    "v_mfma_f32_32x32x16_bf16 v[32:47], v[16:19], a[192:195], v[32:47]\n"
    "ds_read_b64_tr_b16 v[26:27], v1 offset:37376\n"
    "v_exp_f32 v207, v207\n"
    "v_exp_f32 v208, v208\n"
    "v_mfma_f32_32x32x16_bf16 v[48:63], v[16:19], a[196:199], v[48:63]\n"
    "ds_read_b64_tr_b16 v[28:29], v1 offset:38912\n"
    "v_exp_f32 v209, v209\n"
    "v_exp_f32 v210, v210\n"
    "v_mfma_f32_32x32x16_bf16 v[216:231], v[196:199], v[116:119], 0\n"
    "v_exp_f32 v211, v211\n"
    "v_exp_f32 v212, v212\n"
    "v_add_f32 v232, v232, v203\n"
    "s_waitcnt lgkmcnt(3)\n"
    "v_mfma_f32_32x32x16_bf16 v[80:95], v[20:23], a[196:199], v[80:95]\n"
    "ds_read_b64_tr_b16 v[30:31], v1 offset:39424\n"
    "v_exp_f32 v213, v213\n"
    "v_exp_f32 v214, v214\n"
    "v_mfma_f32_32x32x16_bf16 v[64:79], v[20:23], a[192:195], v[64:79]\n"
    "ds_read_b64_tr_b16 v[16:17], v1 offset:40960\n"
    "v_exp_f32 v215, v215\n"
    "v_add_f32 v232, v232, v204\n"
    "v_add_f32 v232, v232, v205\n"
    "s_waitcnt lgkmcnt(3)\n"
    "v_mfma_f32_32x32x16_bf16 v[32:47], v[24:27], a[200:203], v[32:47]\n"
    "ds_read_b64_tr_b16 v[18:19], v1 offset:41472\n"
    "v_add_f32 v232, v232, v206\n"
    "v_add_f32 v232, v232, v207\n"
    "v_add_f32 v233, v208, v209\n"
    "v_add_f32 v233, v233, v210\n"
    "v_mfma_f32_32x32x16_bf16 v[48:63], v[24:27], a[204:207], v[48:63]\n"
    "ds_read_b64_tr_b16 v[20:21], v1 offset:43008\n"
    "v_add_f32 v233, v233, v211\n"
    "v_add_f32 v233, v233, v212\n"
    "v_add_f32 v233, v233, v213\n"
    "v_add_f32 v233, v233, v214\n"
    "s_waitcnt lgkmcnt(3)\n"
    "v_mfma_f32_32x32x16_bf16 v[80:95], v[28:31], a[204:207], v[80:95]\n"
    "ds_read_b64_tr_b16 v[22:23], v1 offset:43520\n"
    "v_add_f32 v233, v233, v215\n"
    "v_max3_f32 v234, v216, v217, v218\n"
    "v_max3_f32 v234, v234, v219, v220\n"
    "v_permlane32_swap_b32 v232, v233\n"
    "v_mfma_f32_32x32x16_bf16 v[64:79], v[28:31], a[200:203], v[64:79]\n"
    "ds_read_b64_tr_b16 v[24:25], v1 offset:45056\n"
    "v_add_f32 v232, v232, v233\n"
    "v_rcp_f32 v232, v232\n"
    "v_max3_f32 v234, v234, v221, v222\n"
    "s_waitcnt lgkmcnt(3)\n"
    "v_mfma_f32_32x32x16_bf16 v[32:47], v[16:19], a[208:211], v[32:47]\n"
    "ds_read_b64_tr_b16 v[26:27], v1 offset:45568\n"
    "v_mov_b32 v233, v232\n"
    "v_max_f32 v234, v234, v223\n"
    "v_max3_f32 v235, v224, v225, v226\n"
    "v_permlane32_swap_b32 v232, v233\n"
    "v_mfma_f32_32x32x16_bf16 v[48:63], v[16:19], a[212:215], v[48:63]\n"
    "ds_read_b64_tr_b16 v[28:29], v1 offset:47104\n"
    "v_mul_f32 v200, v200, v232\n"
    "v_mul_f32 v201, v201, v232\n"
    "v_mul_f32 v202, v202, v232\n"
    "v_mul_f32 v203, v203, v232\n"
    "s_waitcnt lgkmcnt(3)\n"
    "v_mfma_f32_32x32x16_bf16 v[80:95], v[20:23], a[212:215], v[80:95]\n"
    "ds_read_b64_tr_b16 v[30:31], v1 offset:47616\n"
    "v_mul_f32 v204, v204, v232\n"
    "v_mul_f32 v205, v205, v232\n"
    "v_mul_f32 v206, v206, v232\n"
    "v_mul_f32 v207, v207, v232\n"
    "v_mfma_f32_32x32x16_bf16 v[64:79], v[20:23], a[208:211], v[64:79]\n"
    "s_waitcnt vmcnt(25)\n"
    "s_barrier\n"
    "v_add_u32 v1, 0xfffe8000, v1\n"
    "ds_read_b64_tr_b16 v[16:17], v1\n"
    "s_mul_i32 s74, s77, 0x20000\n"
    "s_add_u32 s74, s74, 0x180000\n"
    "s_add_u32 m0, s81, 0x1c000\n"
    "s_nop 0\n"
    "buffer_load_dwordx4 v6, s[52:55], s74 offen lds\n"
    "s_waitcnt lgkmcnt(3)\n"
    "v_mfma_f32_32x32x16_bf16 v[32:47], v[24:27], a[216:219], v[32:47]\n"
    "ds_read_b64_tr_b16 v[18:19], v1 offset:512\n"
    "buffer_load_dwordx4 v7, s[52:55], s74 offen offset:1024 lds\n"
    "v_mul_f32 v208, v208, v233\n"
    "v_mul_f32 v209, v209, v233\n"
    "v_mul_f32 v210, v210, v233\n"
    "v_mfma_f32_32x32x16_bf16 v[48:63], v[24:27], a[220:223], v[48:63]\n"
    "ds_read_b64_tr_b16 v[20:21], v1 offset:2048\n"
    "s_mul_i32 s74, s77, 0x20000\n"
    "s_add_u32 s74, s74, 0x190000\n"
    "s_add_u32 m0, s81, 0x1e000\n"
    "s_nop 0\n"
    "buffer_load_dwordx4 v6, s[52:55], s74 offen lds\n"
    "s_waitcnt lgkmcnt(3)\n"
    "v_mfma_f32_32x32x16_bf16 v[80:95], v[28:31], a[220:223], v[80:95]\n"
    "ds_read_b64_tr_b16 v[22:23], v1 offset:2560\n"
    "buffer_load_dwordx4 v7, s[52:55], s74 offen offset:1024 lds\n"
    "v_mul_f32 v211, v211, v233\n"
    "v_mul_f32 v212, v212, v233\n"
    "v_mul_f32 v213, v213, v233\n"
    "v_mfma_f32_32x32x16_bf16 v[64:79], v[28:31], a[216:219], v[64:79]\n"
    "ds_read_b64_tr_b16 v[24:25], v1 offset:4096\n"
    "v_mul_f32 v214, v214, v233\n"
    "v_mul_f32 v215, v215, v233\n"
    "v_cvt_pk_bf16_f32 v144, v200, v201\n"
    "v_cvt_pk_bf16_f32 v145, v202, v203\n"
    "s_waitcnt lgkmcnt(3)\n"
    "v_mfma_f32_32x32x16_bf16 v[32:47], v[16:19], a[224:227], v[32:47]\n"
    "ds_read_b64_tr_b16 v[26:27], v1 offset:4608\n"
    "v_cvt_pk_bf16_f32 v146, v204, v205\n"
    "v_cvt_pk_bf16_f32 v147, v206, v207\n"
    "v_cvt_pk_bf16_f32 v148, v208, v209\n"
    "v_cvt_pk_bf16_f32 v149, v210, v211\n"
    "v_mfma_f32_32x32x16_bf16 v[48:63], v[16:19], a[228:231], v[48:63]\n"
    "ds_read_b64_tr_b16 v[28:29], v1 offset:6144\n"
    "v_cvt_pk_bf16_f32 v150, v212, v213\n"
    "v_cvt_pk_bf16_f32 v151, v214, v215\n"
    "v_max3_f32 v235, v235, v227, v228\n"
    "v_max3_f32 v235, v235, v229, v230\n"
    "s_waitcnt lgkmcnt(3)\n"
    "v_mfma_f32_32x32x16_bf16 v[80:95], v[20:23], a[228:231], v[80:95]\n"
    "ds_read_b64_tr_b16 v[30:31], v1 offset:6656\n"
    "v_max_f32 v235, v235, v231\n"
    "s_add_u32 s34, s34, s75\n"
    "s_add_u32 s34, s34, 8\n"
    "v_permlane32_swap_b32 v234, v235\n"
    "v_mfma_f32_32x32x16_bf16 v[64:79], v[20:23], a[224:227], v[64:79]\n"
    "ds_read_b64_tr_b16 v[16:17], v1 offset:8192\n"
    "v_max_f32 v234, v234, v235\n"
    "v_mul_f32 v234, -v234, v165\n"
    "v_mov_b32 v235, v234\n"
    "s_lshl_b32 s34, s34, 12\n"
    "s_waitcnt lgkmcnt(3)\n"
    "v_mfma_f32_32x32x16_bf16 v[32:47], v[24:27], a[232:235], v[32:47]\n"
    "ds_read_b64_tr_b16 v[18:19], v1 offset:8704\n"
    "v_permlane32_swap_b32 v234, v235\n"
    "v_fma_f32 v216, v216, v165, v234\n"
    "v_fma_f32 v217, v217, v165, v234\n"
    "v_fma_f32 v218, v218, v165, v234\n"
    "v_mfma_f32_32x32x16_bf16 v[48:63], v[24:27], a[236:239], v[48:63]\n"
    "ds_read_b64_tr_b16 v[20:21], v1 offset:10240\n"
    "v_fma_f32 v219, v219, v165, v234\n"
    "v_fma_f32 v220, v220, v165, v234\n"
    "v_fma_f32 v221, v221, v165, v234\n"
    "v_fma_f32 v222, v222, v165, v234\n"
    "s_waitcnt lgkmcnt(3)\n"
    "v_mfma_f32_32x32x16_bf16 v[80:95], v[28:31], a[236:239], v[80:95]\n"
    "ds_read_b64_tr_b16 v[22:23], v1 offset:10752\n"
    "v_fma_f32 v223, v223, v165, v234\n"
    "v_fma_f32 v224, v224, v165, v235\n"
    "v_fma_f32 v225, v225, v165, v235\n"
    "v_fma_f32 v226, v226, v165, v235\n"
    "v_mfma_f32_32x32x16_bf16 v[64:79], v[28:31], a[232:235], v[64:79]\n"
    "ds_read_b64_tr_b16 v[24:25], v1 offset:12288\n"
    "v_fma_f32 v227, v227, v165, v235\n"
    "v_fma_f32 v228, v228, v165, v235\n"
    "v_fma_f32 v229, v229, v165, v235\n"
    "v_fma_f32 v230, v230, v165, v235\n"
    "s_waitcnt lgkmcnt(3)\n"
    "v_mfma_f32_32x32x16_bf16 v[32:47], v[16:19], a[240:243], v[32:47]\n"
    "ds_read_b64_tr_b16 v[26:27], v1 offset:12800\n"
    "v_fma_f32 v231, v231, v165, v235\n"
    "v_exp_f32 v216, v216\n"
    "s_add_u32 s34, s34, 0x990000\n"
    "v_mfma_f32_32x32x16_bf16 v[48:63], v[16:19], a[244:247], v[48:63]\n"
    "ds_read_b64_tr_b16 v[28:29], v1 offset:14336\n"
    "v_exp_f32 v217, v217\n"
    "v_exp_f32 v218, v218\n"
    "s_waitcnt lgkmcnt(3)\n"
    "v_mfma_f32_32x32x16_bf16 v[80:95], v[20:23], a[244:247], v[80:95]\n"
    "ds_read_b64_tr_b16 v[30:31], v1 offset:14848\n"
    "v_exp_f32 v219, v219\n"
    "v_exp_f32 v220, v220\n"
    "v_mfma_f32_32x32x16_bf16 v[64:79], v[20:23], a[240:243], v[64:79]\n"
    "v_exp_f32 v221, v221\n"
    "v_exp_f32 v222, v222\n"
    "v_add_f32 v234, v216, v217\n"
    "s_waitcnt lgkmcnt(2)\n"
    "v_mfma_f32_32x32x16_bf16 v[32:47], v[24:27], a[248:251], v[32:47]\n"
    "v_exp_f32 v223, v223\n"
    "v_exp_f32 v224, v224\n"
    "v_add_f32 v234, v234, v218\n"
    "v_mfma_f32_32x32x16_bf16 v[48:63], v[24:27], a[252:255], v[48:63]\n"
    "v_exp_f32 v225, v225\n"
    "v_exp_f32 v226, v226\n"
    "v_add_f32 v234, v234, v219\n"
    "s_waitcnt lgkmcnt(0)\n"
    "v_mfma_f32_32x32x16_bf16 v[80:95], v[28:31], a[252:255], v[80:95]\n"
    "v_exp_f32 v227, v227\n"
    "v_exp_f32 v228, v228\n"
    "v_add_f32 v234, v234, v220\n"
    "v_mfma_f32_32x32x16_bf16 v[64:79], v[28:31], a[248:251], v[64:79]\n"
    "v_exp_f32 v229, v229\n"
    "v_exp_f32 v230, v230\n"
    "v_exp_f32 v231, v231\n"
    "v_add_f32 v234, v234, v221\n"
    "v_add_f32 v234, v234, v222\n"
    "v_add_f32 v234, v234, v223\n"
    "v_add_f32 v235, v224, v225\n"
    "v_add_f32 v235, v235, v226\n"
    "v_add_f32 v235, v235, v227\n"
    "v_add_f32 v235, v235, v228\n"
    "v_add_f32 v235, v235, v229\n"
    "v_add_f32 v235, v235, v230\n"
    "v_add_f32 v235, v235, v231\n"
    "s_nop 1\n"
    "v_permlane32_swap_b32 v234, v235\n"
    "v_add_f32 v234, v234, v235\n"
    "v_rcp_f32 v234, v234\n"
    "s_nop 0\n"
    "v_mov_b32 v235, v234\n"
    "s_nop 1\n"
    "v_permlane32_swap_b32 v234, v235\n"
    "v_mul_f32 v216, v216, v234\n"
    "v_mul_f32 v217, v217, v234\n"
    "v_mul_f32 v218, v218, v234\n"
    "v_mul_f32 v219, v219, v234\n"
    "v_mul_f32 v220, v220, v234\n"
    "v_mul_f32 v221, v221, v234\n"
    "v_mul_f32 v222, v222, v234\n"
    "v_mul_f32 v223, v223, v234\n"
    "v_mul_f32 v224, v224, v235\n"
    "v_mul_f32 v225, v225, v235\n"
    "v_mul_f32 v226, v226, v235\n"
    "v_mul_f32 v227, v227, v235\n"
    "v_mul_f32 v228, v228, v235\n"
    "v_mul_f32 v229, v229, v235\n"
    "v_mul_f32 v230, v230, v235\n"
    "v_mul_f32 v231, v231, v235\n"
    "v_cvt_pk_bf16_f32 v152, v216, v217\n"
    "v_cvt_pk_bf16_f32 v153, v218, v219\n"
    "v_cvt_pk_bf16_f32 v154, v220, v221\n"
    "v_cvt_pk_bf16_f32 v155, v222, v223\n"
    "v_cvt_pk_bf16_f32 v156, v224, v225\n"
    "v_cvt_pk_bf16_f32 v157, v226, v227\n"
    "v_cvt_pk_bf16_f32 v158, v228, v229\n"
    "v_cvt_pk_bf16_f32 v159, v230, v231\n"
    "v_and_b32 v184, 31, v0\n"
    "v_lshlrev_b32 v184, 3, v184\n"
    "v_lshrrev_b32 v185, 5, v0\n"
    "v_lshl_add_u32 v15, v185, 11, v184\n"
    "buffer_load_dwordx2 v[168:169], v15, s[48:51], s34 offen sc1\n"
    "buffer_load_dwordx2 v[170:171], v15, s[48:51], s34 offen offset:256 sc1\n"
    "buffer_load_dwordx2 v[172:173], v15, s[48:51], s34 offen offset:512 sc1\n"
    "buffer_load_dwordx2 v[174:175], v15, s[48:51], s34 offen offset:768 sc1\n"
    "buffer_load_dwordx2 v[176:177], v15, s[48:51], s34 offen offset:1024 sc1\n"
    "buffer_load_dwordx2 v[178:179], v15, s[48:51], s34 offen offset:1280 sc1\n"
    "buffer_load_dwordx2 v[180:181], v15, s[48:51], s34 offen offset:1536 sc1\n"
    "buffer_load_dwordx2 v[182:183], v15, s[48:51], s34 offen offset:1792 sc1\n"
    "s_waitcnt vmcnt(7)\n"
    "v_xor_b32 v185, s72, v169\n"
    "s_waitcnt vmcnt(6)\n"
    "v_bitop3_b32 v185, v185, v171, s72 bitop3:0xf6\n"
    "s_waitcnt vmcnt(5)\n"
    "v_bitop3_b32 v185, v185, v173, s72 bitop3:0xf6\n"
    "s_waitcnt vmcnt(4)\n"
    "v_bitop3_b32 v185, v185, v175, s72 bitop3:0xf6\n"
    "s_waitcnt vmcnt(3)\n"
    "v_bitop3_b32 v185, v185, v177, s72 bitop3:0xf6\n"
    "s_waitcnt vmcnt(2)\n"
    "v_bitop3_b32 v185, v185, v179, s72 bitop3:0xf6\n"
    "s_waitcnt vmcnt(1)\n"
    "v_bitop3_b32 v185, v185, v181, s72 bitop3:0xf6\n"
    "s_waitcnt vmcnt(0)\n"
    "v_bitop3_b32 v185, v185, v183, s72 bitop3:0xf6\n"
    "v_cmp_ne_u32 s[88:89], 0, v185\n"
    "s_cmp_lg_u64 s[88:89], 0\n"
    "s_cbranch_scc1 LA_slow_7%=\n"
    "LA_back_8%=:\n"
    "v_add_f32 v168, v168, v170\n"
    "v_add_f32 v172, v172, v174\n"
    "v_add_f32 v176, v176, v178\n"
    "v_add_f32 v180, v180, v182\n"
    "v_add_f32 v168, v168, v172\n"
    "v_add_f32 v176, v176, v180\n"
    "v_add_f32 v168, v168, v176\n"
    "v_cvt_pk_bf16_f32 v185, v168, v168\n"
    "v_and_b32 v185, 0xffff0000, v185\n"
    "v_sub_f32 v185, v168, v185\n"
    "v_cvt_pk_bf16_f32 v185, v168, v185\n"
    "s_lshl_b32 s35, s76, 9\n"
    "s_add_u32 s35, s35, 0x27000\n"
    "v_lshl_add_u32 v184, v0, 2, s35\n"
    "s_lshl_b32 s35, s75, 8\n"
    "v_add_u32 v15, s35, v184\n"
    "ds_write_b32 v15, v185\n"
    "s_waitcnt lgkmcnt(0)\n"
    "s_barrier\n"
    "ds_read_b32 v188, v184\n"
    "ds_read_b32 v190, v184 offset:256\n"
    "v_mov_b32 v189, 0\n"
    "v_mov_b32 v191, 0\n"
    "s_mul_i32 s10, s71, 0x1000\n"
    "s_lshl_b32 s11, s76, 10\n"
    "s_add_u32 s10, s10, s11\n"
    "s_add_u32 s10, s10, s83\n"
    "s_add_u32 s10, s10, 0x800\n"
    "s_add_u32 s11, s10, 0x24000\n"
    "s_waitcnt lgkmcnt(1)\n"
    "v_mfma_f32_32x32x8_bf16 v[32:47], v[188:189], v[160:161], v[32:47]\n"
    "v_mfma_f32_32x32x8_bf16 v[48:63], v[188:189], v[162:163], v[48:63]\n"
    "s_waitcnt lgkmcnt(0)\n"
    "v_mfma_f32_32x32x8_bf16 v[64:79], v[190:191], v[160:161], v[64:79]\n"
    "v_mfma_f32_32x32x8_bf16 v[80:95], v[190:191], v[162:163], v[80:95]\n"
    "s_nop 10\n"
    "v_mul_f32 v32, v32, v64\n"
    "v_mul_f32 v33, v33, v65\n"
    "v_mul_f32 v34, v34, v66\n"
    "v_mul_f32 v35, v35, v67\n"
    "v_mul_f32 v36, v36, v68\n"
    "v_bfe_u32 v236, v0, 2, 2\n"
    "v_and_b32 v237, 3, v0\n"
    "v_bfe_u32 v238, v0, 4, 1\n"
    "v_lshrrev_b32 v239, 5, v0\n"
    "v_lshl_add_u32 v240, v238, 2, v236\n"
    "v_lshl_add_u32 v240, v239, 4, v240\n"
    "v_mul_f32 v37, v37, v69\n"
    "v_mul_f32 v38, v38, v70\n"
    "v_mul_f32 v39, v39, v71\n"
    "v_mul_f32 v40, v40, v72\n"
    "v_mul_f32 v41, v41, v73\n"
    "v_mul_f32 v42, v42, v74\n"
    "v_mul_f32 v43, v43, v75\n"
    "v_mul_f32 v44, v44, v76\n"
    "v_mul_f32 v45, v45, v77\n"
    "v_mul_f32 v46, v46, v78\n"
    "v_mul_f32 v47, v47, v79\n"
    "v_cvt_pk_bf16_f32 v64, v32, v33\n"
    "v_cvt_pk_bf16_f32 v65, v34, v35\n"
    "v_cvt_pk_bf16_f32 v66, v36, v37\n"
    "v_cvt_pk_bf16_f32 v67, v38, v39\n"
    "buffer_store_dwordx4 v[64:67], v14, s[48:51], s10 offen sc1\n"
    "v_cvt_pk_bf16_f32 v68, v40, v41\n"
    "v_cvt_pk_bf16_f32 v69, v42, v43\n"
    "v_cvt_pk_bf16_f32 v70, v44, v45\n"
    "v_cvt_pk_bf16_f32 v71, v46, v47\n"
    "buffer_store_dwordx4 v[68:71], v14, s[48:51], s10 offen offset:512 sc1\n"
    "v_mul_f32 v48, v48, v80\n"
    "v_mul_f32 v49, v49, v81\n"
    "v_mul_f32 v50, v50, v82\n"
    "v_mul_f32 v51, v51, v83\n"
    "v_mul_f32 v52, v52, v84\n"
    "v_mul_f32 v53, v53, v85\n"
    "v_mul_f32 v54, v54, v86\n"
    "v_mul_f32 v55, v55, v87\n"
    "v_mul_f32 v56, v56, v88\n"
    "v_mul_f32 v57, v57, v89\n"
    "v_mul_f32 v58, v58, v90\n"
    "v_mul_f32 v59, v59, v91\n"
    "v_mul_f32 v60, v60, v92\n"
    "v_mul_f32 v61, v61, v93\n"
    "v_mul_f32 v62, v62, v94\n"
    "v_mul_f32 v63, v63, v95\n"
    "v_cvt_pk_bf16_f32 v80, v48, v49\n"
    "v_cvt_pk_bf16_f32 v81, v50, v51\n"
    "v_cvt_pk_bf16_f32 v82, v52, v53\n"
    "v_cvt_pk_bf16_f32 v83, v54, v55\n"
    "buffer_store_dwordx4 v[80:83], v14, s[48:51], s11 offen sc1\n"
    "v_cvt_pk_bf16_f32 v84, v56, v57\n"
    "v_cvt_pk_bf16_f32 v85, v58, v59\n"
    "v_cvt_pk_bf16_f32 v86, v60, v61\n"
    "v_cvt_pk_bf16_f32 v87, v62, v63\n"
    "buffer_store_dwordx4 v[84:87], v14, s[48:51], s11 offen offset:512 sc1\n"
    "v_lshlrev_b32 v240, 7, v240\n"
    "v_lshl_add_u32 v240, v237, 3, v240\n"
    "v_lshrrev_b32 v236, 1, v236\n"
    "v_lshl_add_u32 v241, v238, 1, v236\n"
    "v_xor_b32 v237, 0, v241\n"
    "v_lshl_add_u32 v2, v237, 5, v240\n"
    "v_xor_b32 v237, 1, v241\n"
    "v_lshl_add_u32 v3, v237, 5, v240\n"
    "v_xor_b32 v237, 2, v241\n"
    "v_lshl_add_u32 v4, v237, 5, v240\n"
    "v_xor_b32 v237, 3, v241\n"
    "v_lshl_add_u32 v5, v237, 5, v240\n"
    "v_lshlrev_b32 v8, 4, v0\n"
    "v_lshlrev_b32 v246, 3, v0\n"
    "s_xor_b32 s85, s77, s75\n"
    "s_lshl_b32 s86, s78, 1\n"
    "s_add_u32 s87, s86, s85\n"
    "s_lshl_b32 s8, s87, 9\n"
    "s_add_u32 s8, s8, 0xa54000\n"
    "s_lshl_b32 s87, s87, 1\n"
    "s_add_u32 s87, s87, s76\n"
    "s_mul_i32 s98, s87, 0x24000\n"
    "s_add_u32 s87, s86, s77\n"
    "s_lshl_b32 s9, s87, 9\n"
    "s_add_u32 s9, s9, 0xa54000\n"
    "s_lshl_b32 s87, s87, 1\n"
    "s_add_u32 s87, s87, s76\n"
    "s_mul_i32 s99, s87, 0x24000\n"
    "s_lshl_b32 s85, s75, 13\n"
    "s_add_u32 s99, s99, s85\n"
    "s_add_u32 s99, s99, 0xe000\n"
    "buffer_load_dwordx2 v[244:245], v246, s[48:51], s8 offen sc1\n"
    "v_mov_b32 v249, 0\n"
    "v_mov_b32 v250, s72\n"
    "s_waitcnt vmcnt(1)\n"
    "buffer_store_dword v250, v249, s[48:51], s84 offen offset:16 sc1\n"
    "s_lshl_b32 s85, s71, 2\n"
    "s_lshl_b32 s86, s76, 1\n"
    "s_add_u32 s85, s85, s86\n"
    "s_add_u32 s85, s85, s75\n"
    "s_lshl_b32 s86, s85, 6\n"
    "s_add_u32 s86, s86, 0xa58000\n"
    "v_and_b32 v248, 15, v0\n"
    "v_lshlrev_b32 v248, 2, v248\n"
    "buffer_load_dword v247, v248, s[48:51], s86 offen sc1\n"
    "s_waitcnt vmcnt(0)\n"
    "v_cmp_ne_u32 s[88:89], s72, v247\n"
    "s_cmp_lg_u64 s[88:89], 0\n"
    "s_cbranch_scc1 LA_slow_9%=\n"
    "LA_back_10%=:\n"
    "s_mul_i32 s85, s85, 0x4800\n"
    "s_add_u32 s85, s85, 0x900000\n"
    "v_lshrrev_b32 v248, 5, v0\n"
    "v_mul_u32_u24 v248, 0x480, v248\n"
    "v_bfe_u32 v242, v0, 4, 1\n"
    "v_lshl_add_u32 v248, v242, 4, v248\n"
    "v_and_b32 v242, 15, v0\n"
    "v_lshl_add_u32 v242, v242, 5, v248\n"
    "v_and_b32 v243, 1, v0\n"
    "v_lshl_add_u32 v243, v243, 5, v248\n"
    "buffer_load_dwordx4 a[160:163], v242, s[48:51], s85 offen sc1\n"
    "buffer_load_dwordx4 a[164:167], v242, s[48:51], s85 offen offset:512 sc1\n"
    "buffer_load_dwordx4 a[168:171], v243, s[48:51], s85 offen offset:1024 sc1\n"
    "s_add_u32 s85, s85, 0x900\n"
    "buffer_load_dwordx4 a[172:175], v242, s[48:51], s85 offen sc1\n"
    "buffer_load_dwordx4 a[176:179], v242, s[48:51], s85 offen offset:512 sc1\n"
    "buffer_load_dwordx4 a[180:183], v243, s[48:51], s85 offen offset:1024 sc1\n"
    "s_add_u32 s85, s85, 0x900\n"
    "buffer_load_dwordx4 a[184:187], v242, s[48:51], s85 offen sc1\n"
    "buffer_load_dwordx4 a[188:191], v242, s[48:51], s85 offen offset:512 sc1\n"
    "buffer_load_dwordx4 a[192:195], v243, s[48:51], s85 offen offset:1024 sc1\n"
    "s_add_u32 s85, s85, 0x900\n"
    "buffer_load_dwordx4 a[196:199], v242, s[48:51], s85 offen sc1\n"
    "buffer_load_dwordx4 a[200:203], v242, s[48:51], s85 offen offset:512 sc1\n"
    "buffer_load_dwordx4 a[204:207], v243, s[48:51], s85 offen offset:1024 sc1\n"
    "s_add_u32 s85, s85, 0x900\n"
    "buffer_load_dwordx4 a[208:211], v242, s[48:51], s85 offen sc1\n"
    "buffer_load_dwordx4 a[212:215], v242, s[48:51], s85 offen offset:512 sc1\n"
    "buffer_load_dwordx4 a[216:219], v243, s[48:51], s85 offen offset:1024 sc1\n"
    "s_add_u32 s85, s85, 0x900\n"
    "buffer_load_dwordx4 a[220:223], v242, s[48:51], s85 offen sc1\n"
    "buffer_load_dwordx4 a[224:227], v242, s[48:51], s85 offen offset:512 sc1\n"
    "buffer_load_dwordx4 a[228:231], v243, s[48:51], s85 offen offset:1024 sc1\n"
    "s_add_u32 s85, s85, 0x900\n"
    "buffer_load_dwordx4 a[232:235], v242, s[48:51], s85 offen sc1\n"
    "buffer_load_dwordx4 a[236:239], v242, s[48:51], s85 offen offset:512 sc1\n"
    "buffer_load_dwordx4 a[240:243], v243, s[48:51], s85 offen offset:1024 sc1\n"
    "s_add_u32 s85, s85, 0x900\n"
    "buffer_load_dwordx4 a[244:247], v242, s[48:51], s85 offen sc1\n"
    "buffer_load_dwordx4 a[248:251], v242, s[48:51], s85 offen offset:512 sc1\n"
    "buffer_load_dwordx4 a[252:255], v243, s[48:51], s85 offen offset:1024 sc1\n"
    "s_lshl_b32 s30, s77, 4\n"
    "s_lshl_b32 s94, 0x1111, s30\n"
    "s_mov_b32 s95, 0\n"
    "v_cmp_ne_u32 s[88:89], s72, v244\n"
    "v_cmp_ne_u32 s[90:91], s72, v245\n"
    "s_or_b64 s[88:89], s[88:89], s[90:91]\n"
    "s_and_b64 s[88:89], s[88:89], s[94:95]\n"
    "s_cbranch_scc1 LA_slow_11%=\n"
    "LA_back_12%=:\n"
    "s_mul_i32 s30, s77, 0x4000\n"
    "s_add_u32 s30, s30, s98\n"
    "s_add_u32 s30, s30, 0\n"
    "s_add_u32 s31, s30, 0x12000\n"
    "buffer_load_dwordx4 a[0:3], v8, s[48:51], s30 offen sc1\n"
    "buffer_load_dwordx4 a[4:7], v8, s[48:51], s31 offen sc1\n"
    "buffer_load_dwordx4 a[8:11], v8, s[48:51], s30 offen offset:1024 sc1\n"
    "buffer_load_dwordx4 a[12:15], v8, s[48:51], s31 offen offset:1024 sc1\n"
    "s_mul_i32 s30, s77, 0x4000\n"
    "s_add_u32 s30, s30, s98\n"
    "s_add_u32 s30, s30, 0x1000\n"
    "s_add_u32 s31, s30, 0x12000\n"
    "buffer_load_dwordx4 a[16:19], v8, s[48:51], s30 offen sc1\n"
    "buffer_load_dwordx4 a[20:23], v8, s[48:51], s31 offen sc1\n"
    "buffer_load_dwordx4 a[24:27], v8, s[48:51], s30 offen offset:1024 sc1\n"
    "buffer_load_dwordx4 a[28:31], v8, s[48:51], s31 offen offset:1024 sc1\n"
    "s_mul_i32 s30, s77, 0x4000\n"
    "s_add_u32 s30, s30, s98\n"
    "s_add_u32 s30, s30, 0x2000\n"
    "s_add_u32 s31, s30, 0x12000\n"
    "buffer_load_dwordx4 a[32:35], v8, s[48:51], s30 offen sc1\n"
    "buffer_load_dwordx4 a[36:39], v8, s[48:51], s31 offen sc1\n"
    "buffer_load_dwordx4 a[40:43], v8, s[48:51], s30 offen offset:1024 sc1\n"
    "buffer_load_dwordx4 a[44:47], v8, s[48:51], s31 offen offset:1024 sc1\n"
    "s_mul_i32 s30, s77, 0x4000\n"
    "s_add_u32 s30, s30, s98\n"
    "s_add_u32 s30, s30, 0x3000\n"
    "s_add_u32 s31, s30, 0x12000\n"
    "buffer_load_dwordx4 a[48:51], v8, s[48:51], s30 offen sc1\n"
    "buffer_load_dwordx4 a[52:55], v8, s[48:51], s31 offen sc1\n"
    "buffer_load_dwordx4 a[56:59], v8, s[48:51], s30 offen offset:1024 sc1\n"
    "buffer_load_dwordx4 a[60:63], v8, s[48:51], s31 offen offset:1024 sc1\n"
    "s_lshl_b32 s30, s77, 4\n"
    "s_lshl_b32 s95, 0x2222, s30\n"
    "s_mov_b32 s94, 0\n"
    "v_cmp_ne_u32 s[88:89], s72, v244\n"
    "v_cmp_ne_u32 s[90:91], s72, v245\n"
    "s_or_b64 s[88:89], s[88:89], s[90:91]\n"
    "s_and_b64 s[88:89], s[88:89], s[94:95]\n"
    "s_cbranch_scc1 LA_slow_13%=\n"
    "LA_back_14%=:\n"
    "s_mul_i32 s30, s77, 0x2000\n"
    "s_add_u32 s30, s30, s98\n"
    "s_add_u32 s30, s30, 0x8000\n"
    "s_add_u32 s31, s30, 0x12000\n"
    "buffer_load_dwordx4 a[64:67], v8, s[48:51], s30 offen sc1\n"
    "buffer_load_dwordx4 a[68:71], v8, s[48:51], s31 offen sc1\n"
    "buffer_load_dwordx4 a[72:75], v8, s[48:51], s30 offen offset:1024 sc1\n"
    "buffer_load_dwordx4 a[76:79], v8, s[48:51], s31 offen offset:1024 sc1\n"
    "s_mul_i32 s30, s77, 0x2000\n"
    "s_add_u32 s30, s30, s98\n"
    "s_add_u32 s30, s30, 0x8800\n"
    "s_add_u32 s31, s30, 0x12000\n"
    "buffer_load_dwordx4 a[80:83], v8, s[48:51], s30 offen sc1\n"
    "buffer_load_dwordx4 a[84:87], v8, s[48:51], s31 offen sc1\n"
    "buffer_load_dwordx4 a[88:91], v8, s[48:51], s30 offen offset:1024 sc1\n"
    "buffer_load_dwordx4 a[92:95], v8, s[48:51], s31 offen offset:1024 sc1\n"
    "s_mul_i32 s30, s77, 0x2000\n"
    "s_add_u32 s30, s30, s98\n"
    "s_add_u32 s30, s30, 0x9000\n"
    "s_add_u32 s31, s30, 0x12000\n"
    "buffer_load_dwordx4 a[96:99], v8, s[48:51], s30 offen sc1\n"
    "buffer_load_dwordx4 a[100:103], v8, s[48:51], s31 offen sc1\n"
    "buffer_load_dwordx4 a[104:107], v8, s[48:51], s30 offen offset:1024 sc1\n"
    "buffer_load_dwordx4 a[108:111], v8, s[48:51], s31 offen offset:1024 sc1\n"
    "s_mul_i32 s30, s77, 0x2000\n"
    "s_add_u32 s30, s30, s98\n"
    "s_add_u32 s30, s30, 0x9800\n"
    "s_add_u32 s31, s30, 0x12000\n"
    "buffer_load_dwordx4 a[112:115], v8, s[48:51], s30 offen sc1\n"
    "buffer_load_dwordx4 a[116:119], v8, s[48:51], s31 offen sc1\n"
    "buffer_load_dwordx4 a[120:123], v8, s[48:51], s30 offen offset:1024 sc1\n"
    "buffer_load_dwordx4 a[124:127], v8, s[48:51], s31 offen offset:1024 sc1\n"
    "v_bfe_u32 v224, v0, 3, 1\n"
    "v_bfe_u32 v225, v0, 4, 1\n"
    "v_cmp_eq_u32 vcc, v224, v225\n"
    "v_and_b32 v224, 1, v0\n"
    "v_lshlrev_b32 v224, 4, v224\n"
    "v_mov_b32 v225, 0x3f80\n"
    "v_lshlrev_b32 v224, v224, v225\n"
    "v_cndmask_b32 v224, 0, v224, vcc\n"
    "v_bfe_u32 v225, v0, 1, 2\n"
    "v_cmp_eq_u32 vcc, 0, v225\n"
    "s_mul_i32 s11, s69, 0xc00\n"
    "s_add_u32 s11, s11, 0x24000\n"
    "v_cndmask_b32 v192, 0, v224, vcc\n"
    "v_cmp_eq_u32 vcc, 1, v225\n"
    "s_nop 1\n"
    "v_cndmask_b32 v193, 0, v224, vcc\n"
    "v_cmp_eq_u32 vcc, 2, v225\n"
    "s_nop 1\n"
    "v_cndmask_b32 v194, 0, v224, vcc\n"
    "v_cmp_eq_u32 vcc, 3, v225\n"
    "v_lshrrev_b32 v225, 4, v0\n"
    "v_lshlrev_b32 v225, 3, v225\n"
    "v_cndmask_b32 v195, 0, v224, vcc\n"
    "v_and_b32 v224, 15, v0\n"
    "v_mul_u32_u24 v226, 0x48, v224\n"
    "v_add3_u32 v226, v226, v225, s11\n"
    "v_cmp_gt_u32 vcc, 2, v224\n"
    "v_lshl_add_u32 v224, v224, 5, v225\n"
    "v_mov_b32 v225, 0x200\n"
    "v_cndmask_b32 v224, v225, v224, vcc\n"
    "v_add_u32 v224, s11, v224\n"
    "v_lshrrev_b32 v225, 5, v0\n"
    "s_mul_i32 s11, s76, 0xc00\n"
    "s_add_u32 s11, s11, 0x24000\n"
    "s_lshl_b32 s10, s71, 11\n"
    "s_waitcnt vmcnt(55)\n"
    "v_mfma_f32_16x16x32_bf16 v[196:199], v[192:195], a[160:163], 0\n"
    "s_waitcnt vmcnt(54)\n"
    "v_mfma_f32_16x16x32_bf16 v[200:203], v[192:195], a[164:167], 0\n"
    "s_waitcnt vmcnt(53)\n"
    "v_mfma_f32_16x16x32_bf16 v[204:207], v[192:195], a[168:171], 0\n"
    "s_waitcnt vmcnt(52)\n"
    "v_mfma_f32_16x16x32_bf16 v[196:199], v[192:195], a[172:175], v[196:199]\n"
    "s_waitcnt vmcnt(51)\n"
    "v_mfma_f32_16x16x32_bf16 v[200:203], v[192:195], a[176:179], v[200:203]\n"
    "s_waitcnt vmcnt(50)\n"
    "v_mfma_f32_16x16x32_bf16 v[204:207], v[192:195], a[180:183], v[204:207]\n"
    "s_waitcnt vmcnt(49)\n"
    "v_mfma_f32_16x16x32_bf16 v[196:199], v[192:195], a[184:187], v[196:199]\n"
    "s_waitcnt vmcnt(48)\n"
    "v_mfma_f32_16x16x32_bf16 v[200:203], v[192:195], a[188:191], v[200:203]\n"
    "s_waitcnt vmcnt(47)\n"
    "v_mfma_f32_16x16x32_bf16 v[204:207], v[192:195], a[192:195], v[204:207]\n"
    "s_waitcnt vmcnt(46)\n"
    "v_mfma_f32_16x16x32_bf16 v[196:199], v[192:195], a[196:199], v[196:199]\n"
    "s_waitcnt vmcnt(45)\n"
    "v_mfma_f32_16x16x32_bf16 v[200:203], v[192:195], a[200:203], v[200:203]\n"
    "s_waitcnt vmcnt(44)\n"
    "v_mfma_f32_16x16x32_bf16 v[204:207], v[192:195], a[204:207], v[204:207]\n"
    "s_waitcnt vmcnt(43)\n"
    "v_mfma_f32_16x16x32_bf16 v[196:199], v[192:195], a[208:211], v[196:199]\n"
    "s_waitcnt vmcnt(42)\n"
    "v_mfma_f32_16x16x32_bf16 v[200:203], v[192:195], a[212:215], v[200:203]\n"
    "s_waitcnt vmcnt(41)\n"
    "v_mfma_f32_16x16x32_bf16 v[204:207], v[192:195], a[216:219], v[204:207]\n"
    "s_waitcnt vmcnt(40)\n"
    "v_mfma_f32_16x16x32_bf16 v[196:199], v[192:195], a[220:223], v[196:199]\n"
    "s_waitcnt vmcnt(39)\n"
    "v_mfma_f32_16x16x32_bf16 v[200:203], v[192:195], a[224:227], v[200:203]\n"
    "s_waitcnt vmcnt(38)\n"
    "v_mfma_f32_16x16x32_bf16 v[204:207], v[192:195], a[228:231], v[204:207]\n"
    "s_waitcnt vmcnt(37)\n"
    "v_mfma_f32_16x16x32_bf16 v[196:199], v[192:195], a[232:235], v[196:199]\n"
    "s_waitcnt vmcnt(36)\n"
    "v_mfma_f32_16x16x32_bf16 v[200:203], v[192:195], a[236:239], v[200:203]\n"
    "s_waitcnt vmcnt(35)\n"
    "v_mfma_f32_16x16x32_bf16 v[204:207], v[192:195], a[240:243], v[204:207]\n"
    "s_waitcnt vmcnt(34)\n"
    "v_mfma_f32_16x16x32_bf16 v[196:199], v[192:195], a[244:247], v[196:199]\n"
    "s_waitcnt vmcnt(33)\n"
    "v_mfma_f32_16x16x32_bf16 v[200:203], v[192:195], a[248:251], v[200:203]\n"
    "s_waitcnt vmcnt(32)\n"
    "v_mfma_f32_16x16x32_bf16 v[204:207], v[192:195], a[252:255], v[204:207]\n"
    "s_nop 3\n"
    "v_cvt_pk_bf16_f32 v196, v196, v197\n"
    "v_cvt_pk_bf16_f32 v197, v198, v199\n"
    "ds_write_b64 v226, v[196:197]\n"
    "v_cvt_pk_bf16_f32 v200, v200, v201\n"
    "v_cvt_pk_bf16_f32 v201, v202, v203\n"
    "ds_write_b64 v226, v[200:201] offset:32\n"
    "v_cvt_pk_bf16_f32 v204, v204, v205\n"
    "v_cvt_pk_bf16_f32 v205, v206, v207\n"
    "ds_write_b64 v224, v[204:205] offset:1152\n"
    "v_and_b32 v224, 31, v0\n"
    "v_min_u32 v224, 16, v224\n"
    "v_mul_u32_u24 v224, 0x48, v224\n"
    "v_lshl_add_u32 v224, v225, 3, v224\n"
    "v_add_u32 v225, s11, v224\n"
    "s_lshl_b32 s11, s76, 10\n"
    "s_add_u32 s10, s10, s11\n"
    "s_add_u32 s10, s10, s83\n"
    "s_add_u32 s10, s10, 0xe000\n"
    "s_add_u32 s11, s10, 0x24000\n"
    "s_waitcnt lgkmcnt(0)\n"
    "s_barrier\n"
    "ds_read_b64 v[208:209], v225\n"
    "ds_read_b64 v[210:211], v225 offset:16\n"
    "ds_read_b64 v[212:213], v225 offset:32\n"
    "ds_read_b64 v[214:215], v225 offset:48\n"
    "ds_read_b64 v[216:217], v225 offset:6144\n"
    "ds_read_b64 v[218:219], v225 offset:6160\n"
    "ds_read_b64 v[220:221], v225 offset:6176\n"
    "ds_read_b64 v[222:223], v225 offset:6192\n"
    "s_waitcnt lgkmcnt(6)\n"
    "v_mfma_f32_32x32x16_bf16 v[96:111], v[208:211], v[128:131], 0\n"
    "s_waitcnt lgkmcnt(2)\n"
    "v_mfma_f32_32x32x16_bf16 v[112:127], v[216:219], v[136:139], 0\n"
    "v_mfma_f32_32x32x16_bf16 v[96:111], v[212:215], v[132:135], v[96:111]\n"
    "s_waitcnt lgkmcnt(0)\n"
    "v_mfma_f32_32x32x16_bf16 v[112:127], v[220:223], v[140:143], v[112:127]\n"
    "s_nop 9\n"
    "v_add_f32 v104, 0x358637bd, v104\n"
    "v_rcp_f32 v104, v104\n"
    "s_nop 0\n"
    "v_mul_f32 v96, v96, v104\n"
    "v_mul_f32 v97, v97, v104\n"
    "v_mul_f32 v98, v98, v104\n"
    "v_mul_f32 v99, v99, v104\n"
    "v_mul_f32 v100, v100, v104\n"
    "v_mul_f32 v101, v101, v104\n"
    "v_mul_f32 v102, v102, v104\n"
    "v_mul_f32 v103, v103, v104\n"
    "v_cvt_pk_bf16_f32 v108, v96, v97\n"
    "v_cvt_pk_bf16_f32 v109, v98, v99\n"
    "v_cvt_pk_bf16_f32 v110, v100, v101\n"
    "v_cvt_pk_bf16_f32 v111, v102, v103\n"
    "buffer_store_dwordx4 v[108:111], v14, s[48:51], s10 offen sc1\n"
    "v_add_f32 v120, 0x358637bd, v120\n"
    "v_rcp_f32 v120, v120\n"
    "v_mfma_f32_32x32x16_bf16 v[96:111], v[208:211], v[144:147], 0\n"
    "v_mul_f32 v112, v112, v120\n"
    "v_mul_f32 v113, v113, v120\n"
    "v_mul_f32 v114, v114, v120\n"
    "v_mul_f32 v115, v115, v120\n"
    "v_mul_f32 v116, v116, v120\n"
    "v_mul_f32 v117, v117, v120\n"
    "v_mul_f32 v118, v118, v120\n"
    "v_mul_f32 v119, v119, v120\n"
    "v_cvt_pk_bf16_f32 v124, v112, v113\n"
    "v_cvt_pk_bf16_f32 v125, v114, v115\n"
    "v_cvt_pk_bf16_f32 v126, v116, v117\n"
    "v_cvt_pk_bf16_f32 v127, v118, v119\n"
    "buffer_store_dwordx4 v[124:127], v14, s[48:51], s10 offen offset:512 sc1\n"
    "v_mfma_f32_32x32x16_bf16 v[96:111], v[212:215], v[148:151], v[96:111]\n"
    "v_mov_b32 v249, 0\n"
    "v_mfma_f32_32x32x16_bf16 v[112:127], v[216:219], v[152:155], 0\n"
    "v_mov_b32 v250, s72\n"
    "s_nop 8\n"
    "v_add_f32 v104, 0x358637bd, v104\n"
    "v_rcp_f32 v104, v104\n"
    "s_nop 0\n"
    "v_mul_f32 v96, v96, v104\n"
    "v_mul_f32 v97, v97, v104\n"
    "v_mul_f32 v98, v98, v104\n"
    "v_mul_f32 v99, v99, v104\n"
    "v_mul_f32 v100, v100, v104\n"
    "v_mfma_f32_32x32x16_bf16 v[112:127], v[220:223], v[156:159], v[112:127]\n"
    "v_mul_f32 v101, v101, v104\n"
    "v_mul_f32 v102, v102, v104\n"
    "v_mul_f32 v103, v103, v104\n"
    "v_cvt_pk_bf16_f32 v108, v96, v97\n"
    "v_cvt_pk_bf16_f32 v109, v98, v99\n"
    "v_cvt_pk_bf16_f32 v110, v100, v101\n"
    "v_cvt_pk_bf16_f32 v111, v102, v103\n"
    "buffer_store_dwordx4 v[108:111], v14, s[48:51], s11 offen sc1\n"
    "s_nop 3\n"
    "v_add_f32 v120, 0x358637bd, v120\n"
    "v_rcp_f32 v120, v120\n"
    "s_nop 0\n"
    "v_mul_f32 v112, v112, v120\n"
    "v_mul_f32 v113, v113, v120\n"
    "v_mul_f32 v114, v114, v120\n"
    "v_mul_f32 v115, v115, v120\n"
    "v_mul_f32 v116, v116, v120\n"
    "v_mul_f32 v117, v117, v120\n"
    "v_mul_f32 v118, v118, v120\n"
    "v_mul_f32 v119, v119, v120\n"
    "v_cvt_pk_bf16_f32 v124, v112, v113\n"
    "v_cvt_pk_bf16_f32 v125, v114, v115\n"
    "v_cvt_pk_bf16_f32 v126, v116, v117\n"
    "v_cvt_pk_bf16_f32 v127, v118, v119\n"
    "buffer_store_dwordx4 v[124:127], v14, s[48:51], s11 offen offset:512 sc1\n"
    "s_waitcnt vmcnt(0)\n"
    "buffer_store_dword v250, v249, s[48:51], s84 offen offset:8 sc1\n"
    "s_barrier\n"
    "ds_read_b64_tr_b16 v[16:17], v2 offset:16384\n"
    "ds_read_b64_tr_b16 v[18:19], v2 offset:17408\n"
    "ds_read_b64_tr_b16 v[20:21], v3 offset:16384\n"
    "ds_read_b64_tr_b16 v[22:23], v3 offset:17408\n"
    "ds_read_b64_tr_b16 v[24:25], v4 offset:16384\n"
    "ds_read_b64_tr_b16 v[26:27], v4 offset:17408\n"
    "ds_read_b64_tr_b16 v[28:29], v5 offset:16384\n"
    "ds_read_b64_tr_b16 v[30:31], v5 offset:17408\n"
    "ds_read_b64_tr_b16 v[168:169], v2 offset:20480\n"
    "s_waitcnt lgkmcnt(7)\n"
    "v_mfma_f32_16x16x32_bf16 v[32:35], v[16:19], a[0:3], 0\n"
    "ds_read_b64_tr_b16 v[170:171], v2 offset:21504\n"
    "v_mfma_f32_16x16x32_bf16 v[48:51], v[16:19], a[4:7], 0\n"
    "ds_read_b64_tr_b16 v[172:173], v3 offset:20480\n"
    "s_lshl_b32 s30, s79, 7\n"
    "s_add_u32 s52, s18, s30\n"
    "s_addc_u32 s53, s19, 0\n"
    "s_sub_u32 s54, 0x80000, s30\n"
    "s_mov_b32 s55, 0x20000\n"
    "s_mov_b32 s74, 0\n"
    "s_add_u32 m0, s81, 0x20000\n"
    "s_nop 0\n"
    "buffer_load_dwordx4 v6, s[52:55], s74 offen lds\n"
    "s_waitcnt lgkmcnt(7)\n"
    "v_mfma_f32_16x16x32_bf16 v[52:55], v[20:23], a[4:7], 0\n"
    "ds_read_b64_tr_b16 v[174:175], v3 offset:21504\n"
    "v_mfma_f32_16x16x32_bf16 v[36:39], v[20:23], a[0:3], 0\n"
    "ds_read_b64_tr_b16 v[176:177], v4 offset:20480\n"
    "buffer_load_dwordx4 v7, s[52:55], s74 offen offset:1024 lds\n"
    "s_waitcnt lgkmcnt(7)\n"
    "v_mfma_f32_16x16x32_bf16 v[40:43], v[24:27], a[0:3], 0\n"
    "ds_read_b64_tr_b16 v[178:179], v4 offset:21504\n"
    "v_mfma_f32_16x16x32_bf16 v[56:59], v[24:27], a[4:7], 0\n"
    "ds_read_b64_tr_b16 v[180:181], v5 offset:20480\n"
    "s_mov_b32 s74, 0x40000\n"
    "s_add_u32 m0, s81, 0x22000\n"
    "s_nop 0\n"
    "buffer_load_dwordx4 v6, s[52:55], s74 offen lds\n"
    "s_waitcnt lgkmcnt(7)\n"
    "v_mfma_f32_16x16x32_bf16 v[60:63], v[28:31], a[4:7], 0\n"
    "ds_read_b64_tr_b16 v[182:183], v5 offset:21504\n"
    "v_mfma_f32_16x16x32_bf16 v[44:47], v[28:31], a[0:3], 0\n"
    "ds_read_b64_tr_b16 v[16:17], v2 offset:24576\n"
    "buffer_load_dwordx4 v7, s[52:55], s74 offen offset:1024 lds\n"
    "s_waitcnt lgkmcnt(7)\n"
    "v_mfma_f32_16x16x32_bf16 v[32:35], v[168:171], a[8:11], v[32:35]\n"
    "ds_read_b64_tr_b16 v[18:19], v2 offset:25600\n"
    "v_mfma_f32_16x16x32_bf16 v[48:51], v[168:171], a[12:15], v[48:51]\n"
    "ds_read_b64_tr_b16 v[20:21], v3 offset:24576\n"
    "s_mov_b32 s74, 0x10000\n"
    "s_add_u32 m0, s81, 0\n"
    "s_nop 0\n"
    "buffer_load_dwordx4 v6, s[52:55], s74 offen lds\n"
    "s_waitcnt lgkmcnt(7)\n"
    "v_mfma_f32_16x16x32_bf16 v[52:55], v[172:175], a[12:15], v[52:55]\n"
    "ds_read_b64_tr_b16 v[22:23], v3 offset:25600\n"
    "v_mfma_f32_16x16x32_bf16 v[36:39], v[172:175], a[8:11], v[36:39]\n"
    "ds_read_b64_tr_b16 v[24:25], v4 offset:24576\n"
    "buffer_load_dwordx4 v7, s[52:55], s74 offen offset:1024 lds\n"
    "s_waitcnt lgkmcnt(7)\n"
    "v_mfma_f32_16x16x32_bf16 v[40:43], v[176:179], a[8:11], v[40:43]\n"
    "ds_read_b64_tr_b16 v[26:27], v4 offset:25600\n"
    "v_mfma_f32_16x16x32_bf16 v[56:59], v[176:179], a[12:15], v[56:59]\n"
    "ds_read_b64_tr_b16 v[28:29], v5 offset:24576\n"
    "s_mov_b32 s74, 0x50000\n"
    "s_add_u32 m0, s81, 0x2000\n"
    "s_nop 0\n"
    "buffer_load_dwordx4 v6, s[52:55], s74 offen lds\n"
    "s_waitcnt lgkmcnt(7)\n"
    "v_mfma_f32_16x16x32_bf16 v[60:63], v[180:183], a[12:15], v[60:63]\n"
    "ds_read_b64_tr_b16 v[30:31], v5 offset:25600\n"
    "v_mfma_f32_16x16x32_bf16 v[44:47], v[180:183], a[8:11], v[44:47]\n"
    "ds_read_b64_tr_b16 v[168:169], v2 offset:28672\n"
    "buffer_load_dwordx4 v7, s[52:55], s74 offen offset:1024 lds\n"
    "buffer_load_dwordx2 v[244:245], v246, s[48:51], s8 offen sc1\n"
    "s_waitcnt lgkmcnt(7)\n"
    "v_mfma_f32_16x16x32_bf16 v[32:35], v[16:19], a[16:19], v[32:35]\n"
    "ds_read_b64_tr_b16 v[170:171], v2 offset:29696\n"
    "v_mfma_f32_16x16x32_bf16 v[48:51], v[16:19], a[20:23], v[48:51]\n"
    "ds_read_b64_tr_b16 v[172:173], v3 offset:28672\n"
    "s_waitcnt lgkmcnt(7)\n"
    "v_mfma_f32_16x16x32_bf16 v[52:55], v[20:23], a[20:23], v[52:55]\n"
    "ds_read_b64_tr_b16 v[174:175], v3 offset:29696\n"
    "v_mfma_f32_16x16x32_bf16 v[36:39], v[20:23], a[16:19], v[36:39]\n"
    "ds_read_b64_tr_b16 v[176:177], v4 offset:28672\n"
    "s_waitcnt lgkmcnt(7)\n"
    "v_mfma_f32_16x16x32_bf16 v[40:43], v[24:27], a[16:19], v[40:43]\n"
    "ds_read_b64_tr_b16 v[178:179], v4 offset:29696\n"
    "v_mfma_f32_16x16x32_bf16 v[56:59], v[24:27], a[20:23], v[56:59]\n"
    "ds_read_b64_tr_b16 v[180:181], v5 offset:28672\n"
    "s_waitcnt lgkmcnt(7)\n"
    "v_mfma_f32_16x16x32_bf16 v[60:63], v[28:31], a[20:23], v[60:63]\n"
    "ds_read_b64_tr_b16 v[182:183], v5 offset:29696\n"
    "v_mfma_f32_16x16x32_bf16 v[44:47], v[28:31], a[16:19], v[44:47]\n"
    "ds_read_b64_tr_b16 v[16:17], v2 offset:32768\n"
    "s_waitcnt lgkmcnt(7)\n"
    "v_mfma_f32_16x16x32_bf16 v[32:35], v[168:171], a[24:27], v[32:35]\n"
    "ds_read_b64_tr_b16 v[18:19], v2 offset:33792\n"
    "v_mfma_f32_16x16x32_bf16 v[48:51], v[168:171], a[28:31], v[48:51]\n"
    "ds_read_b64_tr_b16 v[20:21], v3 offset:32768\n"
    "s_waitcnt lgkmcnt(7)\n"
    "v_mfma_f32_16x16x32_bf16 v[52:55], v[172:175], a[28:31], v[52:55]\n"
    "ds_read_b64_tr_b16 v[22:23], v3 offset:33792\n"
    "v_mfma_f32_16x16x32_bf16 v[36:39], v[172:175], a[24:27], v[36:39]\n"
    "ds_read_b64_tr_b16 v[24:25], v4 offset:32768\n"
    "s_waitcnt lgkmcnt(7)\n"
    "v_mfma_f32_16x16x32_bf16 v[40:43], v[176:179], a[24:27], v[40:43]\n"
    "ds_read_b64_tr_b16 v[26:27], v4 offset:33792\n"
    "v_mfma_f32_16x16x32_bf16 v[56:59], v[176:179], a[28:31], v[56:59]\n"
    "ds_read_b64_tr_b16 v[28:29], v5 offset:32768\n"
    "s_waitcnt lgkmcnt(7)\n"
    "v_mfma_f32_16x16x32_bf16 v[60:63], v[180:183], a[28:31], v[60:63]\n"
    "ds_read_b64_tr_b16 v[30:31], v5 offset:33792\n"
    "v_mfma_f32_16x16x32_bf16 v[44:47], v[180:183], a[24:27], v[44:47]\n"
    "ds_read_b64_tr_b16 v[168:169], v2 offset:36864\n"
    "s_waitcnt lgkmcnt(7)\n"
    "v_mfma_f32_16x16x32_bf16 v[32:35], v[16:19], a[32:35], v[32:35]\n"
    "ds_read_b64_tr_b16 v[170:171], v2 offset:37888\n"
    "v_mfma_f32_16x16x32_bf16 v[48:51], v[16:19], a[36:39], v[48:51]\n"
    "ds_read_b64_tr_b16 v[172:173], v3 offset:36864\n"
    "s_waitcnt lgkmcnt(7)\n"
    "v_mfma_f32_16x16x32_bf16 v[52:55], v[20:23], a[36:39], v[52:55]\n"
    "ds_read_b64_tr_b16 v[174:175], v3 offset:37888\n"
    "v_mfma_f32_16x16x32_bf16 v[36:39], v[20:23], a[32:35], v[36:39]\n"
    "ds_read_b64_tr_b16 v[176:177], v4 offset:36864\n"
    "s_waitcnt lgkmcnt(7)\n"
    "v_mfma_f32_16x16x32_bf16 v[40:43], v[24:27], a[32:35], v[40:43]\n"
    "ds_read_b64_tr_b16 v[178:179], v4 offset:37888\n"
    "v_mfma_f32_16x16x32_bf16 v[56:59], v[24:27], a[36:39], v[56:59]\n"
    "ds_read_b64_tr_b16 v[180:181], v5 offset:36864\n"
    "s_waitcnt lgkmcnt(7)\n"
    "v_mfma_f32_16x16x32_bf16 v[60:63], v[28:31], a[36:39], v[60:63]\n"
    "ds_read_b64_tr_b16 v[182:183], v5 offset:37888\n"
    "v_mfma_f32_16x16x32_bf16 v[44:47], v[28:31], a[32:35], v[44:47]\n"
    "ds_read_b64_tr_b16 v[16:17], v2 offset:40960\n"
    "s_waitcnt lgkmcnt(7)\n"
    "v_mfma_f32_16x16x32_bf16 v[32:35], v[168:171], a[40:43], v[32:35]\n"
    "ds_read_b64_tr_b16 v[18:19], v2 offset:41984\n"
    "v_mfma_f32_16x16x32_bf16 v[48:51], v[168:171], a[44:47], v[48:51]\n"
    "ds_read_b64_tr_b16 v[20:21], v3 offset:40960\n"
    "s_waitcnt lgkmcnt(7)\n"
    "v_mfma_f32_16x16x32_bf16 v[52:55], v[172:175], a[44:47], v[52:55]\n"
    "ds_read_b64_tr_b16 v[22:23], v3 offset:41984\n"
    "v_mfma_f32_16x16x32_bf16 v[36:39], v[172:175], a[40:43], v[36:39]\n"
    "ds_read_b64_tr_b16 v[24:25], v4 offset:40960\n"
    "s_waitcnt lgkmcnt(7)\n"
    "v_mfma_f32_16x16x32_bf16 v[40:43], v[176:179], a[40:43], v[40:43]\n"
    "ds_read_b64_tr_b16 v[26:27], v4 offset:41984\n"
    "v_mfma_f32_16x16x32_bf16 v[56:59], v[176:179], a[44:47], v[56:59]\n"
    "ds_read_b64_tr_b16 v[28:29], v5 offset:40960\n"
    "s_waitcnt lgkmcnt(7)\n"
    "v_mfma_f32_16x16x32_bf16 v[60:63], v[180:183], a[44:47], v[60:63]\n"
    "ds_read_b64_tr_b16 v[30:31], v5 offset:41984\n"
    "v_mfma_f32_16x16x32_bf16 v[44:47], v[180:183], a[40:43], v[44:47]\n"
    "ds_read_b64_tr_b16 v[168:169], v2 offset:45056\n"
    "s_waitcnt lgkmcnt(7)\n"
    "v_mfma_f32_16x16x32_bf16 v[32:35], v[16:19], a[48:51], v[32:35]\n"
    "ds_read_b64_tr_b16 v[170:171], v2 offset:46080\n"
    "v_mfma_f32_16x16x32_bf16 v[48:51], v[16:19], a[52:55], v[48:51]\n"
    "ds_read_b64_tr_b16 v[172:173], v3 offset:45056\n"
    "s_waitcnt lgkmcnt(7)\n"
    "v_mfma_f32_16x16x32_bf16 v[52:55], v[20:23], a[52:55], v[52:55]\n"
    "ds_read_b64_tr_b16 v[174:175], v3 offset:46080\n"
    "v_mfma_f32_16x16x32_bf16 v[36:39], v[20:23], a[48:51], v[36:39]\n"
    "ds_read_b64_tr_b16 v[176:177], v4 offset:45056\n"
    "s_waitcnt lgkmcnt(7)\n"
    "v_mfma_f32_16x16x32_bf16 v[40:43], v[24:27], a[48:51], v[40:43]\n"
    "ds_read_b64_tr_b16 v[178:179], v4 offset:46080\n"
    "v_mfma_f32_16x16x32_bf16 v[56:59], v[24:27], a[52:55], v[56:59]\n"
    "ds_read_b64_tr_b16 v[180:181], v5 offset:45056\n"
    "s_waitcnt lgkmcnt(7)\n"
    "v_mfma_f32_16x16x32_bf16 v[60:63], v[28:31], a[52:55], v[60:63]\n"
    "ds_read_b64_tr_b16 v[182:183], v5 offset:46080\n"
    "v_mfma_f32_16x16x32_bf16 v[44:47], v[28:31], a[48:51], v[44:47]\n"
    "s_barrier\n"
    "ds_read_b64_tr_b16 v[16:17], v2 offset:49152\n"
    "s_mov_b32 s74, 0x20000\n"
    "s_add_u32 m0, s81, 0x4000\n"
    "s_nop 0\n"
    "buffer_load_dwordx4 v6, s[52:55], s74 offen lds\n"
    "s_waitcnt lgkmcnt(7)\n"
    "v_mfma_f32_16x16x32_bf16 v[32:35], v[168:171], a[56:59], v[32:35]\n"
    "ds_read_b64_tr_b16 v[18:19], v2 offset:50176\n"
    "v_mfma_f32_16x16x32_bf16 v[48:51], v[168:171], a[60:63], v[48:51]\n"
    "ds_read_b64_tr_b16 v[20:21], v3 offset:49152\n"
    "buffer_load_dwordx4 v7, s[52:55], s74 offen offset:1024 lds\n"
    "s_waitcnt lgkmcnt(7)\n"
    "v_mfma_f32_16x16x32_bf16 v[52:55], v[172:175], a[60:63], v[52:55]\n"
    "ds_read_b64_tr_b16 v[22:23], v3 offset:50176\n"
    "v_mfma_f32_16x16x32_bf16 v[36:39], v[172:175], a[56:59], v[36:39]\n"
    "ds_read_b64_tr_b16 v[24:25], v4 offset:49152\n"
    "s_mov_b32 s74, 0x60000\n"
    "s_add_u32 m0, s81, 0x6000\n"
    "s_nop 0\n"
    "buffer_load_dwordx4 v6, s[52:55], s74 offen lds\n"
    "s_waitcnt lgkmcnt(7)\n"
    "v_mfma_f32_16x16x32_bf16 v[40:43], v[176:179], a[56:59], v[40:43]\n"
    "ds_read_b64_tr_b16 v[26:27], v4 offset:50176\n"
    "v_mfma_f32_16x16x32_bf16 v[56:59], v[176:179], a[60:63], v[56:59]\n"
    "ds_read_b64_tr_b16 v[28:29], v5 offset:49152\n"
    "buffer_load_dwordx4 v7, s[52:55], s74 offen offset:1024 lds\n"
    "s_waitcnt lgkmcnt(7)\n"
    "v_mfma_f32_16x16x32_bf16 v[60:63], v[180:183], a[60:63], v[60:63]\n"
    "ds_read_b64_tr_b16 v[30:31], v5 offset:50176\n"
    "v_mfma_f32_16x16x32_bf16 v[44:47], v[180:183], a[56:59], v[44:47]\n"
    "ds_read_b64_tr_b16 v[168:169], v2 offset:53248\n"
    "s_lshl_b32 s30, s77, 4\n"
    "s_lshl_b32 s94, 0x4444, s30\n"
    "s_mov_b32 s95, 0\n"
    "s_waitcnt vmcnt(4)\n"
    "v_cmp_ne_u32 s[88:89], s72, v244\n"
    "v_cmp_ne_u32 s[90:91], s72, v245\n"
    "s_or_b64 s[88:89], s[88:89], s[90:91]\n"
    "s_and_b64 s[88:89], s[88:89], s[94:95]\n"
    "s_cbranch_scc1 LA_slow_15%=\n"
    "LA_back_16%=:\n"
    "s_mul_i32 s30, s77, 0x4000\n"
    "s_add_u32 s30, s30, s98\n"
    "s_add_u32 s30, s30, 0x800\n"
    "s_add_u32 s31, s30, 0x12000\n"
    "buffer_load_dwordx4 a[128:131], v8, s[48:51], s30 offen sc1\n"
    "buffer_load_dwordx4 a[132:135], v8, s[48:51], s31 offen sc1\n"
    "buffer_load_dwordx4 a[136:139], v8, s[48:51], s30 offen offset:1024 sc1\n"
    "buffer_load_dwordx4 a[140:143], v8, s[48:51], s31 offen offset:1024 sc1\n"
    "s_mul_i32 s30, s77, 0x4000\n"
    "s_add_u32 s30, s30, s98\n"
    "s_add_u32 s30, s30, 0x1800\n"
    "s_add_u32 s31, s30, 0x12000\n"
    "buffer_load_dwordx4 a[144:147], v8, s[48:51], s30 offen sc1\n"
    "buffer_load_dwordx4 a[148:151], v8, s[48:51], s31 offen sc1\n"
    "buffer_load_dwordx4 a[152:155], v8, s[48:51], s30 offen offset:1024 sc1\n"
    "buffer_load_dwordx4 a[156:159], v8, s[48:51], s31 offen offset:1024 sc1\n"
    "s_mul_i32 s30, s77, 0x4000\n"
    "s_add_u32 s30, s30, s98\n"
    "s_add_u32 s30, s30, 0x2800\n"
    "s_add_u32 s31, s30, 0x12000\n"
    "buffer_load_dwordx4 a[160:163], v8, s[48:51], s30 offen sc1\n"
    "buffer_load_dwordx4 a[164:167], v8, s[48:51], s31 offen sc1\n"
    "buffer_load_dwordx4 a[168:171], v8, s[48:51], s30 offen offset:1024 sc1\n"
    "buffer_load_dwordx4 a[172:175], v8, s[48:51], s31 offen offset:1024 sc1\n"
    "s_mul_i32 s30, s77, 0x4000\n"
    "s_add_u32 s30, s30, s98\n"
    "s_add_u32 s30, s30, 0x3800\n"
    "s_add_u32 s31, s30, 0x12000\n"
    "buffer_load_dwordx4 a[176:179], v8, s[48:51], s30 offen sc1\n"
    "buffer_load_dwordx4 a[180:183], v8, s[48:51], s31 offen sc1\n"
    "buffer_load_dwordx4 a[184:187], v8, s[48:51], s30 offen offset:1024 sc1\n"
    "buffer_load_dwordx4 a[188:191], v8, s[48:51], s31 offen offset:1024 sc1\n"
    "s_lshl_b32 s30, s77, 4\n"
    "s_lshl_b32 s95, 0x4444, s30\n"
    "s_mov_b32 s94, 0\n"
    "v_cmp_ne_u32 s[88:89], s72, v244\n"
    "v_cmp_ne_u32 s[90:91], s72, v245\n"
    "s_or_b64 s[88:89], s[88:89], s[90:91]\n"
    "s_and_b64 s[88:89], s[88:89], s[94:95]\n"
    "s_cbranch_scc1 LA_slow_17%=\n"
    "LA_back_18%=:\n"
    "s_mul_i32 s30, s77, 0x1000\n"
    "s_add_u32 s30, s30, s98\n"
    "s_add_u32 s30, s30, 0xc000\n"
    "s_add_u32 s31, s30, 0x12000\n"
    "buffer_load_dwordx4 a[192:195], v8, s[48:51], s30 offen sc1\n"
    "buffer_load_dwordx4 a[196:199], v8, s[48:51], s31 offen sc1\n"
    "buffer_load_dwordx4 a[200:203], v8, s[48:51], s30 offen offset:1024 sc1\n"
    "buffer_load_dwordx4 a[204:207], v8, s[48:51], s31 offen offset:1024 sc1\n"
    "s_mul_i32 s30, s77, 0x1000\n"
    "s_add_u32 s30, s30, s98\n"
    "s_add_u32 s30, s30, 0xc800\n"
    "s_add_u32 s31, s30, 0x12000\n"
    "buffer_load_dwordx4 a[208:211], v8, s[48:51], s30 offen sc1\n"
    "buffer_load_dwordx4 a[212:215], v8, s[48:51], s31 offen sc1\n"
    "buffer_load_dwordx4 a[216:219], v8, s[48:51], s30 offen offset:1024 sc1\n"
    "buffer_load_dwordx4 a[220:223], v8, s[48:51], s31 offen offset:1024 sc1\n"
    "buffer_load_dwordx2 v[244:245], v246, s[48:51], s9 offen sc1\n"
    "s_waitcnt lgkmcnt(7)\n"
    "v_mfma_f32_16x16x32_bf16 v[32:35], v[16:19], a[64:67], v[32:35]\n"
    "ds_read_b64_tr_b16 v[170:171], v2 offset:54272\n"
    "v_mfma_f32_16x16x32_bf16 v[48:51], v[16:19], a[68:71], v[48:51]\n"
    "ds_read_b64_tr_b16 v[172:173], v3 offset:53248\n"
    "s_waitcnt lgkmcnt(7)\n"
    "v_mfma_f32_16x16x32_bf16 v[52:55], v[20:23], a[68:71], v[52:55]\n"
    "ds_read_b64_tr_b16 v[174:175], v3 offset:54272\n"
    "v_mfma_f32_16x16x32_bf16 v[36:39], v[20:23], a[64:67], v[36:39]\n"
    "ds_read_b64_tr_b16 v[176:177], v4 offset:53248\n"
    "s_waitcnt lgkmcnt(7)\n"
    "v_mfma_f32_16x16x32_bf16 v[40:43], v[24:27], a[64:67], v[40:43]\n"
    "ds_read_b64_tr_b16 v[178:179], v4 offset:54272\n"
    "v_mfma_f32_16x16x32_bf16 v[56:59], v[24:27], a[68:71], v[56:59]\n"
    "ds_read_b64_tr_b16 v[180:181], v5 offset:53248\n"
    "s_waitcnt lgkmcnt(7)\n"
    "v_mfma_f32_16x16x32_bf16 v[60:63], v[28:31], a[68:71], v[60:63]\n"
    "ds_read_b64_tr_b16 v[182:183], v5 offset:54272\n"
    "v_mfma_f32_16x16x32_bf16 v[44:47], v[28:31], a[64:67], v[44:47]\n"
    "ds_read_b64_tr_b16 v[16:17], v2 offset:57344\n"
    "s_waitcnt lgkmcnt(7)\n"
    "v_mfma_f32_16x16x32_bf16 v[32:35], v[168:171], a[72:75], v[32:35]\n"
    "ds_read_b64_tr_b16 v[18:19], v2 offset:58368\n"
    "v_mfma_f32_16x16x32_bf16 v[48:51], v[168:171], a[76:79], v[48:51]\n"
    "ds_read_b64_tr_b16 v[20:21], v3 offset:57344\n"
    "s_waitcnt lgkmcnt(7)\n"
    "v_mfma_f32_16x16x32_bf16 v[52:55], v[172:175], a[76:79], v[52:55]\n"
    "ds_read_b64_tr_b16 v[22:23], v3 offset:58368\n"
    "v_mfma_f32_16x16x32_bf16 v[36:39], v[172:175], a[72:75], v[36:39]\n"
    "ds_read_b64_tr_b16 v[24:25], v4 offset:57344\n"
    "s_waitcnt lgkmcnt(7)\n"
    "v_mfma_f32_16x16x32_bf16 v[40:43], v[176:179], a[72:75], v[40:43]\n"
    "ds_read_b64_tr_b16 v[26:27], v4 offset:58368\n"
    "v_mfma_f32_16x16x32_bf16 v[56:59], v[176:179], a[76:79], v[56:59]\n"
    "ds_read_b64_tr_b16 v[28:29], v5 offset:57344\n"
    "s_waitcnt lgkmcnt(7)\n"
    "v_mfma_f32_16x16x32_bf16 v[60:63], v[180:183], a[76:79], v[60:63]\n"
    "ds_read_b64_tr_b16 v[30:31], v5 offset:58368\n"
    "v_mfma_f32_16x16x32_bf16 v[44:47], v[180:183], a[72:75], v[44:47]\n"
    "ds_read_b64_tr_b16 v[168:169], v2 offset:61440\n"
    "s_waitcnt lgkmcnt(7)\n"
    "v_mfma_f32_16x16x32_bf16 v[32:35], v[16:19], a[80:83], v[32:35]\n"
    "ds_read_b64_tr_b16 v[170:171], v2 offset:62464\n"
    "v_mfma_f32_16x16x32_bf16 v[48:51], v[16:19], a[84:87], v[48:51]\n"
    "ds_read_b64_tr_b16 v[172:173], v3 offset:61440\n"
    "s_waitcnt lgkmcnt(7)\n"
    "v_mfma_f32_16x16x32_bf16 v[52:55], v[20:23], a[84:87], v[52:55]\n"
    "ds_read_b64_tr_b16 v[174:175], v3 offset:62464\n"
    "v_mfma_f32_16x16x32_bf16 v[36:39], v[20:23], a[80:83], v[36:39]\n"
    "ds_read_b64_tr_b16 v[176:177], v4 offset:61440\n"
    "s_waitcnt lgkmcnt(7)\n"
    "v_mfma_f32_16x16x32_bf16 v[40:43], v[24:27], a[80:83], v[40:43]\n"
    "ds_read_b64_tr_b16 v[178:179], v4 offset:62464\n"
    "v_mfma_f32_16x16x32_bf16 v[56:59], v[24:27], a[84:87], v[56:59]\n"
    "ds_read_b64_tr_b16 v[180:181], v5 offset:61440\n"
    "s_waitcnt lgkmcnt(7)\n"
    "v_mfma_f32_16x16x32_bf16 v[60:63], v[28:31], a[84:87], v[60:63]\n"
    "ds_read_b64_tr_b16 v[182:183], v5 offset:62464\n"
    "v_mfma_f32_16x16x32_bf16 v[44:47], v[28:31], a[80:83], v[44:47]\n"
    "v_add_u32 v2, 0x10000, v2\n"
    "v_add_u32 v3, 0x10000, v3\n"
    "v_add_u32 v4, 0x10000, v4\n"
    "v_add_u32 v5, 0x10000, v5\n"
    "ds_read_b64_tr_b16 v[16:17], v2\n"
    "s_waitcnt lgkmcnt(7)\n"
    "v_mfma_f32_16x16x32_bf16 v[32:35], v[168:171], a[88:91], v[32:35]\n"
    "ds_read_b64_tr_b16 v[18:19], v2 offset:1024\n"
    "v_mfma_f32_16x16x32_bf16 v[48:51], v[168:171], a[92:95], v[48:51]\n"
    "ds_read_b64_tr_b16 v[20:21], v3\n"
    "s_waitcnt lgkmcnt(7)\n"
    "v_mfma_f32_16x16x32_bf16 v[52:55], v[172:175], a[92:95], v[52:55]\n"
    "ds_read_b64_tr_b16 v[22:23], v3 offset:1024\n"
    "v_mfma_f32_16x16x32_bf16 v[36:39], v[172:175], a[88:91], v[36:39]\n"
    "ds_read_b64_tr_b16 v[24:25], v4\n"
    "s_waitcnt lgkmcnt(7)\n"
    "v_mfma_f32_16x16x32_bf16 v[40:43], v[176:179], a[88:91], v[40:43]\n"
    "ds_read_b64_tr_b16 v[26:27], v4 offset:1024\n"
    "v_mfma_f32_16x16x32_bf16 v[56:59], v[176:179], a[92:95], v[56:59]\n"
    "ds_read_b64_tr_b16 v[28:29], v5\n"
    "s_waitcnt lgkmcnt(7)\n"
    "v_mfma_f32_16x16x32_bf16 v[60:63], v[180:183], a[92:95], v[60:63]\n"
    "ds_read_b64_tr_b16 v[30:31], v5 offset:1024\n"
    "v_mfma_f32_16x16x32_bf16 v[44:47], v[180:183], a[88:91], v[44:47]\n"
    "ds_read_b64_tr_b16 v[168:169], v2 offset:4096\n"
    "s_waitcnt lgkmcnt(7)\n"
    "v_mfma_f32_16x16x32_bf16 v[32:35], v[16:19], a[96:99], v[32:35]\n"
    "ds_read_b64_tr_b16 v[170:171], v2 offset:5120\n"
    "v_mfma_f32_16x16x32_bf16 v[48:51], v[16:19], a[100:103], v[48:51]\n"
    "ds_read_b64_tr_b16 v[172:173], v3 offset:4096\n"
    "s_waitcnt lgkmcnt(7)\n"
    "v_mfma_f32_16x16x32_bf16 v[52:55], v[20:23], a[100:103], v[52:55]\n"
    "ds_read_b64_tr_b16 v[174:175], v3 offset:5120\n"
    "v_mfma_f32_16x16x32_bf16 v[36:39], v[20:23], a[96:99], v[36:39]\n"
    "ds_read_b64_tr_b16 v[176:177], v4 offset:4096\n"
    "s_waitcnt lgkmcnt(7)\n"
    "v_mfma_f32_16x16x32_bf16 v[40:43], v[24:27], a[96:99], v[40:43]\n"
    "ds_read_b64_tr_b16 v[178:179], v4 offset:5120\n"
    "v_mfma_f32_16x16x32_bf16 v[56:59], v[24:27], a[100:103], v[56:59]\n"
    "ds_read_b64_tr_b16 v[180:181], v5 offset:4096\n"
    "s_waitcnt lgkmcnt(7)\n"
    "v_mfma_f32_16x16x32_bf16 v[60:63], v[28:31], a[100:103], v[60:63]\n"
    "ds_read_b64_tr_b16 v[182:183], v5 offset:5120\n"
    "v_mfma_f32_16x16x32_bf16 v[44:47], v[28:31], a[96:99], v[44:47]\n"
    "ds_read_b64_tr_b16 v[16:17], v2 offset:8192\n"
    "s_waitcnt lgkmcnt(7)\n"
    "v_mfma_f32_16x16x32_bf16 v[32:35], v[168:171], a[104:107], v[32:35]\n"
    "ds_read_b64_tr_b16 v[18:19], v2 offset:9216\n"
    "v_mfma_f32_16x16x32_bf16 v[48:51], v[168:171], a[108:111], v[48:51]\n"
    "ds_read_b64_tr_b16 v[20:21], v3 offset:8192\n"
    "s_waitcnt lgkmcnt(7)\n"
    "v_mfma_f32_16x16x32_bf16 v[52:55], v[172:175], a[108:111], v[52:55]\n"
    "ds_read_b64_tr_b16 v[22:23], v3 offset:9216\n"
    "v_mfma_f32_16x16x32_bf16 v[36:39], v[172:175], a[104:107], v[36:39]\n"
    "ds_read_b64_tr_b16 v[24:25], v4 offset:8192\n"
    "s_waitcnt lgkmcnt(7)\n"
    "v_mfma_f32_16x16x32_bf16 v[40:43], v[176:179], a[104:107], v[40:43]\n"
    "ds_read_b64_tr_b16 v[26:27], v4 offset:9216\n"
    "v_mfma_f32_16x16x32_bf16 v[56:59], v[176:179], a[108:111], v[56:59]\n"
    "ds_read_b64_tr_b16 v[28:29], v5 offset:8192\n"
    "s_waitcnt lgkmcnt(7)\n"
    "v_mfma_f32_16x16x32_bf16 v[60:63], v[180:183], a[108:111], v[60:63]\n"
    "ds_read_b64_tr_b16 v[30:31], v5 offset:9216\n"
    "v_mfma_f32_16x16x32_bf16 v[44:47], v[180:183], a[104:107], v[44:47]\n"
    "ds_read_b64_tr_b16 v[168:169], v2 offset:12288\n"
    "s_waitcnt lgkmcnt(7)\n"
    "v_mfma_f32_16x16x32_bf16 v[32:35], v[16:19], a[112:115], v[32:35]\n"
    "ds_read_b64_tr_b16 v[170:171], v2 offset:13312\n"
    "v_mfma_f32_16x16x32_bf16 v[48:51], v[16:19], a[116:119], v[48:51]\n"
    "ds_read_b64_tr_b16 v[172:173], v3 offset:12288\n"
    "s_waitcnt lgkmcnt(7)\n"
    "v_mfma_f32_16x16x32_bf16 v[52:55], v[20:23], a[116:119], v[52:55]\n"
    "ds_read_b64_tr_b16 v[174:175], v3 offset:13312\n"
    "v_mfma_f32_16x16x32_bf16 v[36:39], v[20:23], a[112:115], v[36:39]\n"
    "ds_read_b64_tr_b16 v[176:177], v4 offset:12288\n"
    "s_waitcnt lgkmcnt(7)\n"
    "v_mfma_f32_16x16x32_bf16 v[40:43], v[24:27], a[112:115], v[40:43]\n"
    "ds_read_b64_tr_b16 v[178:179], v4 offset:13312\n"
    "v_mfma_f32_16x16x32_bf16 v[56:59], v[24:27], a[116:119], v[56:59]\n"
    "ds_read_b64_tr_b16 v[180:181], v5 offset:12288\n"
    "s_waitcnt lgkmcnt(7)\n"
    "v_mfma_f32_16x16x32_bf16 v[60:63], v[28:31], a[116:119], v[60:63]\n"
    "ds_read_b64_tr_b16 v[182:183], v5 offset:13312\n"
    "v_mfma_f32_16x16x32_bf16 v[44:47], v[28:31], a[112:115], v[44:47]\n"
    "s_barrier\n"
    "ds_read_b64_tr_b16 v[16:17], v2 offset:16384\n"
    "s_mov_b32 s74, 0x30000\n"
    "s_add_u32 m0, s81, 0x8000\n"
    "s_nop 0\n"
    "buffer_load_dwordx4 v6, s[52:55], s74 offen lds\n"
    "s_waitcnt lgkmcnt(7)\n"
    "v_mfma_f32_16x16x32_bf16 v[32:35], v[168:171], a[120:123], v[32:35]\n"
    "ds_read_b64_tr_b16 v[18:19], v2 offset:17408\n"
    "v_mfma_f32_16x16x32_bf16 v[48:51], v[168:171], a[124:127], v[48:51]\n"
    "ds_read_b64_tr_b16 v[20:21], v3 offset:16384\n"
    "buffer_load_dwordx4 v7, s[52:55], s74 offen offset:1024 lds\n"
    "s_waitcnt lgkmcnt(7)\n"
    "v_mfma_f32_16x16x32_bf16 v[52:55], v[172:175], a[124:127], v[52:55]\n"
    "ds_read_b64_tr_b16 v[22:23], v3 offset:17408\n"
    "v_mfma_f32_16x16x32_bf16 v[36:39], v[172:175], a[120:123], v[36:39]\n"
    "ds_read_b64_tr_b16 v[24:25], v4 offset:16384\n"
    "s_mov_b32 s74, 0x70000\n"
    "s_add_u32 m0, s81, 0xa000\n"
    "s_nop 0\n"
    "buffer_load_dwordx4 v6, s[52:55], s74 offen lds\n"
    "s_waitcnt lgkmcnt(7)\n"
    "v_mfma_f32_16x16x32_bf16 v[40:43], v[176:179], a[120:123], v[40:43]\n"
    "ds_read_b64_tr_b16 v[26:27], v4 offset:17408\n"
    "v_mfma_f32_16x16x32_bf16 v[56:59], v[176:179], a[124:127], v[56:59]\n"
    "ds_read_b64_tr_b16 v[28:29], v5 offset:16384\n"
    "buffer_load_dwordx4 v7, s[52:55], s74 offen offset:1024 lds\n"
    "s_waitcnt lgkmcnt(7)\n"
    "v_mfma_f32_16x16x32_bf16 v[60:63], v[180:183], a[124:127], v[60:63]\n"
    "ds_read_b64_tr_b16 v[30:31], v5 offset:17408\n"
    "v_mfma_f32_16x16x32_bf16 v[44:47], v[180:183], a[120:123], v[44:47]\n"
    "ds_read_b64_tr_b16 v[168:169], v2 offset:20480\n"
    "s_waitcnt vmcnt(28) lgkmcnt(7)\n"
    "v_mfma_f32_16x16x32_bf16 v[32:35], v[16:19], a[128:131], v[32:35]\n"
    "ds_read_b64_tr_b16 v[170:171], v2 offset:21504\n"
    "s_waitcnt vmcnt(27)\n"
    "v_mfma_f32_16x16x32_bf16 v[48:51], v[16:19], a[132:135], v[48:51]\n"
    "ds_read_b64_tr_b16 v[172:173], v3 offset:20480\n"
    "s_waitcnt lgkmcnt(7)\n"
    "v_mfma_f32_16x16x32_bf16 v[52:55], v[20:23], a[132:135], v[52:55]\n"
    "ds_read_b64_tr_b16 v[174:175], v3 offset:21504\n"
    "v_mfma_f32_16x16x32_bf16 v[36:39], v[20:23], a[128:131], v[36:39]\n"
    "ds_read_b64_tr_b16 v[176:177], v4 offset:20480\n"
    "s_waitcnt lgkmcnt(7)\n"
    "v_mfma_f32_16x16x32_bf16 v[40:43], v[24:27], a[128:131], v[40:43]\n"
    "ds_read_b64_tr_b16 v[178:179], v4 offset:21504\n"
    "v_mfma_f32_16x16x32_bf16 v[56:59], v[24:27], a[132:135], v[56:59]\n"
    "ds_read_b64_tr_b16 v[180:181], v5 offset:20480\n"
    "s_waitcnt lgkmcnt(7)\n"
    "v_mfma_f32_16x16x32_bf16 v[60:63], v[28:31], a[132:135], v[60:63]\n"
    "ds_read_b64_tr_b16 v[182:183], v5 offset:21504\n"
    "v_mfma_f32_16x16x32_bf16 v[44:47], v[28:31], a[128:131], v[44:47]\n"
    "ds_read_b64_tr_b16 v[16:17], v2 offset:24576\n"
    "s_waitcnt vmcnt(26) lgkmcnt(7)\n"
    "v_mfma_f32_16x16x32_bf16 v[32:35], v[168:171], a[136:139], v[32:35]\n"
    "ds_read_b64_tr_b16 v[18:19], v2 offset:25600\n"
    "s_waitcnt vmcnt(25)\n"
    "v_mfma_f32_16x16x32_bf16 v[48:51], v[168:171], a[140:143], v[48:51]\n"
    "ds_read_b64_tr_b16 v[20:21], v3 offset:24576\n"
    "s_waitcnt lgkmcnt(7)\n"
    "v_mfma_f32_16x16x32_bf16 v[52:55], v[172:175], a[140:143], v[52:55]\n"
    "ds_read_b64_tr_b16 v[22:23], v3 offset:25600\n"
    "v_mfma_f32_16x16x32_bf16 v[36:39], v[172:175], a[136:139], v[36:39]\n"
    "ds_read_b64_tr_b16 v[24:25], v4 offset:24576\n"
    "s_waitcnt lgkmcnt(7)\n"
    "v_mfma_f32_16x16x32_bf16 v[40:43], v[176:179], a[136:139], v[40:43]\n"
    "ds_read_b64_tr_b16 v[26:27], v4 offset:25600\n"
    "v_mfma_f32_16x16x32_bf16 v[56:59], v[176:179], a[140:143], v[56:59]\n"
    "ds_read_b64_tr_b16 v[28:29], v5 offset:24576\n"
    "s_waitcnt lgkmcnt(7)\n"
    "v_mfma_f32_16x16x32_bf16 v[60:63], v[180:183], a[140:143], v[60:63]\n"
    "ds_read_b64_tr_b16 v[30:31], v5 offset:25600\n"
    "v_mfma_f32_16x16x32_bf16 v[44:47], v[180:183], a[136:139], v[44:47]\n"
    "ds_read_b64_tr_b16 v[168:169], v2 offset:28672\n"
    "s_waitcnt vmcnt(24) lgkmcnt(7)\n"
    "v_mfma_f32_16x16x32_bf16 v[32:35], v[16:19], a[144:147], v[32:35]\n"
    "ds_read_b64_tr_b16 v[170:171], v2 offset:29696\n"
    "s_waitcnt vmcnt(23)\n"
    "v_mfma_f32_16x16x32_bf16 v[48:51], v[16:19], a[148:151], v[48:51]\n"
    "ds_read_b64_tr_b16 v[172:173], v3 offset:28672\n"
    "s_waitcnt lgkmcnt(7)\n"
    "v_mfma_f32_16x16x32_bf16 v[52:55], v[20:23], a[148:151], v[52:55]\n"
    "ds_read_b64_tr_b16 v[174:175], v3 offset:29696\n"
    "v_mfma_f32_16x16x32_bf16 v[36:39], v[20:23], a[144:147], v[36:39]\n"
    "ds_read_b64_tr_b16 v[176:177], v4 offset:28672\n"
    "s_waitcnt lgkmcnt(7)\n"
    "v_mfma_f32_16x16x32_bf16 v[40:43], v[24:27], a[144:147], v[40:43]\n"
    "ds_read_b64_tr_b16 v[178:179], v4 offset:29696\n"
    "v_mfma_f32_16x16x32_bf16 v[56:59], v[24:27], a[148:151], v[56:59]\n"
    "ds_read_b64_tr_b16 v[180:181], v5 offset:28672\n"
    "s_waitcnt lgkmcnt(7)\n"
    "v_mfma_f32_16x16x32_bf16 v[60:63], v[28:31], a[148:151], v[60:63]\n"
    "ds_read_b64_tr_b16 v[182:183], v5 offset:29696\n"
    "v_mfma_f32_16x16x32_bf16 v[44:47], v[28:31], a[144:147], v[44:47]\n"
    "ds_read_b64_tr_b16 v[16:17], v2 offset:32768\n"
    "s_waitcnt vmcnt(22) lgkmcnt(7)\n"
    "v_mfma_f32_16x16x32_bf16 v[32:35], v[168:171], a[152:155], v[32:35]\n"
    "ds_read_b64_tr_b16 v[18:19], v2 offset:33792\n"
    "s_waitcnt vmcnt(21)\n"
    "v_mfma_f32_16x16x32_bf16 v[48:51], v[168:171], a[156:159], v[48:51]\n"
    "ds_read_b64_tr_b16 v[20:21], v3 offset:32768\n"
    "s_waitcnt lgkmcnt(7)\n"
    "v_mfma_f32_16x16x32_bf16 v[52:55], v[172:175], a[156:159], v[52:55]\n"
    "ds_read_b64_tr_b16 v[22:23], v3 offset:33792\n"
    "v_mfma_f32_16x16x32_bf16 v[36:39], v[172:175], a[152:155], v[36:39]\n"
    "ds_read_b64_tr_b16 v[24:25], v4 offset:32768\n"
    "s_waitcnt lgkmcnt(7)\n"
    "v_mfma_f32_16x16x32_bf16 v[40:43], v[176:179], a[152:155], v[40:43]\n"
    "ds_read_b64_tr_b16 v[26:27], v4 offset:33792\n"
    "v_mfma_f32_16x16x32_bf16 v[56:59], v[176:179], a[156:159], v[56:59]\n"
    "ds_read_b64_tr_b16 v[28:29], v5 offset:32768\n"
    "s_waitcnt lgkmcnt(7)\n"
    "v_mfma_f32_16x16x32_bf16 v[60:63], v[180:183], a[156:159], v[60:63]\n"
    "ds_read_b64_tr_b16 v[30:31], v5 offset:33792\n"
    "v_mfma_f32_16x16x32_bf16 v[44:47], v[180:183], a[152:155], v[44:47]\n"
    "ds_read_b64_tr_b16 v[168:169], v2 offset:36864\n"
    "s_lshl_b32 s30, s75, 4\n"
    "s_lshl_b32 s94, 0x2222, s30\n"
    "s_mov_b32 s95, 0\n"
    "s_waitcnt vmcnt(4)\n"
    "v_cmp_ne_u32 s[88:89], s72, v244\n"
    "v_cmp_ne_u32 s[90:91], s72, v245\n"
    "s_or_b64 s[88:89], s[88:89], s[90:91]\n"
    "s_and_b64 s[88:89], s[88:89], s[94:95]\n"
    "s_cbranch_scc1 LA_slow_19%=\n"
    "LA_back_20%=:\n"
    "s_add_u32 s30, s99, 0\n"
    "s_add_u32 s31, s30, 0x12000\n"
    "buffer_load_dwordx4 a[224:227], v8, s[48:51], s30 offen sc1\n"
    "buffer_load_dwordx4 a[228:231], v8, s[48:51], s31 offen sc1\n"
    "buffer_load_dwordx4 a[232:235], v8, s[48:51], s30 offen offset:1024 sc1\n"
    "buffer_load_dwordx4 a[236:239], v8, s[48:51], s31 offen offset:1024 sc1\n"
    "buffer_load_dwordx4 a[240:243], v8, s[48:51], s30 offen offset:2048 sc1\n"
    "buffer_load_dwordx4 a[244:247], v8, s[48:51], s31 offen offset:2048 sc1\n"
    "buffer_load_dwordx4 a[248:251], v8, s[48:51], s30 offen offset:3072 sc1\n"
    "buffer_load_dwordx4 a[252:255], v8, s[48:51], s31 offen offset:3072 sc1\n"
    "s_add_u32 s30, s99, 0x1000\n"
    "s_add_u32 s31, s30, 0x12000\n"
    "buffer_load_dwordx4 a[0:3], v8, s[48:51], s30 offen sc1\n"
    "buffer_load_dwordx4 a[4:7], v8, s[48:51], s31 offen sc1\n"
    "buffer_load_dwordx4 a[8:11], v8, s[48:51], s30 offen offset:1024 sc1\n"
    "buffer_load_dwordx4 a[12:15], v8, s[48:51], s31 offen offset:1024 sc1\n"
    "buffer_load_dwordx4 a[16:19], v8, s[48:51], s30 offen offset:2048 sc1\n"
    "buffer_load_dwordx4 a[20:23], v8, s[48:51], s31 offen offset:2048 sc1\n"
    "buffer_load_dwordx4 a[24:27], v8, s[48:51], s30 offen offset:3072 sc1\n"
    "buffer_load_dwordx4 a[28:31], v8, s[48:51], s31 offen offset:3072 sc1\n"
    "s_lshl_b32 s30, s78, 1\n"
    "s_add_u32 s30, s30, s77\n"
    "s_lshl_b32 s30, s30, 1\n"
    "s_add_u32 s30, s30, s76\n"
    "s_lshl_b32 s30, s30, 1\n"
    "s_add_u32 s30, s30, s75\n"
    "s_lshl_b32 s31, s30, 7\n"
    "s_add_u32 s31, s31, 0xa50000\n"
    "v_and_b32 v155, 15, v0\n"
    "v_lshlrev_b32 v156, 3, v155\n"
    "buffer_load_dwordx2 v[152:153], v156, s[48:51], s31 offen sc1\n"
    "s_lshl_b32 s30, s30, 14\n"
    "s_lshl_b32 s31, s79, 7\n"
    "s_add_u32 s30, s30, s31\n"
    "s_add_u32 s36, s12, s30\n"
    "s_addc_u32 s37, s13, 0\n"
    "s_sub_u32 s38, 0x200000, s30\n"
    "s_mov_b32 s39, 0x20000\n"
    "s_add_u32 s56, s26, s30\n"
    "s_addc_u32 s57, s27, 0\n"
    "s_sub_u32 s58, 0x200000, s30\n"
    "s_mov_b32 s59, 0x20000\n"
    "v_lshlrev_b32 v155, 10, v155\n"
    "v_bfe_u32 v156, v0, 4, 1\n"
    "v_lshl_add_u32 v155, v156, 5, v155\n"
    "v_lshrrev_b32 v156, 5, v0\n"
    "v_lshl_add_u32 v154, v156, 4, v155\n"
    "buffer_load_dwordx4 v[144:147], v154, s[36:39], 0 offen\n"
    "buffer_load_dwordx4 v[148:151], v154, s[36:39], 0 offen offset:64\n"
    "s_waitcnt lgkmcnt(7)\n"
    "v_mfma_f32_16x16x32_bf16 v[32:35], v[16:19], a[160:163], v[32:35]\n"
    "ds_read_b64_tr_b16 v[170:171], v2 offset:37888\n"
    "v_mfma_f32_16x16x32_bf16 v[48:51], v[16:19], a[164:167], v[48:51]\n"
    "ds_read_b64_tr_b16 v[172:173], v3 offset:36864\n"
    "s_waitcnt lgkmcnt(7)\n"
    "v_mfma_f32_16x16x32_bf16 v[52:55], v[20:23], a[164:167], v[52:55]\n"
    "ds_read_b64_tr_b16 v[174:175], v3 offset:37888\n"
    "v_mfma_f32_16x16x32_bf16 v[36:39], v[20:23], a[160:163], v[36:39]\n"
    "ds_read_b64_tr_b16 v[176:177], v4 offset:36864\n"
    "s_waitcnt lgkmcnt(7)\n"
    "v_mfma_f32_16x16x32_bf16 v[40:43], v[24:27], a[160:163], v[40:43]\n"
    "ds_read_b64_tr_b16 v[178:179], v4 offset:37888\n"
    "v_mfma_f32_16x16x32_bf16 v[56:59], v[24:27], a[164:167], v[56:59]\n"
    "ds_read_b64_tr_b16 v[180:181], v5 offset:36864\n"
    "s_waitcnt lgkmcnt(7)\n"
    "v_mfma_f32_16x16x32_bf16 v[60:63], v[28:31], a[164:167], v[60:63]\n"
    "ds_read_b64_tr_b16 v[182:183], v5 offset:37888\n"
    "v_mfma_f32_16x16x32_bf16 v[44:47], v[28:31], a[160:163], v[44:47]\n"
    "ds_read_b64_tr_b16 v[16:17], v2 offset:40960\n"
    "s_waitcnt lgkmcnt(7)\n"
    "v_mfma_f32_16x16x32_bf16 v[32:35], v[168:171], a[168:171], v[32:35]\n"
    "ds_read_b64_tr_b16 v[18:19], v2 offset:41984\n"
    "v_mfma_f32_16x16x32_bf16 v[48:51], v[168:171], a[172:175], v[48:51]\n"
    "ds_read_b64_tr_b16 v[20:21], v3 offset:40960\n"
    "s_waitcnt lgkmcnt(7)\n"
    "v_mfma_f32_16x16x32_bf16 v[52:55], v[172:175], a[172:175], v[52:55]\n"
    "ds_read_b64_tr_b16 v[22:23], v3 offset:41984\n"
    "v_mfma_f32_16x16x32_bf16 v[36:39], v[172:175], a[168:171], v[36:39]\n"
    "ds_read_b64_tr_b16 v[24:25], v4 offset:40960\n"
    "s_waitcnt lgkmcnt(7)\n"
    "v_mfma_f32_16x16x32_bf16 v[40:43], v[176:179], a[168:171], v[40:43]\n"
    "ds_read_b64_tr_b16 v[26:27], v4 offset:41984\n"
    "v_mfma_f32_16x16x32_bf16 v[56:59], v[176:179], a[172:175], v[56:59]\n"
    "ds_read_b64_tr_b16 v[28:29], v5 offset:40960\n"
    "s_waitcnt lgkmcnt(7)\n"
    "v_mfma_f32_16x16x32_bf16 v[60:63], v[180:183], a[172:175], v[60:63]\n"
    "ds_read_b64_tr_b16 v[30:31], v5 offset:41984\n"
    "v_mfma_f32_16x16x32_bf16 v[44:47], v[180:183], a[168:171], v[44:47]\n"
    "ds_read_b64_tr_b16 v[168:169], v2 offset:45056\n"
    "s_waitcnt lgkmcnt(7)\n"
    "v_mfma_f32_16x16x32_bf16 v[32:35], v[16:19], a[176:179], v[32:35]\n"
    "ds_read_b64_tr_b16 v[170:171], v2 offset:46080\n"
    "v_mfma_f32_16x16x32_bf16 v[48:51], v[16:19], a[180:183], v[48:51]\n"
    "ds_read_b64_tr_b16 v[172:173], v3 offset:45056\n"
    "s_waitcnt lgkmcnt(7)\n"
    "v_mfma_f32_16x16x32_bf16 v[52:55], v[20:23], a[180:183], v[52:55]\n"
    "ds_read_b64_tr_b16 v[174:175], v3 offset:46080\n"
    "v_mfma_f32_16x16x32_bf16 v[36:39], v[20:23], a[176:179], v[36:39]\n"
    "ds_read_b64_tr_b16 v[176:177], v4 offset:45056\n"
    "s_waitcnt lgkmcnt(7)\n"
    "v_mfma_f32_16x16x32_bf16 v[40:43], v[24:27], a[176:179], v[40:43]\n"
    "ds_read_b64_tr_b16 v[178:179], v4 offset:46080\n"
    "v_mfma_f32_16x16x32_bf16 v[56:59], v[24:27], a[180:183], v[56:59]\n"
    "ds_read_b64_tr_b16 v[180:181], v5 offset:45056\n"
    "s_waitcnt lgkmcnt(7)\n"
    "v_mfma_f32_16x16x32_bf16 v[60:63], v[28:31], a[180:183], v[60:63]\n"
    "ds_read_b64_tr_b16 v[182:183], v5 offset:46080\n"
    "v_mfma_f32_16x16x32_bf16 v[44:47], v[28:31], a[176:179], v[44:47]\n"
    "s_barrier\n"
    "ds_read_b64_tr_b16 v[16:17], v2 offset:49152\n"
    "s_waitcnt lgkmcnt(7)\n"
    "v_mfma_f32_16x16x32_bf16 v[32:35], v[168:171], a[184:187], v[32:35]\n"
    "ds_read_b64_tr_b16 v[18:19], v2 offset:50176\n"
    "v_mfma_f32_16x16x32_bf16 v[48:51], v[168:171], a[188:191], v[48:51]\n"
    "ds_read_b64_tr_b16 v[20:21], v3 offset:49152\n"
    "s_waitcnt lgkmcnt(7)\n"
    "v_mfma_f32_16x16x32_bf16 v[52:55], v[172:175], a[188:191], v[52:55]\n"
    "ds_read_b64_tr_b16 v[22:23], v3 offset:50176\n"
    "v_mfma_f32_16x16x32_bf16 v[36:39], v[172:175], a[184:187], v[36:39]\n"
    "ds_read_b64_tr_b16 v[24:25], v4 offset:49152\n"
    "s_waitcnt lgkmcnt(7)\n"
    "v_mfma_f32_16x16x32_bf16 v[40:43], v[176:179], a[184:187], v[40:43]\n"
    "ds_read_b64_tr_b16 v[26:27], v4 offset:50176\n"
    "v_mfma_f32_16x16x32_bf16 v[56:59], v[176:179], a[188:191], v[56:59]\n"
    "ds_read_b64_tr_b16 v[28:29], v5 offset:49152\n"
    "s_waitcnt lgkmcnt(7)\n"
    "v_mfma_f32_16x16x32_bf16 v[60:63], v[180:183], a[188:191], v[60:63]\n"
    "ds_read_b64_tr_b16 v[30:31], v5 offset:50176\n"
    "v_mfma_f32_16x16x32_bf16 v[44:47], v[180:183], a[184:187], v[44:47]\n"
    "ds_read_b64_tr_b16 v[168:169], v2 offset:53248\n"
    "s_waitcnt lgkmcnt(7)\n"
    "v_mfma_f32_16x16x32_bf16 v[32:35], v[16:19], a[192:195], v[32:35]\n"
    "ds_read_b64_tr_b16 v[170:171], v2 offset:54272\n"
    "v_mfma_f32_16x16x32_bf16 v[48:51], v[16:19], a[196:199], v[48:51]\n"
    "ds_read_b64_tr_b16 v[172:173], v3 offset:53248\n"
    "s_waitcnt lgkmcnt(7)\n"
    "v_mfma_f32_16x16x32_bf16 v[52:55], v[20:23], a[196:199], v[52:55]\n"
    "ds_read_b64_tr_b16 v[174:175], v3 offset:54272\n"
    "v_mfma_f32_16x16x32_bf16 v[36:39], v[20:23], a[192:195], v[36:39]\n"
    "ds_read_b64_tr_b16 v[176:177], v4 offset:53248\n"
    "s_waitcnt lgkmcnt(7)\n"
    "v_mfma_f32_16x16x32_bf16 v[40:43], v[24:27], a[192:195], v[40:43]\n"
    "ds_read_b64_tr_b16 v[178:179], v4 offset:54272\n"
    "v_mfma_f32_16x16x32_bf16 v[56:59], v[24:27], a[196:199], v[56:59]\n"
    "ds_read_b64_tr_b16 v[180:181], v5 offset:53248\n"
    "s_waitcnt lgkmcnt(7)\n"
    "v_mfma_f32_16x16x32_bf16 v[60:63], v[28:31], a[196:199], v[60:63]\n"
    "ds_read_b64_tr_b16 v[182:183], v5 offset:54272\n"
    "v_mfma_f32_16x16x32_bf16 v[44:47], v[28:31], a[192:195], v[44:47]\n"
    "ds_read_b64_tr_b16 v[16:17], v2 offset:57344\n"
    "s_waitcnt lgkmcnt(7)\n"
    "v_mfma_f32_16x16x32_bf16 v[32:35], v[168:171], a[200:203], v[32:35]\n"
    "ds_read_b64_tr_b16 v[18:19], v2 offset:58368\n"
    "v_mfma_f32_16x16x32_bf16 v[48:51], v[168:171], a[204:207], v[48:51]\n"
    "ds_read_b64_tr_b16 v[20:21], v3 offset:57344\n"
    "s_waitcnt lgkmcnt(7)\n"
    "v_mfma_f32_16x16x32_bf16 v[52:55], v[172:175], a[204:207], v[52:55]\n"
    "ds_read_b64_tr_b16 v[22:23], v3 offset:58368\n"
    "v_mfma_f32_16x16x32_bf16 v[36:39], v[172:175], a[200:203], v[36:39]\n"
    "ds_read_b64_tr_b16 v[24:25], v4 offset:57344\n"
    "s_waitcnt lgkmcnt(7)\n"
    "v_mfma_f32_16x16x32_bf16 v[40:43], v[176:179], a[200:203], v[40:43]\n"
    "ds_read_b64_tr_b16 v[26:27], v4 offset:58368\n"
    "v_mfma_f32_16x16x32_bf16 v[56:59], v[176:179], a[204:207], v[56:59]\n"
    "ds_read_b64_tr_b16 v[28:29], v5 offset:57344\n"
    "s_waitcnt lgkmcnt(7)\n"
    "v_mfma_f32_16x16x32_bf16 v[60:63], v[180:183], a[204:207], v[60:63]\n"
    "ds_read_b64_tr_b16 v[30:31], v5 offset:58368\n"
    "v_mfma_f32_16x16x32_bf16 v[44:47], v[180:183], a[200:203], v[44:47]\n"
    "ds_read_b64_tr_b16 v[168:169], v2 offset:61440\n"
    "s_waitcnt lgkmcnt(7)\n"
    "v_mfma_f32_16x16x32_bf16 v[32:35], v[16:19], a[208:211], v[32:35]\n"
    "ds_read_b64_tr_b16 v[170:171], v2 offset:62464\n"
    "v_mfma_f32_16x16x32_bf16 v[48:51], v[16:19], a[212:215], v[48:51]\n"
    "ds_read_b64_tr_b16 v[172:173], v3 offset:61440\n"
    "s_waitcnt lgkmcnt(7)\n"
    "v_mfma_f32_16x16x32_bf16 v[52:55], v[20:23], a[212:215], v[52:55]\n"
    "ds_read_b64_tr_b16 v[174:175], v3 offset:62464\n"
    "v_mfma_f32_16x16x32_bf16 v[36:39], v[20:23], a[208:211], v[36:39]\n"
    "ds_read_b64_tr_b16 v[176:177], v4 offset:61440\n"
    "s_waitcnt lgkmcnt(7)\n"
    "v_mfma_f32_16x16x32_bf16 v[40:43], v[24:27], a[208:211], v[40:43]\n"
    "ds_read_b64_tr_b16 v[178:179], v4 offset:62464\n"
    "v_mfma_f32_16x16x32_bf16 v[56:59], v[24:27], a[212:215], v[56:59]\n"
    "ds_read_b64_tr_b16 v[180:181], v5 offset:61440\n"
    "s_waitcnt lgkmcnt(7)\n"
    "v_mfma_f32_16x16x32_bf16 v[60:63], v[28:31], a[212:215], v[60:63]\n"
    "ds_read_b64_tr_b16 v[182:183], v5 offset:62464\n"
    "v_mfma_f32_16x16x32_bf16 v[44:47], v[28:31], a[208:211], v[44:47]\n"
    "s_waitcnt lgkmcnt(6)\n"
    "v_mfma_f32_16x16x32_bf16 v[32:35], v[168:171], a[216:219], v[32:35]\n"
    "v_mfma_f32_16x16x32_bf16 v[48:51], v[168:171], a[220:223], v[48:51]\n"
    "s_waitcnt lgkmcnt(4)\n"
    "v_mfma_f32_16x16x32_bf16 v[52:55], v[172:175], a[220:223], v[52:55]\n"
    "v_mfma_f32_16x16x32_bf16 v[36:39], v[172:175], a[216:219], v[36:39]\n"
    "s_waitcnt lgkmcnt(2)\n"
    "v_mfma_f32_16x16x32_bf16 v[40:43], v[176:179], a[216:219], v[40:43]\n"
    "v_mfma_f32_16x16x32_bf16 v[56:59], v[176:179], a[220:223], v[56:59]\n"
    "s_waitcnt lgkmcnt(0)\n"
    "v_mfma_f32_16x16x32_bf16 v[60:63], v[180:183], a[220:223], v[60:63]\n"
    "v_mfma_f32_16x16x32_bf16 v[44:47], v[180:183], a[216:219], v[44:47]\n"
    "s_cmp_eq_u32 s75, 0\n"
    "s_cbranch_scc1 LA_nosend_21%=\n"
    "s_lshl_b32 s30, s68, 1\n"
    "s_add_u32 s30, s30, s76\n"
    "s_lshl_b32 s30, s30, 13\n"
    "s_add_u32 s30, s30, 0xa58900\n"
    "s_add_u32 s31, s30, 0x1000\n"
    "v_mov_b32 v97, s72\n"
    "v_mov_b32 v99, s72\n"
    "v_mov_b32 v101, s72\n"
    "v_mov_b32 v103, s72\n"
    "v_mov_b32 v105, s72\n"
    "v_mov_b32 v107, s72\n"
    "v_mov_b32 v109, s72\n"
    "v_mov_b32 v111, s72\n"
    "v_mov_b32 v113, s72\n"
    "v_mov_b32 v115, s72\n"
    "v_mov_b32 v117, s72\n"
    "v_mov_b32 v119, s72\n"
    "v_mov_b32 v121, s72\n"
    "v_mov_b32 v123, s72\n"
    "v_mov_b32 v125, s72\n"
    "v_mov_b32 v127, s72\n"
    "v_cvt_pk_bf16_f32 v96, v32, v33\n"
    "v_cvt_pk_bf16_f32 v98, v34, v35\n"
    "buffer_store_dwordx4 v[96:99], v8, s[48:51], s30 offen sc1\n"
    "v_cvt_pk_bf16_f32 v100, v36, v37\n"
    "v_cvt_pk_bf16_f32 v102, v38, v39\n"
    "buffer_store_dwordx4 v[100:103], v8, s[48:51], s30 offen offset:1024 sc1\n"
    "v_cvt_pk_bf16_f32 v104, v40, v41\n"
    "v_cvt_pk_bf16_f32 v106, v42, v43\n"
    "buffer_store_dwordx4 v[104:107], v8, s[48:51], s30 offen offset:2048 sc1\n"
    "v_cvt_pk_bf16_f32 v108, v44, v45\n"
    "v_cvt_pk_bf16_f32 v110, v46, v47\n"
    "buffer_store_dwordx4 v[108:111], v8, s[48:51], s30 offen offset:3072 sc1\n"
    "v_cvt_pk_bf16_f32 v112, v48, v49\n"
    "v_cvt_pk_bf16_f32 v114, v50, v51\n"
    "buffer_store_dwordx4 v[112:115], v8, s[48:51], s31 offen sc1\n"
    "v_cvt_pk_bf16_f32 v116, v52, v53\n"
    "v_cvt_pk_bf16_f32 v118, v54, v55\n"
    "buffer_store_dwordx4 v[116:119], v8, s[48:51], s31 offen offset:1024 sc1\n"
    "v_cvt_pk_bf16_f32 v120, v56, v57\n"
    "v_cvt_pk_bf16_f32 v122, v58, v59\n"
    "buffer_store_dwordx4 v[120:123], v8, s[48:51], s31 offen offset:2048 sc1\n"
    "v_cvt_pk_bf16_f32 v124, v60, v61\n"
    "v_cvt_pk_bf16_f32 v126, v62, v63\n"
    "buffer_store_dwordx4 v[124:127], v8, s[48:51], s31 offen offset:3072 sc1\n"
    "s_nop 1\n"
    "LA_nosend_21%=:\n"
    "s_lshl_b32 s30, s75, 13\n"
    "v_add_u32 v2, s30, v2\n"
    "v_add_u32 v3, s30, v3\n"
    "v_add_u32 v4, s30, v4\n"
    "v_add_u32 v5, s30, v5\n"
    "s_barrier\n"
    "v_add_u32 v2, 0x8000, v2\n"
    "v_add_u32 v3, 0x8000, v3\n"
    "v_add_u32 v4, 0x8000, v4\n"
    "v_add_u32 v5, 0x8000, v5\n"
    "ds_read_b64_tr_b16 v[16:17], v2 offset:32768\n"
    "ds_read_b64_tr_b16 v[18:19], v2 offset:33792\n"
    "ds_read_b64_tr_b16 v[20:21], v3 offset:32768\n"
    "ds_read_b64_tr_b16 v[22:23], v3 offset:33792\n"
    "ds_read_b64_tr_b16 v[24:25], v4 offset:32768\n"
    "ds_read_b64_tr_b16 v[26:27], v4 offset:33792\n"
    "ds_read_b64_tr_b16 v[28:29], v5 offset:32768\n"
    "ds_read_b64_tr_b16 v[30:31], v5 offset:33792\n"
    "ds_read_b64_tr_b16 v[168:169], v2 offset:36864\n"
    "s_waitcnt vmcnt(18) lgkmcnt(7)\n"
    "v_mfma_f32_16x16x32_bf16 v[64:67], v[16:19], a[224:227], 0\n"
    "ds_read_b64_tr_b16 v[170:171], v2 offset:37888\n"
    "s_waitcnt vmcnt(17)\n"
    "v_mfma_f32_16x16x32_bf16 v[80:83], v[16:19], a[228:231], 0\n"
    "ds_read_b64_tr_b16 v[172:173], v3 offset:36864\n"
    "s_waitcnt lgkmcnt(7)\n"
    "v_mfma_f32_16x16x32_bf16 v[84:87], v[20:23], a[228:231], 0\n"
    "ds_read_b64_tr_b16 v[174:175], v3 offset:37888\n"
    "v_mfma_f32_16x16x32_bf16 v[68:71], v[20:23], a[224:227], 0\n"
    "ds_read_b64_tr_b16 v[176:177], v4 offset:36864\n"
    "s_waitcnt lgkmcnt(7)\n"
    "v_mfma_f32_16x16x32_bf16 v[72:75], v[24:27], a[224:227], 0\n"
    "ds_read_b64_tr_b16 v[178:179], v4 offset:37888\n"
    "v_mfma_f32_16x16x32_bf16 v[88:91], v[24:27], a[228:231], 0\n"
    "ds_read_b64_tr_b16 v[180:181], v5 offset:36864\n"
    "s_waitcnt lgkmcnt(7)\n"
    "v_mfma_f32_16x16x32_bf16 v[92:95], v[28:31], a[228:231], 0\n"
    "ds_read_b64_tr_b16 v[182:183], v5 offset:37888\n"
    "v_mfma_f32_16x16x32_bf16 v[76:79], v[28:31], a[224:227], 0\n"
    "v_add_u32 v2, 0xfffe8000, v2\n"
    "v_add_u32 v3, 0xfffe8000, v3\n"
    "v_add_u32 v4, 0xfffe8000, v4\n"
    "v_add_u32 v5, 0xfffe8000, v5\n"
    "ds_read_b64_tr_b16 v[16:17], v2\n"
    "s_waitcnt vmcnt(16) lgkmcnt(7)\n"
    "v_mfma_f32_16x16x32_bf16 v[64:67], v[168:171], a[232:235], v[64:67]\n"
    "ds_read_b64_tr_b16 v[18:19], v2 offset:1024\n"
    "s_waitcnt vmcnt(15)\n"
    "v_mfma_f32_16x16x32_bf16 v[80:83], v[168:171], a[236:239], v[80:83]\n"
    "ds_read_b64_tr_b16 v[20:21], v3\n"
    "s_waitcnt lgkmcnt(7)\n"
    "v_mfma_f32_16x16x32_bf16 v[84:87], v[172:175], a[236:239], v[84:87]\n"
    "ds_read_b64_tr_b16 v[22:23], v3 offset:1024\n"
    "v_mfma_f32_16x16x32_bf16 v[68:71], v[172:175], a[232:235], v[68:71]\n"
    "ds_read_b64_tr_b16 v[24:25], v4\n"
    "s_waitcnt lgkmcnt(7)\n"
    "v_mfma_f32_16x16x32_bf16 v[72:75], v[176:179], a[232:235], v[72:75]\n"
    "ds_read_b64_tr_b16 v[26:27], v4 offset:1024\n"
    "v_mfma_f32_16x16x32_bf16 v[88:91], v[176:179], a[236:239], v[88:91]\n"
    "ds_read_b64_tr_b16 v[28:29], v5\n"
    "s_waitcnt lgkmcnt(7)\n"
    "v_mfma_f32_16x16x32_bf16 v[92:95], v[180:183], a[236:239], v[92:95]\n"
    "ds_read_b64_tr_b16 v[30:31], v5 offset:1024\n"
    "v_mfma_f32_16x16x32_bf16 v[76:79], v[180:183], a[232:235], v[76:79]\n"
    "ds_read_b64_tr_b16 v[168:169], v2 offset:4096\n"
    "s_waitcnt vmcnt(14) lgkmcnt(7)\n"
    "v_mfma_f32_16x16x32_bf16 v[64:67], v[16:19], a[240:243], v[64:67]\n"
    "ds_read_b64_tr_b16 v[170:171], v2 offset:5120\n"
    "s_waitcnt vmcnt(13)\n"
    "v_mfma_f32_16x16x32_bf16 v[80:83], v[16:19], a[244:247], v[80:83]\n"
    "ds_read_b64_tr_b16 v[172:173], v3 offset:4096\n"
    "s_waitcnt lgkmcnt(7)\n"
    "v_mfma_f32_16x16x32_bf16 v[84:87], v[20:23], a[244:247], v[84:87]\n"
    "ds_read_b64_tr_b16 v[174:175], v3 offset:5120\n"
    "v_mfma_f32_16x16x32_bf16 v[68:71], v[20:23], a[240:243], v[68:71]\n"
    "ds_read_b64_tr_b16 v[176:177], v4 offset:4096\n"
    "s_waitcnt lgkmcnt(7)\n"
    "v_mfma_f32_16x16x32_bf16 v[72:75], v[24:27], a[240:243], v[72:75]\n"
    "ds_read_b64_tr_b16 v[178:179], v4 offset:5120\n"
    "v_mfma_f32_16x16x32_bf16 v[88:91], v[24:27], a[244:247], v[88:91]\n"
    "ds_read_b64_tr_b16 v[180:181], v5 offset:4096\n"
    "s_waitcnt lgkmcnt(7)\n"
    "v_mfma_f32_16x16x32_bf16 v[92:95], v[28:31], a[244:247], v[92:95]\n"
    "ds_read_b64_tr_b16 v[182:183], v5 offset:5120\n"
    "v_mfma_f32_16x16x32_bf16 v[76:79], v[28:31], a[240:243], v[76:79]\n"
    "s_barrier\n"
    "ds_read_b64_tr_b16 v[16:17], v2 offset:16384\n"
    "s_waitcnt vmcnt(12) lgkmcnt(7)\n"
    "v_mfma_f32_16x16x32_bf16 v[64:67], v[168:171], a[248:251], v[64:67]\n"
    "ds_read_b64_tr_b16 v[18:19], v2 offset:17408\n"
    "s_waitcnt vmcnt(11)\n"
    "v_mfma_f32_16x16x32_bf16 v[80:83], v[168:171], a[252:255], v[80:83]\n"
    "ds_read_b64_tr_b16 v[20:21], v3 offset:16384\n"
    "s_waitcnt lgkmcnt(7)\n"
    "v_mfma_f32_16x16x32_bf16 v[84:87], v[172:175], a[252:255], v[84:87]\n"
    "ds_read_b64_tr_b16 v[22:23], v3 offset:17408\n"
    "v_mfma_f32_16x16x32_bf16 v[68:71], v[172:175], a[248:251], v[68:71]\n"
    "ds_read_b64_tr_b16 v[24:25], v4 offset:16384\n"
    "s_waitcnt lgkmcnt(7)\n"
    "v_mfma_f32_16x16x32_bf16 v[72:75], v[176:179], a[248:251], v[72:75]\n"
    "ds_read_b64_tr_b16 v[26:27], v4 offset:17408\n"
    "v_mfma_f32_16x16x32_bf16 v[88:91], v[176:179], a[252:255], v[88:91]\n"
    "ds_read_b64_tr_b16 v[28:29], v5 offset:16384\n"
    "s_waitcnt lgkmcnt(7)\n"
    "v_mfma_f32_16x16x32_bf16 v[92:95], v[180:183], a[252:255], v[92:95]\n"
    "ds_read_b64_tr_b16 v[30:31], v5 offset:17408\n"
    "v_mfma_f32_16x16x32_bf16 v[76:79], v[180:183], a[248:251], v[76:79]\n"
    "ds_read_b64_tr_b16 v[168:169], v2 offset:20480\n"
    "s_waitcnt vmcnt(10) lgkmcnt(7)\n"
    "v_mfma_f32_16x16x32_bf16 v[64:67], v[16:19], a[0:3], v[64:67]\n"
    "ds_read_b64_tr_b16 v[170:171], v2 offset:21504\n"
    "s_waitcnt vmcnt(9)\n"
    "v_mfma_f32_16x16x32_bf16 v[80:83], v[16:19], a[4:7], v[80:83]\n"
    "ds_read_b64_tr_b16 v[172:173], v3 offset:20480\n"
    "s_waitcnt lgkmcnt(7)\n"
    "v_mfma_f32_16x16x32_bf16 v[84:87], v[20:23], a[4:7], v[84:87]\n"
    "ds_read_b64_tr_b16 v[174:175], v3 offset:21504\n"
    "v_mfma_f32_16x16x32_bf16 v[68:71], v[20:23], a[0:3], v[68:71]\n"
    "ds_read_b64_tr_b16 v[176:177], v4 offset:20480\n"
    "s_waitcnt lgkmcnt(7)\n"
    "v_mfma_f32_16x16x32_bf16 v[72:75], v[24:27], a[0:3], v[72:75]\n"
    "ds_read_b64_tr_b16 v[178:179], v4 offset:21504\n"
    "v_mfma_f32_16x16x32_bf16 v[88:91], v[24:27], a[4:7], v[88:91]\n"
    "ds_read_b64_tr_b16 v[180:181], v5 offset:20480\n"
    "s_waitcnt lgkmcnt(7)\n"
    "v_mfma_f32_16x16x32_bf16 v[92:95], v[28:31], a[4:7], v[92:95]\n"
    "ds_read_b64_tr_b16 v[182:183], v5 offset:21504\n"
    "v_mfma_f32_16x16x32_bf16 v[76:79], v[28:31], a[0:3], v[76:79]\n"
    "ds_read_b64_tr_b16 v[16:17], v2 offset:32768\n"
    "s_xor_b32 s87, s68, 1\n"
    "s_lshl_b32 s87, s87, 1\n"
    "s_add_u32 s87, s87, s76\n"
    "s_lshl_b32 s87, s87, 1\n"
    "s_add_u32 s87, s87, s75\n"
    "s_lshl_b32 s87, s87, 12\n"
    "s_add_u32 s87, s87, 0xa58900\n"
    "buffer_load_dwordx4 v[128:131], v8, s[48:51], s87 offen sc1\n"
    "buffer_load_dwordx4 v[132:135], v8, s[48:51], s87 offen offset:1024 sc1\n"
    "buffer_load_dwordx4 v[136:139], v8, s[48:51], s87 offen offset:2048 sc1\n"
    "buffer_load_dwordx4 v[140:143], v8, s[48:51], s87 offen offset:3072 sc1\n"
    "s_waitcnt vmcnt(12) lgkmcnt(7)\n"
    "v_mfma_f32_16x16x32_bf16 v[64:67], v[168:171], a[8:11], v[64:67]\n"
    "ds_read_b64_tr_b16 v[18:19], v2 offset:33792\n"
    "s_waitcnt vmcnt(11)\n"
    "v_mfma_f32_16x16x32_bf16 v[80:83], v[168:171], a[12:15], v[80:83]\n"
    "ds_read_b64_tr_b16 v[20:21], v3 offset:32768\n"
    "s_waitcnt lgkmcnt(7)\n"
    "v_mfma_f32_16x16x32_bf16 v[84:87], v[172:175], a[12:15], v[84:87]\n"
    "ds_read_b64_tr_b16 v[22:23], v3 offset:33792\n"
    "v_mfma_f32_16x16x32_bf16 v[68:71], v[172:175], a[8:11], v[68:71]\n"
    "ds_read_b64_tr_b16 v[24:25], v4 offset:32768\n"
    "s_waitcnt lgkmcnt(7)\n"
    "v_mfma_f32_16x16x32_bf16 v[72:75], v[176:179], a[8:11], v[72:75]\n"
    "ds_read_b64_tr_b16 v[26:27], v4 offset:33792\n"
    "v_mfma_f32_16x16x32_bf16 v[88:91], v[176:179], a[12:15], v[88:91]\n"
    "ds_read_b64_tr_b16 v[28:29], v5 offset:32768\n"
    "s_waitcnt lgkmcnt(7)\n"
    "v_mfma_f32_16x16x32_bf16 v[92:95], v[180:183], a[12:15], v[92:95]\n"
    "ds_read_b64_tr_b16 v[30:31], v5 offset:33792\n"
    "v_mfma_f32_16x16x32_bf16 v[76:79], v[180:183], a[8:11], v[76:79]\n"
    "ds_read_b64_tr_b16 v[168:169], v2 offset:36864\n"
    "s_waitcnt vmcnt(10) lgkmcnt(7)\n"
    "v_mfma_f32_16x16x32_bf16 v[64:67], v[16:19], a[16:19], v[64:67]\n"
    "ds_read_b64_tr_b16 v[170:171], v2 offset:37888\n"
    "s_waitcnt vmcnt(9)\n"
    "v_mfma_f32_16x16x32_bf16 v[80:83], v[16:19], a[20:23], v[80:83]\n"
    "ds_read_b64_tr_b16 v[172:173], v3 offset:36864\n"
    "s_waitcnt lgkmcnt(7)\n"
    "v_mfma_f32_16x16x32_bf16 v[84:87], v[20:23], a[20:23], v[84:87]\n"
    "ds_read_b64_tr_b16 v[174:175], v3 offset:37888\n"
    "v_mfma_f32_16x16x32_bf16 v[68:71], v[20:23], a[16:19], v[68:71]\n"
    "ds_read_b64_tr_b16 v[176:177], v4 offset:36864\n"
    "s_waitcnt lgkmcnt(7)\n"
    "v_mfma_f32_16x16x32_bf16 v[72:75], v[24:27], a[16:19], v[72:75]\n"
    "ds_read_b64_tr_b16 v[178:179], v4 offset:37888\n"
    "v_mfma_f32_16x16x32_bf16 v[88:91], v[24:27], a[20:23], v[88:91]\n"
    "ds_read_b64_tr_b16 v[180:181], v5 offset:36864\n"
    "s_waitcnt lgkmcnt(7)\n"
    "v_mfma_f32_16x16x32_bf16 v[92:95], v[28:31], a[20:23], v[92:95]\n"
    "ds_read_b64_tr_b16 v[182:183], v5 offset:37888\n"
    "v_mfma_f32_16x16x32_bf16 v[76:79], v[28:31], a[16:19], v[76:79]\n"
    "s_waitcnt vmcnt(8) lgkmcnt(6)\n"
    "v_mfma_f32_16x16x32_bf16 v[64:67], v[168:171], a[24:27], v[64:67]\n"
    "s_waitcnt vmcnt(7)\n"
    "v_mfma_f32_16x16x32_bf16 v[80:83], v[168:171], a[28:31], v[80:83]\n"
    "s_waitcnt lgkmcnt(4)\n"
    "v_mfma_f32_16x16x32_bf16 v[84:87], v[172:175], a[28:31], v[84:87]\n"
    "v_mfma_f32_16x16x32_bf16 v[68:71], v[172:175], a[24:27], v[68:71]\n"
    "s_waitcnt lgkmcnt(2)\n"
    "v_mfma_f32_16x16x32_bf16 v[72:75], v[176:179], a[24:27], v[72:75]\n"
    "v_mfma_f32_16x16x32_bf16 v[88:91], v[176:179], a[28:31], v[88:91]\n"
    "s_waitcnt lgkmcnt(0)\n"
    "v_mfma_f32_16x16x32_bf16 v[92:95], v[180:183], a[28:31], v[92:95]\n"
    "v_mfma_f32_16x16x32_bf16 v[76:79], v[180:183], a[24:27], v[76:79]\n"
    "s_mul_i32 s30, s76, 0x3000\n"
    "s_add_u32 s30, s30, 0xc000\n"
    "v_add_u32 v192, s30, v8\n"
    "s_nop 11\n"
    "s_cmp_lg_u32 s75, 0\n"
    "s_cbranch_scc1 LA_else_22%=\n"
    "ds_write_b128 v192, v[48:51]\n"
    "ds_write_b128 v192, v[52:55] offset:1024\n"
    "ds_write_b128 v192, v[56:59] offset:2048\n"
    "ds_write_b128 v192, v[60:63] offset:3072\n"
    "ds_write_b128 v192, v[80:83] offset:4096\n"
    "ds_write_b128 v192, v[84:87] offset:5120\n"
    "ds_write_b128 v192, v[88:91] offset:6144\n"
    "ds_write_b128 v192, v[92:95] offset:7168\n"
    "s_waitcnt lgkmcnt(0)\n"
    "s_barrier\n"
    "ds_read_b128 v[80:83], v192 offset:8192\n"
    "ds_read_b128 v[84:87], v192 offset:9216\n"
    "ds_read_b128 v[88:91], v192 offset:10240\n"
    "ds_read_b128 v[92:95], v192 offset:11264\n"
    "s_branch LA_join_23%=\n"
    "LA_else_22%=:\n"
    "ds_write_b128 v192, v[64:67] offset:8192\n"
    "ds_write_b128 v192, v[68:71] offset:9216\n"
    "ds_write_b128 v192, v[72:75] offset:10240\n"
    "ds_write_b128 v192, v[76:79] offset:11264\n"
    "s_waitcnt lgkmcnt(0)\n"
    "s_barrier\n"
    "ds_read_b128 v[32:35], v192\n"
    "ds_read_b128 v[36:39], v192 offset:1024\n"
    "ds_read_b128 v[40:43], v192 offset:2048\n"
    "ds_read_b128 v[44:47], v192 offset:3072\n"
    "ds_read_b128 v[64:67], v192 offset:4096\n"
    "ds_read_b128 v[68:71], v192 offset:5120\n"
    "ds_read_b128 v[72:75], v192 offset:6144\n"
    "ds_read_b128 v[76:79], v192 offset:7168\n"
    "LA_join_23%=:\n"
    "s_waitcnt vmcnt(6)\n"
    "v_and_b32 v193, 0xffff0000, v152\n"
    "v_rcp_f32 v193, v193\n"
    "s_waitcnt vmcnt(5)\n"
    "v_permlane16_swap_b32 v144, v146\n"
    "v_mul_f32 v193, v193, v193\n"
    "v_mul_f32 v194, 0xbfb8aa3b, v193\n"
    "v_permlane16_swap_b32 v145, v147\n"
    "s_waitcnt vmcnt(4)\n"
    "v_permlane16_swap_b32 v148, v150\n"
    "v_permlane16_swap_b32 v149, v151\n"
    "s_waitcnt vmcnt(3)\n"
    "v_xor_b32 v195, s72, v129\n"
    "v_bitop3_b32 v195, v195, v131, s72 bitop3:0xf6\n"
    "s_waitcnt vmcnt(2)\n"
    "v_bitop3_b32 v195, v195, v133, s72 bitop3:0xf6\n"
    "v_bitop3_b32 v195, v195, v135, s72 bitop3:0xf6\n"
    "s_waitcnt vmcnt(1)\n"
    "v_bitop3_b32 v195, v195, v137, s72 bitop3:0xf6\n"
    "v_bitop3_b32 v195, v195, v139, s72 bitop3:0xf6\n"
    "s_waitcnt vmcnt(0)\n"
    "v_bitop3_b32 v195, v195, v141, s72 bitop3:0xf6\n"
    "v_bitop3_b32 v195, v195, v143, s72 bitop3:0xf6\n"
    "v_cmp_ne_u32 s[88:89], 0, v195\n"
    "s_cmp_lg_u64 s[88:89], 0\n"
    "s_cbranch_scc1 LA_slow_24%=\n"
    "LA_back_25%=:\n"
    "s_waitcnt lgkmcnt(0)\n"
    "v_lshlrev_b32 v196, 16, v128\n"
    "v_add_f32 v32, v32, v196\n"
    "v_add_f32 v64, v64, v80\n"
    "v_mul_f32 v196, v194, v32\n"
    "v_exp_f32 v196, v196\n"
    "v_lshlrev_b32 v197, 16, v144\n"
    "v_mul_f32 v64, v64, v32\n"
    "v_add_f32 v196, 1.0, v196\n"
    "v_rcp_f32 v196, v196\n"
    "v_mul_f32 v64, v64, v193\n"
    "v_fma_f32 v32, v64, v196, v197\n"
    "v_and_b32 v198, 0xffff0000, v128\n"
    "v_add_f32 v33, v33, v198\n"
    "v_add_f32 v65, v65, v81\n"
    "v_mul_f32 v198, v194, v33\n"
    "v_exp_f32 v198, v198\n"
    "v_and_b32 v199, 0xffff0000, v144\n"
    "v_mul_f32 v65, v65, v33\n"
    "v_add_f32 v198, 1.0, v198\n"
    "v_rcp_f32 v198, v198\n"
    "v_mul_f32 v65, v65, v193\n"
    "v_fma_f32 v33, v65, v198, v199\n"
    "v_lshlrev_b32 v200, 16, v130\n"
    "v_add_f32 v34, v34, v200\n"
    "v_add_f32 v66, v66, v82\n"
    "v_mul_f32 v200, v194, v34\n"
    "v_exp_f32 v200, v200\n"
    "v_lshlrev_b32 v201, 16, v145\n"
    "v_mul_f32 v66, v66, v34\n"
    "v_add_f32 v200, 1.0, v200\n"
    "v_rcp_f32 v200, v200\n"
    "v_mul_f32 v66, v66, v193\n"
    "v_fma_f32 v34, v66, v200, v201\n"
    "v_and_b32 v202, 0xffff0000, v130\n"
    "v_add_f32 v35, v35, v202\n"
    "v_add_f32 v67, v67, v83\n"
    "v_mul_f32 v202, v194, v35\n"
    "v_exp_f32 v202, v202\n"
    "v_and_b32 v203, 0xffff0000, v145\n"
    "v_mul_f32 v67, v67, v35\n"
    "v_add_f32 v202, 1.0, v202\n"
    "v_rcp_f32 v202, v202\n"
    "v_mul_f32 v67, v67, v193\n"
    "v_fma_f32 v35, v67, v202, v203\n"
    "v_lshlrev_b32 v204, 16, v132\n"
    "v_add_f32 v36, v36, v204\n"
    "v_add_f32 v68, v68, v84\n"
    "v_mul_f32 v204, v194, v36\n"
    "v_exp_f32 v204, v204\n"
    "v_lshlrev_b32 v205, 16, v146\n"
    "v_mul_f32 v68, v68, v36\n"
    "v_add_f32 v204, 1.0, v204\n"
    "v_rcp_f32 v204, v204\n"
    "v_mul_f32 v68, v68, v193\n"
    "v_fma_f32 v36, v68, v204, v205\n"
    "v_and_b32 v206, 0xffff0000, v132\n"
    "v_add_f32 v37, v37, v206\n"
    "v_add_f32 v69, v69, v85\n"
    "v_mul_f32 v206, v194, v37\n"
    "v_exp_f32 v206, v206\n"
    "v_and_b32 v207, 0xffff0000, v146\n"
    "v_mul_f32 v69, v69, v37\n"
    "v_add_f32 v206, 1.0, v206\n"
    "v_rcp_f32 v206, v206\n"
    "v_mul_f32 v69, v69, v193\n"
    "v_fma_f32 v37, v69, v206, v207\n"
    "v_lshlrev_b32 v208, 16, v134\n"
    "v_add_f32 v38, v38, v208\n"
    "v_add_f32 v70, v70, v86\n"
    "v_mul_f32 v208, v194, v38\n"
    "v_exp_f32 v208, v208\n"
    "v_lshlrev_b32 v209, 16, v147\n"
    "v_mul_f32 v70, v70, v38\n"
    "v_add_f32 v208, 1.0, v208\n"
    "v_rcp_f32 v208, v208\n"
    "v_mul_f32 v70, v70, v193\n"
    "v_fma_f32 v38, v70, v208, v209\n"
    "v_and_b32 v210, 0xffff0000, v134\n"
    "v_add_f32 v39, v39, v210\n"
    "v_add_f32 v71, v71, v87\n"
    "v_mul_f32 v210, v194, v39\n"
    "v_exp_f32 v210, v210\n"
    "v_and_b32 v211, 0xffff0000, v147\n"
    "v_mul_f32 v71, v71, v39\n"
    "v_add_f32 v210, 1.0, v210\n"
    "v_rcp_f32 v210, v210\n"
    "v_mul_f32 v71, v71, v193\n"
    "v_fma_f32 v39, v71, v210, v211\n"
    "v_lshlrev_b32 v212, 16, v136\n"
    "v_add_f32 v40, v40, v212\n"
    "v_add_f32 v72, v72, v88\n"
    "v_mul_f32 v212, v194, v40\n"
    "v_exp_f32 v212, v212\n"
    "v_lshlrev_b32 v213, 16, v148\n"
    "v_mul_f32 v72, v72, v40\n"
    "v_add_f32 v212, 1.0, v212\n"
    "v_rcp_f32 v212, v212\n"
    "v_mul_f32 v72, v72, v193\n"
    "v_fma_f32 v40, v72, v212, v213\n"
    "v_and_b32 v214, 0xffff0000, v136\n"
    "v_add_f32 v41, v41, v214\n"
    "v_add_f32 v73, v73, v89\n"
    "v_mul_f32 v214, v194, v41\n"
    "v_exp_f32 v214, v214\n"
    "v_and_b32 v215, 0xffff0000, v148\n"
    "v_mul_f32 v73, v73, v41\n"
    "v_add_f32 v214, 1.0, v214\n"
    "v_rcp_f32 v214, v214\n"
    "v_mul_f32 v73, v73, v193\n"
    "v_fma_f32 v41, v73, v214, v215\n"
    "v_lshlrev_b32 v216, 16, v138\n"
    "v_add_f32 v42, v42, v216\n"
    "v_add_f32 v74, v74, v90\n"
    "v_mul_f32 v216, v194, v42\n"
    "v_exp_f32 v216, v216\n"
    "v_lshlrev_b32 v217, 16, v149\n"
    "v_mul_f32 v74, v74, v42\n"
    "v_add_f32 v216, 1.0, v216\n"
    "v_rcp_f32 v216, v216\n"
    "v_mul_f32 v74, v74, v193\n"
    "v_fma_f32 v42, v74, v216, v217\n"
    "v_and_b32 v218, 0xffff0000, v138\n"
    "v_add_f32 v43, v43, v218\n"
    "v_add_f32 v75, v75, v91\n"
    "v_mul_f32 v218, v194, v43\n"
    "v_exp_f32 v218, v218\n"
    "v_and_b32 v219, 0xffff0000, v149\n"
    "v_mul_f32 v75, v75, v43\n"
    "v_add_f32 v218, 1.0, v218\n"
    "v_rcp_f32 v218, v218\n"
    "v_mul_f32 v75, v75, v193\n"
    "v_fma_f32 v43, v75, v218, v219\n"
    "v_lshlrev_b32 v220, 16, v140\n"
    "v_add_f32 v44, v44, v220\n"
    "v_add_f32 v76, v76, v92\n"
    "v_mul_f32 v220, v194, v44\n"
    "v_exp_f32 v220, v220\n"
    "v_lshlrev_b32 v221, 16, v150\n"
    "v_mul_f32 v76, v76, v44\n"
    "v_add_f32 v220, 1.0, v220\n"
    "v_rcp_f32 v220, v220\n"
    "v_mul_f32 v76, v76, v193\n"
    "v_fma_f32 v44, v76, v220, v221\n"
    "v_and_b32 v222, 0xffff0000, v140\n"
    "v_add_f32 v45, v45, v222\n"
    "v_add_f32 v77, v77, v93\n"
    "v_mul_f32 v222, v194, v45\n"
    "v_exp_f32 v222, v222\n"
    "v_and_b32 v223, 0xffff0000, v150\n"
    "v_mul_f32 v77, v77, v45\n"
    "v_add_f32 v222, 1.0, v222\n"
    "v_rcp_f32 v222, v222\n"
    "v_mul_f32 v77, v77, v193\n"
    "v_fma_f32 v45, v77, v222, v223\n"
    "v_lshlrev_b32 v224, 16, v142\n"
    "v_add_f32 v46, v46, v224\n"
    "v_add_f32 v78, v78, v94\n"
    "v_mul_f32 v224, v194, v46\n"
    "v_exp_f32 v224, v224\n"
    "v_lshlrev_b32 v225, 16, v151\n"
    "v_mul_f32 v78, v78, v46\n"
    "v_add_f32 v224, 1.0, v224\n"
    "v_rcp_f32 v224, v224\n"
    "v_mul_f32 v78, v78, v193\n"
    "v_fma_f32 v46, v78, v224, v225\n"
    "v_and_b32 v226, 0xffff0000, v142\n"
    "v_add_f32 v47, v47, v226\n"
    "v_add_f32 v79, v79, v95\n"
    "v_mul_f32 v226, v194, v47\n"
    "v_exp_f32 v226, v226\n"
    "v_and_b32 v227, 0xffff0000, v151\n"
    "v_mul_f32 v79, v79, v47\n"
    "v_add_f32 v226, 1.0, v226\n"
    "v_rcp_f32 v226, v226\n"
    "v_mul_f32 v79, v79, v193\n"
    "v_fma_f32 v47, v79, v226, v227\n"
    "v_cvt_pk_bf16_f32 v228, v32, v33\n"
    "v_cvt_pk_bf16_f32 v229, v34, v35\n"
    "v_cvt_pk_bf16_f32 v230, v36, v37\n"
    "v_cvt_pk_bf16_f32 v231, v38, v39\n"
    "v_cvt_pk_bf16_f32 v232, v40, v41\n"
    "v_permlane16_swap_b32 v228, v230\n"
    "v_permlane16_swap_b32 v229, v231\n"
    "buffer_store_dwordx4 v[228:231], v154, s[56:59], 0 offen\n"
    "v_cvt_pk_bf16_f32 v233, v42, v43\n"
    "v_cvt_pk_bf16_f32 v234, v44, v45\n"
    "v_cvt_pk_bf16_f32 v235, v46, v47\n"
    "s_nop 0\n"
    "v_permlane16_swap_b32 v232, v234\n"
    "v_permlane16_swap_b32 v233, v235\n"
    "buffer_store_dwordx4 v[232:235], v154, s[56:59], 0 offen offset:64\n"
    "s_or_b32 s30, s68, s69\n"
    "s_cbranch_scc1 LA_nogen_26%=\n"
    "v_mov_b32 v249, 0\n"
    "v_mov_b32 v250, s72\n"
    "s_mov_b32 s31, 0xa58800\n"
    "buffer_store_dword v250, v249, s[48:51], s31 offen sc1\n"
    "LA_nogen_26%=:\n"
    "s_branch L_end%=\n"
    "L_B%=:\n"
    "v_lshrrev_b32 v10, 3, v0\n"
    "s_mul_i32 s30, s69, 0xc000\n"
    "s_mov_b32 s31, 0xc00\n"
    "v_mul_lo_u32 v10, v10, s31\n"
    "v_bfe_u32 v251, v0, 4, 1\n"
    "v_lshlrev_b32 v251, 2, v251\n"
    "v_bitop3_b32 v251, v0, 7, v251 bitop3:0x6a\n"
    "v_lshl_add_u32 v10, v251, 4, v10\n"
    "v_add_u32 v10, s30, v10\n"
    "v_add_u32 v11, 0x5c00, v10\n"
    "s_waitcnt lgkmcnt(0)\n"
    "s_lshl_b32 s30, s70, 17\n"
    "s_add_u32 s36, s12, s30\n"
    "s_addc_u32 s37, s13, 0\n"
    "s_sub_u32 s38, 0x200000, s30\n"
    "s_mov_b32 s39, 0x20000\n"
    "s_mov_b32 s48, s28\n"
    "s_mov_b32 s49, s29\n"
    "s_mov_b32 s50, 0xe98900\n"
    "s_mov_b32 s51, 0x20000\n"
    "s_add_u32 s82, s71, 8\n"
    "s_lshl_b32 s85, s82, 13\n"
    "s_add_u32 s85, s85, s81\n"
    "v_lshlrev_b32 v232, 4, v0\n"
    "v_mov_b32 v233, 0\n"
    "s_mov_b32 s86, 0xa58800\n"
    "buffer_load_dword v234, v233, s[48:51], s86 offen sc1\n"
    "buffer_load_dwordx4 v[192:195], v232, s[36:39], s85 offen\n"
    "buffer_load_dwordx4 v[196:199], v232, s[36:39], s85 offen offset:1024\n"
    "s_lshl_b32 s30, s71, 7\n"
    "s_add_u32 s44, s14, s30\n"
    "s_addc_u32 s45, s15, 0\n"
    "s_sub_u32 s46, 0x180000, s30\n"
    "s_mov_b32 s47, 0x20000\n"
    "s_add_u32 s30, s30, 0x400\n"
    "s_add_u32 s40, s14, s30\n"
    "s_addc_u32 s41, s15, 0\n"
    "s_sub_u32 s42, 0x180000, s30\n"
    "s_mov_b32 s43, 0x20000\n"
    "s_add_u32 m0, s80, 0\n"
    "s_nop 0\n"
    "buffer_load_dwordx4 v6, s[36:39], s60 offen lds\n"
    "buffer_load_dwordx4 v7, s[36:39], s60 offen offset:1024 lds\n"
    "buffer_load_dwordx4 v8, s[36:39], s60 offen offset:2048 lds\n"
    "buffer_load_dwordx4 v9, s[36:39], s60 offen offset:3072 lds\n"
    "s_mul_i32 s74, s60, 0x600\n"
    "s_add_u32 m0, s80, 0x4000\n"
    "s_nop 0\n"
    "buffer_load_dwordx4 v10, s[40:43], s74 offen lds\n"
    "buffer_load_dwordx4 v11, s[40:43], s74 offen offset:1024 lds\n"
    "buffer_load_dwordx4 v10, s[44:47], s74 offen offset:2048 lds\n"
    "buffer_load_dwordx4 v11, s[44:47], s74 offen offset:3072 lds\n"
    "s_getpc_b64 s[88:89]\n"
    "LB_pf%=:\n"
    "s_bfe_u32 s85, s68, 0x40003\n"
    "s_lshl_b32 s85, s85, 2\n"
    "s_add_u32 s85, s85, s69\n"
    "s_lshl_b32 s85, s85, 9\n"
    "v_lshl_add_u32 v235, v0, 3, s85\n"
    "s_mov_b32 s85, L_end%=-LB_pf%=-8\n"
    "v_min_u32 v235, s85, v235\n"
    "global_load_dwordx2 v[236:237], v235, s[88:89]\n"
    "s_add_u32 m0, s80, 0x8000\n"
    "s_nop 0\n"
    "buffer_load_dwordx4 v6, s[36:39], s61 offen lds\n"
    "buffer_load_dwordx4 v7, s[36:39], s61 offen offset:1024 lds\n"
    "buffer_load_dwordx4 v8, s[36:39], s61 offen offset:2048 lds\n"
    "buffer_load_dwordx4 v9, s[36:39], s61 offen offset:3072 lds\n"
    "s_mul_i32 s74, s61, 0x600\n"
    "s_add_u32 m0, s80, 0xc000\n"
    "s_nop 0\n"
    "buffer_load_dwordx4 v10, s[40:43], s74 offen lds\n"
    "buffer_load_dwordx4 v11, s[40:43], s74 offen offset:1024 lds\n"
    "buffer_load_dwordx4 v10, s[44:47], s74 offen offset:2048 lds\n"
    "buffer_load_dwordx4 v11, s[44:47], s74 offen offset:3072 lds\n"
    "s_add_u32 m0, s80, 0x10000\n"
    "s_nop 0\n"
    "buffer_load_dwordx4 v6, s[36:39], s62 offen lds\n"
    "buffer_load_dwordx4 v7, s[36:39], s62 offen offset:1024 lds\n"
    "buffer_load_dwordx4 v8, s[36:39], s62 offen offset:2048 lds\n"
    "buffer_load_dwordx4 v9, s[36:39], s62 offen offset:3072 lds\n"
    "s_mul_i32 s74, s62, 0x600\n"
    "s_add_u32 m0, s80, 0x14000\n"
    "s_nop 0\n"
    "buffer_load_dwordx4 v10, s[40:43], s74 offen lds\n"
    "buffer_load_dwordx4 v11, s[40:43], s74 offen offset:1024 lds\n"
    "buffer_load_dwordx4 v10, s[44:47], s74 offen offset:2048 lds\n"
    "buffer_load_dwordx4 v11, s[44:47], s74 offen offset:3072 lds\n"
    "s_add_u32 m0, s80, 0x18000\n"
    "s_nop 0\n"
    "buffer_load_dwordx4 v6, s[36:39], s63 offen lds\n"
    "buffer_load_dwordx4 v7, s[36:39], s63 offen offset:1024 lds\n"
    "buffer_load_dwordx4 v8, s[36:39], s63 offen offset:2048 lds\n"
    "buffer_load_dwordx4 v9, s[36:39], s63 offen offset:3072 lds\n"
    "s_mul_i32 s74, s63, 0x600\n"
    "s_add_u32 m0, s80, 0x1c000\n"
    "s_nop 0\n"
    "buffer_load_dwordx4 v10, s[40:43], s74 offen lds\n"
    "buffer_load_dwordx4 v11, s[40:43], s74 offen offset:1024 lds\n"
    "buffer_load_dwordx4 v10, s[44:47], s74 offen offset:2048 lds\n"
    "buffer_load_dwordx4 v11, s[44:47], s74 offen offset:3072 lds\n"
    "s_add_u32 m0, s80, 0x20000\n"
    "s_nop 0\n"
    "buffer_load_dwordx4 v6, s[36:39], s64 offen lds\n"
    "buffer_load_dwordx4 v7, s[36:39], s64 offen offset:1024 lds\n"
    "buffer_load_dwordx4 v8, s[36:39], s64 offen offset:2048 lds\n"
    "buffer_load_dwordx4 v9, s[36:39], s64 offen offset:3072 lds\n"
    "s_lshl_b32 s85, s71, 11\n"
    "s_lshl_b32 s86, s76, 10\n"
    "s_add_u32 s85, s85, s86\n"
    "s_add_u32 s56, s16, 0\n"
    "s_addc_u32 s57, s17, 0\n"
    "s_sub_u32 s58, 0x4000, 0\n"
    "s_mov_b32 s59, 0x20000\n"
    "s_mul_i32 s86, s69, 0xc00\n"
    "s_add_u32 m0, s86, 0x24000\n"
    "s_nop 0\n"
    "buffer_load_dwordx4 v232, s[56:59], s85 offen lds\n"
    "s_and_b32 s77, s68, 1\n"
    "s_bfe_u32 s78, s68, 0x20001\n"
    "s_lshl_b32 s78, s78, 2\n"
    "s_bfe_u32 s85, s68, 0x20003\n"
    "s_add_u32 s78, s78, s85\n"
    "s_lshr_b32 s79, s68, 5\n"
    "s_mov_b32 s73, 0x20000\n"
    "s_mov_b32 s92, 0xffff0000\n"
    "s_mov_b32 s93, 0xffff0000\n"
    "v_lshrrev_b32 v12, 3, v0\n"
    "s_mul_i32 s30, s69, 0xe000\n"
    "s_mov_b32 s31, 0xe00\n"
    "v_mul_lo_u32 v12, v12, s31\n"
    "v_bfe_u32 v251, v0, 4, 1\n"
    "v_lshlrev_b32 v251, 2, v251\n"
    "v_bitop3_b32 v251, v0, 7, v251 bitop3:0x6a\n"
    "v_lshl_add_u32 v12, v251, 4, v12\n"
    "v_add_u32 v12, s30, v12\n"
    "v_add_u32 v13, 0x6c00, v12\n"
    "v_bfe_u32 v1, v0, 3, 1\n"
    "v_xor_b32 v1, s76, v1\n"
    "v_lshlrev_b32 v1, 6, v1\n"
    "v_bfe_u32 v251, v0, 4, 1\n"
    "v_xor_b32 v251, s75, v251\n"
    "v_lshl_add_u32 v1, v251, 5, v1\n"
    "v_and_b32 v251, 3, v0\n"
    "v_lshl_add_u32 v1, v251, 3, v1\n"
    "v_bfe_u32 v251, v0, 2, 2\n"
    "v_lshl_add_u32 v1, v251, 7, v1\n"
    "v_lshrrev_b32 v251, 5, v0\n"
    "v_lshl_add_u32 v1, v251, 10, v1\n"
    "v_and_b32 v168, 31, v0\n"
    "v_lshrrev_b32 v169, 5, v0\n"
    "v_bfe_u32 v170, v0, 1, 3\n"
    "v_xor_b32 v170, v169, v170\n"
    "s_lshl_b32 s85, s75, 13\n"
    "v_lshl_add_u32 v171, v168, 7, s85\n"
    "v_xor_b32 v172, 0, v170\n"
    "v_lshl_add_u32 v2, v172, 4, v171\n"
    "v_xor_b32 v172, 2, v170\n"
    "v_lshl_add_u32 v3, v172, 4, v171\n"
    "v_xor_b32 v172, 4, v170\n"
    "v_lshl_add_u32 v4, v172, 4, v171\n"
    "v_xor_b32 v172, 6, v170\n"
    "v_lshl_add_u32 v5, v172, 4, v171\n"
    "v_bfe_u32 v170, v0, 4, 1\n"
    "s_mov_b32 s85, 0x12000\n"
    "v_mul_lo_u32 v170, v170, s85\n"
    "v_lshl_add_u32 v170, v169, 8, v170\n"
    "v_and_b32 v171, 15, v0\n"
    "v_lshl_add_u32 v14, v171, 4, v170\n"
    "s_lshl_b32 s85, s70, 1\n"
    "s_add_u32 s85, s85, s75\n"
    "s_mul_i32 s83, s85, 0x48000\n"
    "s_lshl_b32 s85, s85, 9\n"
    "s_lshl_b32 s86, s82, 5\n"
    "s_add_u32 s85, s85, s86\n"
    "s_lshl_b32 s86, s76, 2\n"
    "s_add_u32 s85, s85, s86\n"
    "s_add_u32 s84, s85, 0xa54000\n"
    "s_waitcnt vmcnt(40)\n"
    "v_readfirstlane_b32 s72, v234\n"
    "s_add_u32 s72, s72, 1\n"
    "s_waitcnt vmcnt(39)\n"
    "v_lshlrev_b32 v200, 16, v192\n"
    "v_and_b32 v201, 0xffff0000, v192\n"
    "v_lshlrev_b32 v202, 16, v193\n"
    "v_and_b32 v203, 0xffff0000, v193\n"
    "v_lshlrev_b32 v204, 16, v194\n"
    "v_and_b32 v205, 0xffff0000, v194\n"
    "v_lshlrev_b32 v206, 16, v195\n"
    "v_and_b32 v207, 0xffff0000, v195\n"
    "s_waitcnt vmcnt(38)\n"
    "v_lshlrev_b32 v208, 16, v196\n"
    "v_and_b32 v209, 0xffff0000, v196\n"
    "v_lshlrev_b32 v210, 16, v197\n"
    "v_and_b32 v211, 0xffff0000, v197\n"
    "v_lshlrev_b32 v212, 16, v198\n"
    "v_and_b32 v213, 0xffff0000, v198\n"
    "v_lshlrev_b32 v214, 16, v199\n"
    "v_and_b32 v215, 0xffff0000, v199\n"
    "v_add_f32 v216, v200, v201\n"
    "v_add_f32 v216, v216, v202\n"
    "v_add_f32 v216, v216, v203\n"
    "v_add_f32 v216, v216, v204\n"
    "v_add_f32 v216, v216, v205\n"
    "v_add_f32 v216, v216, v206\n"
    "v_add_f32 v216, v216, v207\n"
    "v_add_f32 v217, v208, v209\n"
    "v_add_f32 v217, v217, v210\n"
    "v_add_f32 v217, v217, v211\n"
    "v_add_f32 v217, v217, v212\n"
    "v_add_f32 v217, v217, v213\n"
    "v_add_f32 v217, v217, v214\n"
    "v_add_f32 v217, v217, v215\n"
    "s_nop 1\n"
    "v_permlane32_swap_b32 v216, v217\n"
    "v_add_f32 v216, v216, v217\n"
    "s_nop 1\n"
    "v_add_f32_dpp v216, v216, v216 row_ror:8 row_mask:0xf bank_mask:0xf\n"
    "s_nop 1\n"
    "v_add_f32_dpp v216, v216, v216 row_ror:4 row_mask:0xf bank_mask:0xf\n"
    "s_nop 1\n"
    "v_add_f32_dpp v216, v216, v216 row_ror:2 row_mask:0xf bank_mask:0xf\n"
    "s_nop 1\n"
    "v_add_f32_dpp v216, v216, v216 row_ror:1 row_mask:0xf bank_mask:0xf\n"
    "v_mov_b32 v218, v216\n"
    "s_nop 1\n"
    "v_permlane16_swap_b32 v216, v218\n"
    "v_add_f32 v216, v216, v218\n"
    "v_mul_f32 v216, 0x3b000000, v216\n"
    "v_mov_b32 v219, v216\n"
    "v_mov_b32 v217, v216\n"
    "s_nop 1\n"
    "v_permlane32_swap_b32 v216, v217\n"
    "v_sub_f32 v200, v200, v216\n"
    "v_sub_f32 v201, v201, v216\n"
    "v_sub_f32 v202, v202, v216\n"
    "v_sub_f32 v203, v203, v216\n"
    "v_sub_f32 v204, v204, v216\n"
    "v_sub_f32 v205, v205, v216\n"
    "v_sub_f32 v206, v206, v216\n"
    "v_sub_f32 v207, v207, v216\n"
    "v_sub_f32 v208, v208, v217\n"
    "v_sub_f32 v209, v209, v217\n"
    "v_sub_f32 v210, v210, v217\n"
    "v_sub_f32 v211, v211, v217\n"
    "v_sub_f32 v212, v212, v217\n"
    "v_sub_f32 v213, v213, v217\n"
    "v_sub_f32 v214, v214, v217\n"
    "v_sub_f32 v215, v215, v217\n"
    "v_mul_f32 v216, v200, v200\n"
    "v_fmac_f32 v216, v201, v201\n"
    "v_fmac_f32 v216, v202, v202\n"
    "v_fmac_f32 v216, v203, v203\n"
    "v_fmac_f32 v216, v204, v204\n"
    "v_fmac_f32 v216, v205, v205\n"
    "v_fmac_f32 v216, v206, v206\n"
    "v_fmac_f32 v216, v207, v207\n"
    "v_mul_f32 v217, v208, v208\n"
    "v_fmac_f32 v217, v209, v209\n"
    "v_fmac_f32 v217, v210, v210\n"
    "v_fmac_f32 v217, v211, v211\n"
    "v_fmac_f32 v217, v212, v212\n"
    "v_fmac_f32 v217, v213, v213\n"
    "v_fmac_f32 v217, v214, v214\n"
    "v_fmac_f32 v217, v215, v215\n"
    "v_mov_b32 v221, s72\n"
    "v_lshrrev_b32 v222, 2, v0\n"
    "v_permlane32_swap_b32 v216, v217\n"
    "v_add_f32 v216, v216, v217\n"
    "v_and_b32 v223, 31, v0\n"
    "v_cmp_eq_u32 vcc, 0, v223\n"
    "v_add_f32_dpp v216, v216, v216 row_ror:8 row_mask:0xf bank_mask:0xf\n"
    "v_mov_b32 v223, 0x7ff00000\n"
    "v_cndmask_b32 v222, v223, v222, vcc\n"
    "v_add_f32_dpp v216, v216, v216 row_ror:4 row_mask:0xf bank_mask:0xf\n"
    "s_lshl_b32 s85, s70, 10\n"
    "s_lshl_b32 s86, s82, 6\n"
    "v_add_f32_dpp v216, v216, v216 row_ror:2 row_mask:0xf bank_mask:0xf\n"
    "s_add_u32 s85, s85, s86\n"
    "s_lshl_b32 s86, s69, 4\n"
    "v_add_f32_dpp v216, v216, v216 row_ror:1 row_mask:0xf bank_mask:0xf\n"
    "v_mov_b32 v218, v216\n"
    "s_add_u32 s85, s85, s86\n"
    "s_add_u32 s85, s85, 0xa50000\n"
    "v_permlane16_swap_b32 v216, v218\n"
    "v_add_f32 v216, v216, v218\n"
    "v_mul_f32 v216, 0x3b000000, v216\n"
    "v_cvt_pk_bf16_f32 v216, v216, v216\n"
    "v_and_b32 v216, 0xffff0000, v216\n"
    "v_add_f32 v216, 0x3727c5ac, v216\n"
    "v_cvt_pk_bf16_f32 v216, v216, v216\n"
    "v_and_b32 v216, 0xffff0000, v216\n"
    "v_sqrt_f32 v216, v216\n"
    "s_nop 0\n"
    "v_cvt_pk_bf16_f32 v220, v219, v216\n"
    "buffer_store_dwordx2 v[220:221], v222, s[48:51], s85 offen sc1\n"
    "s_waitcnt vmcnt(31)\n"
    "s_barrier\n"
    "ds_read_b64_tr_b16 v[16:17], v1 offset:16384\n"
    "ds_read_b128 a[0:3], v2\n"
    "ds_read_b64_tr_b16 v[18:19], v1 offset:16896\n"
    "ds_read_b128 a[4:7], v2 offset:4096\n"
    "ds_read_b64_tr_b16 v[20:21], v1 offset:18432\n"
    "ds_read_b64_tr_b16 v[22:23], v1 offset:18944\n"
    "ds_read_b64_tr_b16 v[24:25], v1 offset:20480\n"
    "ds_read_b128 a[8:11], v3\n"
    "s_waitcnt lgkmcnt(5)\n"
    "v_mfma_f32_32x32x16_bf16 v[32:47], v[16:19], a[0:3], 0\n"
    "ds_read_b64_tr_b16 v[26:27], v1 offset:20992\n"
    "ds_read_b128 a[12:15], v3 offset:4096\n"
    "v_bfe_u32 v180, v0, 2, 2\n"
    "v_and_b32 v181, 3, v0\n"
    "v_bfe_u32 v182, v0, 4, 1\n"
    "s_waitcnt lgkmcnt(6)\n"
    "v_mfma_f32_32x32x16_bf16 v[48:63], v[16:19], a[4:7], 0\n"
    "ds_read_b64_tr_b16 v[28:29], v1 offset:22528\n"
    "v_lshrrev_b32 v183, 5, v0\n"
    "s_lshr_b32 s34, s70, 3\n"
    "s_mul_i32 s34, s34, 0x2000\n"
    "s_add_u32 s34, s34, 0x4000\n"
    "s_waitcnt lgkmcnt(5)\n"
    "v_mfma_f32_32x32x16_bf16 v[80:95], v[20:23], a[4:7], 0\n"
    "ds_read_b64_tr_b16 v[30:31], v1 offset:23040\n"
    "s_lshl_b32 s35, s75, 11\n"
    "s_add_u32 s34, s34, s35\n"
    "v_lshlrev_b32 v183, 12, v183\n"
    "v_lshl_add_u32 v183, v181, 3, v183\n"
    "v_mfma_f32_32x32x16_bf16 v[64:79], v[20:23], a[0:3], 0\n"
    "ds_read_b64_tr_b16 v[16:17], v1 offset:24576\n"
    "ds_read_b128 a[16:19], v4\n"
    "v_lshl_add_u32 v182, v182, 3, v180\n"
    "v_lshl_add_u32 v183, v182, 7, v183\n"
    "v_lshrrev_b32 v180, 1, v180\n"
    "s_waitcnt lgkmcnt(5)\n"
    "v_mfma_f32_32x32x16_bf16 v[32:47], v[24:27], a[8:11], v[32:47]\n"
    "ds_read_b64_tr_b16 v[18:19], v1 offset:25088\n"
    "ds_read_b128 a[20:23], v4 offset:4096\n"
    "v_xor_b32 v180, s76, v180\n"
    "v_lshl_add_u32 v183, v180, 6, v183\n"
    "v_add_u32 v15, s34, v183\n"
    "s_waitcnt lgkmcnt(6)\n"
    "v_mfma_f32_32x32x16_bf16 v[48:63], v[24:27], a[12:15], v[48:63]\n"
    "ds_read_b64_tr_b16 v[20:21], v1 offset:26624\n"
    "ds_read_b64_tr_b16 v[168:169], v15\n"
    "ds_read_b64_tr_b16 v[170:171], v15 offset:512\n"
    "ds_read_b64_tr_b16 v[172:173], v15 offset:32\n"
    "ds_read_b64_tr_b16 v[174:175], v15 offset:544\n"
    "s_waitcnt lgkmcnt(9)\n"
    "v_mfma_f32_32x32x16_bf16 v[80:95], v[28:31], a[12:15], v[80:95]\n"
    "ds_read_b64_tr_b16 v[22:23], v1 offset:27136\n"
    "v_mov_b32 v176, 0x3f803f80\n"
    "v_mov_b32 v177, 0x3f803f80\n"
    "v_mov_b32 v178, 0x3f803f80\n"
    "v_mov_b32 v179, 0x3f803f80\n"
    "v_mfma_f32_32x32x16_bf16 v[64:79], v[28:31], a[8:11], v[64:79]\n"
    "ds_read_b64_tr_b16 v[24:25], v1 offset:28672\n"
    "ds_read_b128 a[24:27], v5\n"
    "s_mul_i32 s34, s82, 12\n"
    "s_lshl_b32 s35, s76, 1\n"
    "s_add_u32 s34, s34, s35\n"
    "s_waitcnt lgkmcnt(9)\n"
    "v_mfma_f32_32x32x16_bf16 v[32:47], v[16:19], a[16:19], v[32:47]\n"
    "ds_read_b64_tr_b16 v[26:27], v1 offset:29184\n"
    "ds_read_b128 a[28:31], v5 offset:4096\n"
    "s_add_u32 s34, s34, s75\n"
    "s_add_u32 s34, s34, 0\n"
    "s_lshl_b32 s34, s34, 12\n"
    "s_waitcnt lgkmcnt(10)\n"
    "v_mfma_f32_32x32x16_bf16 v[48:63], v[16:19], a[20:23], v[48:63]\n"
    "ds_read_b64_tr_b16 v[28:29], v1 offset:30720\n"
    "s_add_u32 s34, s34, 0x990000\n"
    "s_and_b32 s35, s70, 7\n"
    "s_lshl_b32 s35, s35, 9\n"
    "s_waitcnt lgkmcnt(8)\n"
    "v_mfma_f32_16x16x32_bf16 v[180:183], v[176:179], v[168:171], 0\n"
    "s_waitcnt lgkmcnt(6)\n"
    "v_mfma_f32_16x16x32_bf16 v[184:187], v[176:179], v[172:175], 0\n"
    "s_add_u32 s34, s34, s35\n"
    "s_waitcnt lgkmcnt(5)\n"
    "v_mfma_f32_32x32x16_bf16 v[80:95], v[20:23], a[20:23], v[80:95]\n"
    "ds_read_b64_tr_b16 v[30:31], v1 offset:31232\n"
    "s_lshr_b32 s35, s70, 3\n"
    "s_lshl_b32 s35, s35, 8\n"
    "s_add_u32 s34, s34, s35\n"
    "v_and_b32 v15, 31, v0\n"
    "v_mfma_f32_32x32x16_bf16 v[64:79], v[20:23], a[16:19], v[64:79]\n"
    "s_waitcnt vmcnt(22)\n"
    "s_barrier\n"
    "ds_read_b64_tr_b16 v[16:17], v1 offset:49152\n"
    "ds_read_b128 a[32:35], v2 offset:32768\n"
    "v_lshlrev_b32 v15, 3, v15\n"
    "v_cndmask_b32 v180, v180, v184, s[92:93]\n"
    "s_waitcnt lgkmcnt(5)\n"
    "v_mfma_f32_32x32x16_bf16 v[32:47], v[24:27], a[24:27], v[32:47]\n"
    "ds_read_b64_tr_b16 v[18:19], v1 offset:49664\n"
    "ds_read_b128 a[36:39], v2 offset:36864\n"
    "v_mov_b32 v181, s72\n"
    "buffer_store_dwordx2 v[180:181], v15, s[48:51], s34 offen sc1\n"
    "s_waitcnt lgkmcnt(6)\n"
    "v_mfma_f32_32x32x16_bf16 v[48:63], v[24:27], a[28:31], v[48:63]\n"
    "ds_read_b64_tr_b16 v[20:21], v1 offset:51200\n"
    "s_waitcnt lgkmcnt(5)\n"
    "v_mfma_f32_32x32x16_bf16 v[80:95], v[28:31], a[28:31], v[80:95]\n"
    "ds_read_b64_tr_b16 v[22:23], v1 offset:51712\n"
    "v_mfma_f32_32x32x16_bf16 v[64:79], v[28:31], a[24:27], v[64:79]\n"
    "ds_read_b64_tr_b16 v[24:25], v1 offset:53248\n"
    "ds_read_b128 a[40:43], v3 offset:32768\n"
    "s_waitcnt lgkmcnt(5)\n"
    "v_mfma_f32_32x32x16_bf16 v[32:47], v[16:19], a[32:35], v[32:47]\n"
    "ds_read_b64_tr_b16 v[26:27], v1 offset:53760\n"
    "ds_read_b128 a[44:47], v3 offset:36864\n"
    "s_waitcnt lgkmcnt(6)\n"
    "v_mfma_f32_32x32x16_bf16 v[48:63], v[16:19], a[36:39], v[48:63]\n"
    "ds_read_b64_tr_b16 v[28:29], v1 offset:55296\n"
    "s_waitcnt lgkmcnt(5)\n"
    "v_mfma_f32_32x32x16_bf16 v[80:95], v[20:23], a[36:39], v[80:95]\n"
    "ds_read_b64_tr_b16 v[30:31], v1 offset:55808\n"
    "v_mfma_f32_32x32x16_bf16 v[64:79], v[20:23], a[32:35], v[64:79]\n"
    "ds_read_b64_tr_b16 v[16:17], v1 offset:57344\n"
    "ds_read_b128 a[48:51], v4 offset:32768\n"
    "s_waitcnt lgkmcnt(5)\n"
    "v_mfma_f32_32x32x16_bf16 v[32:47], v[24:27], a[40:43], v[32:47]\n"
    "ds_read_b64_tr_b16 v[18:19], v1 offset:57856\n"
    "ds_read_b128 a[52:55], v4 offset:36864\n"
    "s_waitcnt lgkmcnt(6)\n"
    "v_mfma_f32_32x32x16_bf16 v[48:63], v[24:27], a[44:47], v[48:63]\n"
    "ds_read_b64_tr_b16 v[20:21], v1 offset:59392\n"
    "s_waitcnt lgkmcnt(5)\n"
    "v_mfma_f32_32x32x16_bf16 v[80:95], v[28:31], a[44:47], v[80:95]\n"
    "ds_read_b64_tr_b16 v[22:23], v1 offset:59904\n"
    "v_mfma_f32_32x32x16_bf16 v[64:79], v[28:31], a[40:43], v[64:79]\n"
    "ds_read_b64_tr_b16 v[24:25], v1 offset:61440\n"
    "ds_read_b128 a[56:59], v5 offset:32768\n"
    "s_waitcnt lgkmcnt(5)\n"
    "v_mfma_f32_32x32x16_bf16 v[32:47], v[16:19], a[48:51], v[32:47]\n"
    "ds_read_b64_tr_b16 v[26:27], v1 offset:61952\n"
    "ds_read_b128 a[60:63], v5 offset:36864\n"
    "s_waitcnt lgkmcnt(6)\n"
    "v_mfma_f32_32x32x16_bf16 v[48:63], v[16:19], a[52:55], v[48:63]\n"
    "ds_read_b64_tr_b16 v[28:29], v1 offset:63488\n"
    "s_waitcnt lgkmcnt(5)\n"
    "v_mfma_f32_32x32x16_bf16 v[80:95], v[20:23], a[52:55], v[80:95]\n"
    "ds_read_b64_tr_b16 v[30:31], v1 offset:64000\n"
    "v_mfma_f32_32x32x16_bf16 v[64:79], v[20:23], a[48:51], v[64:79]\n"
    "s_waitcnt vmcnt(15)\n"
    "s_barrier\n"
    "v_add_u32 v1, 0x14000, v1\n"
    "ds_read_b64_tr_b16 v[16:17], v1\n"
    "v_add_u32 v2, 0x10000, v2\n"
    "v_add_u32 v3, 0x10000, v3\n"
    "v_add_u32 v4, 0x10000, v4\n"
    "v_add_u32 v5, 0x10000, v5\n"
    "ds_read_b128 a[64:67], v2\n"
    "s_mul_i32 s74, s64, 0x600\n"
    "s_add_u32 m0, s80, 0\n"
    "s_nop 0\n"
    "buffer_load_dwordx4 v10, s[40:43], s74 offen lds\n"
    "s_waitcnt lgkmcnt(5)\n"
    "v_mfma_f32_32x32x16_bf16 v[32:47], v[24:27], a[56:59], v[32:47]\n"
    "ds_read_b64_tr_b16 v[18:19], v1 offset:512\n"
    "ds_read_b128 a[68:71], v2 offset:4096\n"
    "buffer_load_dwordx4 v11, s[40:43], s74 offen offset:1024 lds\n"
    "s_waitcnt lgkmcnt(6)\n"
    "v_mfma_f32_32x32x16_bf16 v[48:63], v[24:27], a[60:63], v[48:63]\n"
    "ds_read_b64_tr_b16 v[20:21], v1 offset:2048\n"
    "buffer_load_dwordx4 v10, s[44:47], s74 offen offset:2048 lds\n"
    "s_waitcnt lgkmcnt(5)\n"
    "v_mfma_f32_32x32x16_bf16 v[80:95], v[28:31], a[60:63], v[80:95]\n"
    "ds_read_b64_tr_b16 v[22:23], v1 offset:2560\n"
    "buffer_load_dwordx4 v11, s[44:47], s74 offen offset:3072 lds\n"
    "v_mfma_f32_32x32x16_bf16 v[64:79], v[28:31], a[56:59], v[64:79]\n"
    "ds_read_b64_tr_b16 v[24:25], v1 offset:4096\n"
    "ds_read_b128 a[72:75], v3\n"
    "s_add_u32 m0, s80, 0x4000\n"
    "s_nop 0\n"
    "buffer_load_dwordx4 v6, s[36:39], s65 offen lds\n"
    "s_waitcnt lgkmcnt(5)\n"
    "v_mfma_f32_32x32x16_bf16 v[32:47], v[16:19], a[64:67], v[32:47]\n"
    "ds_read_b64_tr_b16 v[26:27], v1 offset:4608\n"
    "ds_read_b128 a[76:79], v3 offset:4096\n"
    "buffer_load_dwordx4 v7, s[36:39], s65 offen offset:1024 lds\n"
    "s_waitcnt lgkmcnt(6)\n"
    "v_mfma_f32_32x32x16_bf16 v[48:63], v[16:19], a[68:71], v[48:63]\n"
    "ds_read_b64_tr_b16 v[28:29], v1 offset:6144\n"
    "buffer_load_dwordx4 v8, s[36:39], s65 offen offset:2048 lds\n"
    "s_waitcnt lgkmcnt(5)\n"
    "v_mfma_f32_32x32x16_bf16 v[80:95], v[20:23], a[68:71], v[80:95]\n"
    "ds_read_b64_tr_b16 v[30:31], v1 offset:6656\n"
    "buffer_load_dwordx4 v9, s[36:39], s65 offen offset:3072 lds\n"
    "v_mfma_f32_32x32x16_bf16 v[64:79], v[20:23], a[64:67], v[64:79]\n"
    "ds_read_b64_tr_b16 v[16:17], v1 offset:8192\n"
    "ds_read_b128 a[80:83], v4\n"
    "s_waitcnt lgkmcnt(5)\n"
    "v_mfma_f32_32x32x16_bf16 v[32:47], v[24:27], a[72:75], v[32:47]\n"
    "ds_read_b64_tr_b16 v[18:19], v1 offset:8704\n"
    "ds_read_b128 a[84:87], v4 offset:4096\n"
    "s_waitcnt lgkmcnt(6)\n"
    "v_mfma_f32_32x32x16_bf16 v[48:63], v[24:27], a[76:79], v[48:63]\n"
    "ds_read_b64_tr_b16 v[20:21], v1 offset:10240\n"
    "s_waitcnt lgkmcnt(5)\n"
    "v_mfma_f32_32x32x16_bf16 v[80:95], v[28:31], a[76:79], v[80:95]\n"
    "ds_read_b64_tr_b16 v[22:23], v1 offset:10752\n"
    "v_mfma_f32_32x32x16_bf16 v[64:79], v[28:31], a[72:75], v[64:79]\n"
    "ds_read_b64_tr_b16 v[24:25], v1 offset:12288\n"
    "ds_read_b128 a[88:91], v5\n"
    "s_waitcnt lgkmcnt(5)\n"
    "v_mfma_f32_32x32x16_bf16 v[32:47], v[16:19], a[80:83], v[32:47]\n"
    "ds_read_b64_tr_b16 v[26:27], v1 offset:12800\n"
    "ds_read_b128 a[92:95], v5 offset:4096\n"
    "s_waitcnt lgkmcnt(6)\n"
    "v_mfma_f32_32x32x16_bf16 v[48:63], v[16:19], a[84:87], v[48:63]\n"
    "ds_read_b64_tr_b16 v[28:29], v1 offset:14336\n"
    "s_waitcnt lgkmcnt(5)\n"
    "v_mfma_f32_32x32x16_bf16 v[80:95], v[20:23], a[84:87], v[80:95]\n"
    "ds_read_b64_tr_b16 v[30:31], v1 offset:14848\n"
    "v_mfma_f32_32x32x16_bf16 v[64:79], v[20:23], a[80:83], v[64:79]\n"
    "s_waitcnt vmcnt(15)\n"
    "s_barrier\n"
    "ds_read_b64_tr_b16 v[16:17], v1 offset:32768\n"
    "ds_read_b128 a[96:99], v2 offset:32768\n"
    "s_mul_i32 s74, s65, 0x600\n"
    "s_add_u32 m0, s80, 0x8000\n"
    "s_nop 0\n"
    "buffer_load_dwordx4 v10, s[40:43], s74 offen lds\n"
    "s_waitcnt lgkmcnt(5)\n"
    "v_mfma_f32_32x32x16_bf16 v[32:47], v[24:27], a[88:91], v[32:47]\n"
    "ds_read_b64_tr_b16 v[18:19], v1 offset:33280\n"
    "ds_read_b128 a[100:103], v2 offset:36864\n"
    "buffer_load_dwordx4 v11, s[40:43], s74 offen offset:1024 lds\n"
    "s_waitcnt lgkmcnt(6)\n"
    "v_mfma_f32_32x32x16_bf16 v[48:63], v[24:27], a[92:95], v[48:63]\n"
    "ds_read_b64_tr_b16 v[20:21], v1 offset:34816\n"
    "buffer_load_dwordx4 v10, s[44:47], s74 offen offset:2048 lds\n"
    "s_waitcnt lgkmcnt(5)\n"
    "v_mfma_f32_32x32x16_bf16 v[80:95], v[28:31], a[92:95], v[80:95]\n"
    "ds_read_b64_tr_b16 v[22:23], v1 offset:35328\n"
    "buffer_load_dwordx4 v11, s[44:47], s74 offen offset:3072 lds\n"
    "v_mfma_f32_32x32x16_bf16 v[64:79], v[28:31], a[88:91], v[64:79]\n"
    "ds_read_b64_tr_b16 v[24:25], v1 offset:36864\n"
    "ds_read_b128 a[104:107], v3 offset:32768\n"
    "s_add_u32 m0, s80, 0xc000\n"
    "s_nop 0\n"
    "buffer_load_dwordx4 v6, s[36:39], s66 offen lds\n"
    "s_waitcnt lgkmcnt(5)\n"
    "v_mfma_f32_32x32x16_bf16 v[32:47], v[16:19], a[96:99], v[32:47]\n"
    "ds_read_b64_tr_b16 v[26:27], v1 offset:37376\n"
    "ds_read_b128 a[108:111], v3 offset:36864\n"
    "buffer_load_dwordx4 v7, s[36:39], s66 offen offset:1024 lds\n"
    "s_waitcnt lgkmcnt(6)\n"
    "v_mfma_f32_32x32x16_bf16 v[48:63], v[16:19], a[100:103], v[48:63]\n"
    "ds_read_b64_tr_b16 v[28:29], v1 offset:38912\n"
    "buffer_load_dwordx4 v8, s[36:39], s66 offen offset:2048 lds\n"
    "s_waitcnt lgkmcnt(5)\n"
    "v_mfma_f32_32x32x16_bf16 v[80:95], v[20:23], a[100:103], v[80:95]\n"
    "ds_read_b64_tr_b16 v[30:31], v1 offset:39424\n"
    "buffer_load_dwordx4 v9, s[36:39], s66 offen offset:3072 lds\n"
    "v_mfma_f32_32x32x16_bf16 v[64:79], v[20:23], a[96:99], v[64:79]\n"
    "ds_read_b64_tr_b16 v[16:17], v1 offset:40960\n"
    "ds_read_b128 a[112:115], v4 offset:32768\n"
    "s_waitcnt lgkmcnt(5)\n"
    "v_mfma_f32_32x32x16_bf16 v[32:47], v[24:27], a[104:107], v[32:47]\n"
    "ds_read_b64_tr_b16 v[18:19], v1 offset:41472\n"
    "ds_read_b128 a[116:119], v4 offset:36864\n"
    "s_waitcnt lgkmcnt(6)\n"
    "v_mfma_f32_32x32x16_bf16 v[48:63], v[24:27], a[108:111], v[48:63]\n"
    "ds_read_b64_tr_b16 v[20:21], v1 offset:43008\n"
    "s_waitcnt lgkmcnt(5)\n"
    "v_mfma_f32_32x32x16_bf16 v[80:95], v[28:31], a[108:111], v[80:95]\n"
    "ds_read_b64_tr_b16 v[22:23], v1 offset:43520\n"
    "v_mfma_f32_32x32x16_bf16 v[64:79], v[28:31], a[104:107], v[64:79]\n"
    "ds_read_b64_tr_b16 v[24:25], v1 offset:45056\n"
    "ds_read_b128 a[120:123], v5 offset:32768\n"
    "s_waitcnt lgkmcnt(5)\n"
    "v_mfma_f32_32x32x16_bf16 v[32:47], v[16:19], a[112:115], v[32:47]\n"
    "ds_read_b64_tr_b16 v[26:27], v1 offset:45568\n"
    "ds_read_b128 a[124:127], v5 offset:36864\n"
    "s_waitcnt lgkmcnt(6)\n"
    "v_mfma_f32_32x32x16_bf16 v[48:63], v[16:19], a[116:119], v[48:63]\n"
    "ds_read_b64_tr_b16 v[28:29], v1 offset:47104\n"
    "s_waitcnt lgkmcnt(5)\n"
    "v_mfma_f32_32x32x16_bf16 v[80:95], v[20:23], a[116:119], v[80:95]\n"
    "ds_read_b64_tr_b16 v[30:31], v1 offset:47616\n"
    "v_mfma_f32_32x32x16_bf16 v[64:79], v[20:23], a[112:115], v[64:79]\n"
    "s_waitcnt vmcnt(12)\n"
    "s_barrier\n"
    "v_add_u32 v1, 0xfffec000, v1\n"
    "ds_read_b64_tr_b16 v[16:17], v1\n"
    "v_add_u32 v2, 0x8000, v2\n"
    "v_add_u32 v3, 0x8000, v3\n"
    "v_add_u32 v4, 0x8000, v4\n"
    "v_add_u32 v5, 0x8000, v5\n"
    "ds_read_b128 a[128:131], v2 offset:32768\n"
    "s_mul_i32 s74, s66, 0x600\n"
    "s_add_u32 m0, s80, 0x10000\n"
    "s_nop 0\n"
    "buffer_load_dwordx4 v10, s[40:43], s74 offen lds\n"
    "s_waitcnt lgkmcnt(5)\n"
    "v_mfma_f32_32x32x16_bf16 v[32:47], v[24:27], a[120:123], v[32:47]\n"
    "ds_read_b64_tr_b16 v[18:19], v1 offset:512\n"
    "ds_read_b128 a[132:135], v2 offset:36864\n"
    "buffer_load_dwordx4 v11, s[40:43], s74 offen offset:1024 lds\n"
    "s_waitcnt lgkmcnt(6)\n"
    "v_mfma_f32_32x32x16_bf16 v[48:63], v[24:27], a[124:127], v[48:63]\n"
    "ds_read_b64_tr_b16 v[20:21], v1 offset:2048\n"
    "buffer_load_dwordx4 v10, s[44:47], s74 offen offset:2048 lds\n"
    "s_waitcnt lgkmcnt(5)\n"
    "v_mfma_f32_32x32x16_bf16 v[80:95], v[28:31], a[124:127], v[80:95]\n"
    "ds_read_b64_tr_b16 v[22:23], v1 offset:2560\n"
    "buffer_load_dwordx4 v11, s[44:47], s74 offen offset:3072 lds\n"
    "v_mfma_f32_32x32x16_bf16 v[64:79], v[28:31], a[120:123], v[64:79]\n"
    "ds_read_b64_tr_b16 v[24:25], v1 offset:4096\n"
    "ds_read_b128 a[136:139], v3 offset:32768\n"
    "s_add_u32 m0, s80, 0x14000\n"
    "s_nop 0\n"
    "buffer_load_dwordx4 v6, s[36:39], s67 offen lds\n"
    "s_waitcnt lgkmcnt(5)\n"
    "v_mfma_f32_32x32x16_bf16 v[32:47], v[16:19], a[128:131], v[32:47]\n"
    "ds_read_b64_tr_b16 v[26:27], v1 offset:4608\n"
    "ds_read_b128 a[140:143], v3 offset:36864\n"
    "buffer_load_dwordx4 v7, s[36:39], s67 offen offset:1024 lds\n"
    "s_waitcnt lgkmcnt(6)\n"
    "v_mfma_f32_32x32x16_bf16 v[48:63], v[16:19], a[132:135], v[48:63]\n"
    "ds_read_b64_tr_b16 v[28:29], v1 offset:6144\n"
    "buffer_load_dwordx4 v8, s[36:39], s67 offen offset:2048 lds\n"
    "s_waitcnt lgkmcnt(5)\n"
    "v_mfma_f32_32x32x16_bf16 v[80:95], v[20:23], a[132:135], v[80:95]\n"
    "ds_read_b64_tr_b16 v[30:31], v1 offset:6656\n"
    "buffer_load_dwordx4 v9, s[36:39], s67 offen offset:3072 lds\n"
    "v_mfma_f32_32x32x16_bf16 v[64:79], v[20:23], a[128:131], v[64:79]\n"
    "ds_read_b64_tr_b16 v[16:17], v1 offset:8192\n"
    "ds_read_b128 a[144:147], v4 offset:32768\n"
    "s_waitcnt lgkmcnt(5)\n"
    "v_mfma_f32_32x32x16_bf16 v[32:47], v[24:27], a[136:139], v[32:47]\n"
    "ds_read_b64_tr_b16 v[18:19], v1 offset:8704\n"
    "ds_read_b128 a[148:151], v4 offset:36864\n"
    "s_waitcnt lgkmcnt(6)\n"
    "v_mfma_f32_32x32x16_bf16 v[48:63], v[24:27], a[140:143], v[48:63]\n"
    "ds_read_b64_tr_b16 v[20:21], v1 offset:10240\n"
    "v_and_b32 v196, 31, v0\n"
    "v_lshlrev_b32 v196, 3, v196\n"
    "s_lshl_b32 s34, s70, 10\n"
    "s_lshl_b32 s35, s75, 9\n"
    "s_waitcnt lgkmcnt(5)\n"
    "v_mfma_f32_32x32x16_bf16 v[80:95], v[28:31], a[140:143], v[80:95]\n"
    "ds_read_b64_tr_b16 v[22:23], v1 offset:10752\n"
    "s_add_u32 s34, s34, s35\n"
    "s_add_u32 s34, s34, 0xa50000\n"
    "buffer_load_dwordx2 v[192:193], v196, s[48:51], s34 offen sc1\n"
    "buffer_load_dwordx2 v[194:195], v196, s[48:51], s34 offen offset:256 sc1\n"
    "v_mfma_f32_32x32x16_bf16 v[64:79], v[28:31], a[136:139], v[64:79]\n"
    "ds_read_b64_tr_b16 v[24:25], v1 offset:12288\n"
    "ds_read_b128 a[152:155], v5 offset:32768\n"
    "s_waitcnt lgkmcnt(5)\n"
    "v_mfma_f32_32x32x16_bf16 v[32:47], v[16:19], a[144:147], v[32:47]\n"
    "ds_read_b64_tr_b16 v[26:27], v1 offset:12800\n"
    "ds_read_b128 a[156:159], v5 offset:36864\n"
    "s_waitcnt lgkmcnt(6)\n"
    "v_mfma_f32_32x32x16_bf16 v[48:63], v[16:19], a[148:151], v[48:63]\n"
    "ds_read_b64_tr_b16 v[28:29], v1 offset:14336\n"
    "s_waitcnt lgkmcnt(5)\n"
    "v_mfma_f32_32x32x16_bf16 v[80:95], v[20:23], a[148:151], v[80:95]\n"
    "ds_read_b64_tr_b16 v[30:31], v1 offset:14848\n"
    "v_mfma_f32_32x32x16_bf16 v[64:79], v[20:23], a[144:147], v[64:79]\n"
    "s_waitcnt vmcnt(14)\n"
    "s_barrier\n"
    "ds_read_b64_tr_b16 v[16:17], v1 offset:32768\n"
    "v_add_u32 v2, 0xfffec000, v2\n"
    "v_add_u32 v3, 0xfffec000, v3\n"
    "v_add_u32 v4, 0xfffec000, v4\n"
    "v_add_u32 v5, 0xfffec000, v5\n"
    "ds_read_b128 a[160:163], v2\n"
    "s_mul_i32 s74, s67, 0x600\n"
    "s_add_u32 m0, s80, 0x18000\n"
    "s_nop 0\n"
    "buffer_load_dwordx4 v10, s[40:43], s74 offen lds\n"
    "s_waitcnt lgkmcnt(5)\n"
    "v_mfma_f32_32x32x16_bf16 v[32:47], v[24:27], a[152:155], v[32:47]\n"
    "ds_read_b64_tr_b16 v[18:19], v1 offset:33280\n"
    "ds_read_b128 a[164:167], v2 offset:4096\n"
    "buffer_load_dwordx4 v11, s[40:43], s74 offen offset:1024 lds\n"
    "s_waitcnt lgkmcnt(6)\n"
    "v_mfma_f32_32x32x16_bf16 v[48:63], v[24:27], a[156:159], v[48:63]\n"
    "ds_read_b64_tr_b16 v[20:21], v1 offset:34816\n"
    "buffer_load_dwordx4 v10, s[44:47], s74 offen offset:2048 lds\n"
    "s_waitcnt lgkmcnt(5)\n"
    "v_mfma_f32_32x32x16_bf16 v[80:95], v[28:31], a[156:159], v[80:95]\n"
    "ds_read_b64_tr_b16 v[22:23], v1 offset:35328\n"
    "buffer_load_dwordx4 v11, s[44:47], s74 offen offset:3072 lds\n"
    "v_mfma_f32_32x32x16_bf16 v[64:79], v[28:31], a[152:155], v[64:79]\n"
    "ds_read_b64_tr_b16 v[24:25], v1 offset:36864\n"
    "ds_read_b128 a[168:171], v3\n"
    "s_mul_i32 s30, s71, 0x80\n"
    "s_add_u32 s30, s30, 0x800\n"
    "s_add_u32 s40, s20, s30\n"
    "s_addc_u32 s41, s21, 0\n"
    "s_sub_u32 s42, 0x1c0000, s30\n"
    "s_mov_b32 s43, 0x20000\n"
    "s_add_u32 s44, s22, s30\n"
    "s_addc_u32 s45, s23, 0\n"
    "s_sub_u32 s44, s44, 0x800\n"
    "s_subb_u32 s45, s45, 0\n"
    "s_sub_u32 s46, 0x1c0800, s30\n"
    "s_mov_b32 s47, 0x20000\n"
    "s_mul_i32 s74, s60, 0x700\n"
    "s_add_u32 m0, s80, 0x1c000\n"
    "s_nop 0\n"
    "buffer_load_dwordx4 v12, s[40:43], s74 offen lds\n"
    "s_waitcnt lgkmcnt(5)\n"
    "v_mfma_f32_32x32x16_bf16 v[32:47], v[16:19], a[160:163], v[32:47]\n"
    "ds_read_b64_tr_b16 v[26:27], v1 offset:37376\n"
    "ds_read_b128 a[172:175], v3 offset:4096\n"
    "buffer_load_dwordx4 v13, s[40:43], s74 offen offset:1024 lds\n"
    "s_waitcnt lgkmcnt(6)\n"
    "v_mfma_f32_32x32x16_bf16 v[48:63], v[16:19], a[164:167], v[48:63]\n"
    "ds_read_b64_tr_b16 v[28:29], v1 offset:38912\n"
    "buffer_load_dwordx4 v12, s[44:47], s74 offen offset:2048 lds\n"
    "s_waitcnt lgkmcnt(5)\n"
    "v_mfma_f32_32x32x16_bf16 v[80:95], v[20:23], a[164:167], v[80:95]\n"
    "ds_read_b64_tr_b16 v[30:31], v1 offset:39424\n"
    "buffer_load_dwordx4 v13, s[44:47], s74 offen offset:3072 lds\n"
    "v_mfma_f32_32x32x16_bf16 v[64:79], v[20:23], a[160:163], v[64:79]\n"
    "ds_read_b64_tr_b16 v[16:17], v1 offset:40960\n"
    "ds_read_b128 a[176:179], v4\n"
    "s_waitcnt lgkmcnt(5)\n"
    "v_mfma_f32_32x32x16_bf16 v[32:47], v[24:27], a[168:171], v[32:47]\n"
    "ds_read_b64_tr_b16 v[18:19], v1 offset:41472\n"
    "ds_read_b128 a[180:183], v4 offset:4096\n"
    "s_waitcnt lgkmcnt(6)\n"
    "v_mfma_f32_32x32x16_bf16 v[48:63], v[24:27], a[172:175], v[48:63]\n"
    "ds_read_b64_tr_b16 v[20:21], v1 offset:43008\n"
    "s_waitcnt lgkmcnt(5)\n"
    "v_mfma_f32_32x32x16_bf16 v[80:95], v[28:31], a[172:175], v[80:95]\n"
    "ds_read_b64_tr_b16 v[22:23], v1 offset:43520\n"
    "v_mfma_f32_32x32x16_bf16 v[64:79], v[28:31], a[168:171], v[64:79]\n"
    "ds_read_b64_tr_b16 v[24:25], v1 offset:45056\n"
    "ds_read_b128 a[184:187], v5\n"
    "s_waitcnt lgkmcnt(5)\n"
    "v_mfma_f32_32x32x16_bf16 v[32:47], v[16:19], a[176:179], v[32:47]\n"
    "ds_read_b64_tr_b16 v[26:27], v1 offset:45568\n"
    "ds_read_b128 a[188:191], v5 offset:4096\n"
    "s_waitcnt lgkmcnt(6)\n"
    "v_mfma_f32_32x32x16_bf16 v[48:63], v[16:19], a[180:183], v[48:63]\n"
    "ds_read_b64_tr_b16 v[28:29], v1 offset:47104\n"
    "s_waitcnt lgkmcnt(5)\n"
    "v_mfma_f32_32x32x16_bf16 v[80:95], v[20:23], a[180:183], v[80:95]\n"
    "ds_read_b64_tr_b16 v[30:31], v1 offset:47616\n"
    "v_mfma_f32_32x32x16_bf16 v[64:79], v[20:23], a[176:179], v[64:79]\n"
    "s_waitcnt vmcnt(14)\n"
    "s_barrier\n"
    "v_add_u32 v1, 0x10000, v1\n"
    "ds_read_b64_tr_b16 v[16:17], v1\n"
    "ds_read_b128 a[192:195], v2 offset:32768\n"
    "s_mul_i32 s74, s61, 0x700\n"
    "s_add_u32 m0, s80, 0x20000\n"
    "s_nop 0\n"
    "buffer_load_dwordx4 v12, s[40:43], s74 offen lds\n"
    "s_waitcnt lgkmcnt(5)\n"
    "v_mfma_f32_32x32x16_bf16 v[32:47], v[24:27], a[184:187], v[32:47]\n"
    "ds_read_b64_tr_b16 v[18:19], v1 offset:512\n"
    "ds_read_b128 a[196:199], v2 offset:36864\n"
    "buffer_load_dwordx4 v13, s[40:43], s74 offen offset:1024 lds\n"
    "s_waitcnt lgkmcnt(6)\n"
    "v_mfma_f32_32x32x16_bf16 v[48:63], v[24:27], a[188:191], v[48:63]\n"
    "ds_read_b64_tr_b16 v[20:21], v1 offset:2048\n"
    "buffer_load_dwordx4 v12, s[44:47], s74 offen offset:2048 lds\n"
    "s_waitcnt lgkmcnt(5)\n"
    "v_mfma_f32_32x32x16_bf16 v[80:95], v[28:31], a[188:191], v[80:95]\n"
    "ds_read_b64_tr_b16 v[22:23], v1 offset:2560\n"
    "buffer_load_dwordx4 v13, s[44:47], s74 offen offset:3072 lds\n"
    "v_mfma_f32_32x32x16_bf16 v[64:79], v[28:31], a[184:187], v[64:79]\n"
    "ds_read_b64_tr_b16 v[24:25], v1 offset:4096\n"
    "ds_read_b128 a[200:203], v3 offset:32768\n"
    "s_mul_i32 s74, s62, 0x700\n"
    "s_add_u32 m0, s80, 0\n"
    "s_nop 0\n"
    "buffer_load_dwordx4 v12, s[40:43], s74 offen lds\n"
    "s_waitcnt lgkmcnt(5)\n"
    "v_mfma_f32_32x32x16_bf16 v[32:47], v[16:19], a[192:195], v[32:47]\n"
    "ds_read_b64_tr_b16 v[26:27], v1 offset:4608\n"
    "ds_read_b128 a[204:207], v3 offset:36864\n"
    "buffer_load_dwordx4 v13, s[40:43], s74 offen offset:1024 lds\n"
    "s_waitcnt lgkmcnt(6)\n"
    "v_mfma_f32_32x32x16_bf16 v[48:63], v[16:19], a[196:199], v[48:63]\n"
    "ds_read_b64_tr_b16 v[28:29], v1 offset:6144\n"
    "buffer_load_dwordx4 v12, s[44:47], s74 offen offset:2048 lds\n"
    "s_waitcnt lgkmcnt(5)\n"
    "v_mfma_f32_32x32x16_bf16 v[80:95], v[20:23], a[196:199], v[80:95]\n"
    "ds_read_b64_tr_b16 v[30:31], v1 offset:6656\n"
    "buffer_load_dwordx4 v13, s[44:47], s74 offen offset:3072 lds\n"
    "v_mfma_f32_32x32x16_bf16 v[64:79], v[20:23], a[192:195], v[64:79]\n"
    "ds_read_b64_tr_b16 v[16:17], v1 offset:8192\n"
    "ds_read_b128 a[208:211], v4 offset:32768\n"
    "s_waitcnt lgkmcnt(5)\n"
    "v_mfma_f32_32x32x16_bf16 v[32:47], v[24:27], a[200:203], v[32:47]\n"
    "ds_read_b64_tr_b16 v[18:19], v1 offset:8704\n"
    "ds_read_b128 a[212:215], v4 offset:36864\n"
    "s_waitcnt lgkmcnt(6)\n"
    "v_mfma_f32_32x32x16_bf16 v[48:63], v[24:27], a[204:207], v[48:63]\n"
    "ds_read_b64_tr_b16 v[20:21], v1 offset:10240\n"
    "s_waitcnt lgkmcnt(5)\n"
    "v_mfma_f32_32x32x16_bf16 v[80:95], v[28:31], a[204:207], v[80:95]\n"
    "ds_read_b64_tr_b16 v[22:23], v1 offset:10752\n"
    "v_mfma_f32_32x32x16_bf16 v[64:79], v[28:31], a[200:203], v[64:79]\n"
    "ds_read_b64_tr_b16 v[24:25], v1 offset:12288\n"
    "ds_read_b128 a[216:219], v5 offset:32768\n"
    "s_waitcnt lgkmcnt(5)\n"
    "v_mfma_f32_32x32x16_bf16 v[32:47], v[16:19], a[208:211], v[32:47]\n"
    "ds_read_b64_tr_b16 v[26:27], v1 offset:12800\n"
    "ds_read_b128 a[220:223], v5 offset:36864\n"
    "s_waitcnt lgkmcnt(6)\n"
    "v_mfma_f32_32x32x16_bf16 v[48:63], v[16:19], a[212:215], v[48:63]\n"
    "ds_read_b64_tr_b16 v[28:29], v1 offset:14336\n"
    "s_waitcnt lgkmcnt(5)\n"
    "v_mfma_f32_32x32x16_bf16 v[80:95], v[20:23], a[212:215], v[80:95]\n"
    "ds_read_b64_tr_b16 v[30:31], v1 offset:14848\n"
    "v_mfma_f32_32x32x16_bf16 v[64:79], v[20:23], a[208:211], v[64:79]\n"
    "s_waitcnt vmcnt(12)\n"
    "s_barrier\n"
    "ds_read_b64_tr_b16 v[16:17], v1 offset:32768\n"
    "v_add_u32 v2, 0x10000, v2\n"
    "v_add_u32 v3, 0x10000, v3\n"
    "v_add_u32 v4, 0x10000, v4\n"
    "v_add_u32 v5, 0x10000, v5\n"
    "ds_read_b128 a[224:227], v2\n"
    "s_mul_i32 s74, s63, 0x700\n"
    "s_add_u32 m0, s80, 0x4000\n"
    "s_nop 0\n"
    "buffer_load_dwordx4 v12, s[40:43], s74 offen lds\n"
    "s_waitcnt lgkmcnt(5)\n"
    "v_mfma_f32_32x32x16_bf16 v[32:47], v[24:27], a[216:219], v[32:47]\n"
    "ds_read_b64_tr_b16 v[18:19], v1 offset:33280\n"
    "ds_read_b128 a[228:231], v2 offset:4096\n"
    "buffer_load_dwordx4 v13, s[40:43], s74 offen offset:1024 lds\n"
    "s_waitcnt lgkmcnt(6)\n"
    "v_mfma_f32_32x32x16_bf16 v[48:63], v[24:27], a[220:223], v[48:63]\n"
    "ds_read_b64_tr_b16 v[20:21], v1 offset:34816\n"
    "buffer_load_dwordx4 v12, s[44:47], s74 offen offset:2048 lds\n"
    "s_waitcnt lgkmcnt(5)\n"
    "v_mfma_f32_32x32x16_bf16 v[80:95], v[28:31], a[220:223], v[80:95]\n"
    "ds_read_b64_tr_b16 v[22:23], v1 offset:35328\n"
    "buffer_load_dwordx4 v13, s[44:47], s74 offen offset:3072 lds\n"
    "v_mfma_f32_32x32x16_bf16 v[64:79], v[28:31], a[216:219], v[64:79]\n"
    "ds_read_b64_tr_b16 v[24:25], v1 offset:36864\n"
    "ds_read_b128 a[232:235], v3\n"
    "s_mul_i32 s74, s64, 0x700\n"
    "s_add_u32 m0, s80, 0x8000\n"
    "s_nop 0\n"
    "buffer_load_dwordx4 v12, s[40:43], s74 offen lds\n"
    "s_waitcnt lgkmcnt(5)\n"
    "v_mfma_f32_32x32x16_bf16 v[32:47], v[16:19], a[224:227], v[32:47]\n"
    "ds_read_b64_tr_b16 v[26:27], v1 offset:37376\n"
    "ds_read_b128 a[236:239], v3 offset:4096\n"
    "buffer_load_dwordx4 v13, s[40:43], s74 offen offset:1024 lds\n"
    "s_waitcnt lgkmcnt(6)\n"
    "v_mfma_f32_32x32x16_bf16 v[48:63], v[16:19], a[228:231], v[48:63]\n"
    "ds_read_b64_tr_b16 v[28:29], v1 offset:38912\n"
    "buffer_load_dwordx4 v12, s[44:47], s74 offen offset:2048 lds\n"
    "s_waitcnt lgkmcnt(5)\n"
    "v_mfma_f32_32x32x16_bf16 v[80:95], v[20:23], a[228:231], v[80:95]\n"
    "ds_read_b64_tr_b16 v[30:31], v1 offset:39424\n"
    "buffer_load_dwordx4 v13, s[44:47], s74 offen offset:3072 lds\n"
    "v_mfma_f32_32x32x16_bf16 v[64:79], v[20:23], a[224:227], v[64:79]\n"
    "ds_read_b64_tr_b16 v[16:17], v1 offset:40960\n"
    "ds_read_b128 a[240:243], v4\n"
    "s_waitcnt lgkmcnt(5)\n"
    "v_mfma_f32_32x32x16_bf16 v[32:47], v[24:27], a[232:235], v[32:47]\n"
    "ds_read_b64_tr_b16 v[18:19], v1 offset:41472\n"
    "ds_read_b128 a[244:247], v4 offset:4096\n"
    "s_waitcnt lgkmcnt(6)\n"
    "v_mfma_f32_32x32x16_bf16 v[48:63], v[24:27], a[236:239], v[48:63]\n"
    "ds_read_b64_tr_b16 v[20:21], v1 offset:43008\n"
    "s_waitcnt lgkmcnt(5)\n"
    "v_mfma_f32_32x32x16_bf16 v[80:95], v[28:31], a[236:239], v[80:95]\n"
    "ds_read_b64_tr_b16 v[22:23], v1 offset:43520\n"
    "v_mfma_f32_32x32x16_bf16 v[64:79], v[28:31], a[232:235], v[64:79]\n"
    "ds_read_b64_tr_b16 v[24:25], v1 offset:45056\n"
    "ds_read_b128 a[248:251], v5\n"
    "s_waitcnt lgkmcnt(5)\n"
    "v_mfma_f32_32x32x16_bf16 v[32:47], v[16:19], a[240:243], v[32:47]\n"
    "ds_read_b64_tr_b16 v[26:27], v1 offset:45568\n"
    "ds_read_b128 a[252:255], v5 offset:4096\n"
    "s_waitcnt lgkmcnt(6)\n"
    "v_mfma_f32_32x32x16_bf16 v[48:63], v[16:19], a[244:247], v[48:63]\n"
    "ds_read_b64_tr_b16 v[28:29], v1 offset:47104\n"
    "s_waitcnt lgkmcnt(5)\n"
    "v_mfma_f32_32x32x16_bf16 v[80:95], v[20:23], a[244:247], v[80:95]\n"
    "ds_read_b64_tr_b16 v[30:31], v1 offset:47616\n"
    "v_mfma_f32_32x32x16_bf16 v[64:79], v[20:23], a[240:243], v[64:79]\n"
    "s_waitcnt vmcnt(16)\n"
    "s_barrier\n"
    "v_bfe_u32 v1, v0, 3, 1\n"
    "v_xor_b32 v1, s76, v1\n"
    "v_lshlrev_b32 v1, 6, v1\n"
    "v_bfe_u32 v251, v0, 4, 1\n"
    "v_lshl_add_u32 v1, v251, 5, v1\n"
    "v_and_b32 v251, 3, v0\n"
    "v_lshl_add_u32 v1, v251, 3, v1\n"
    "v_bfe_u32 v251, v0, 2, 2\n"
    "v_lshl_add_u32 v1, v251, 7, v1\n"
    "v_lshrrev_b32 v251, 5, v0\n"
    "v_lshl_add_u32 v1, v251, 10, v1\n"
    "v_add_u32 v1, 0x18000, v1\n"
    "ds_read_b64_tr_b16 v[16:17], v1 offset:16384\n"
    "s_mul_i32 s74, s65, 0x700\n"
    "s_add_u32 m0, s80, 0xc000\n"
    "s_nop 0\n"
    "buffer_load_dwordx4 v12, s[40:43], s74 offen lds\n"
    "s_waitcnt lgkmcnt(4)\n"
    "v_mfma_f32_32x32x16_bf16 v[32:47], v[24:27], a[248:251], v[32:47]\n"
    "ds_read_b64_tr_b16 v[18:19], v1 offset:16896\n"
    "buffer_load_dwordx4 v13, s[40:43], s74 offen offset:1024 lds\n"
    "s_waitcnt lgkmcnt(4)\n"
    "v_mfma_f32_32x32x16_bf16 v[48:63], v[24:27], a[252:255], v[48:63]\n"
    "ds_read_b64_tr_b16 v[20:21], v1 offset:18432\n"
    "buffer_load_dwordx4 v12, s[44:47], s74 offen offset:2048 lds\n"
    "s_waitcnt lgkmcnt(3)\n"
    "v_mfma_f32_32x32x16_bf16 v[80:95], v[28:31], a[252:255], v[80:95]\n"
    "ds_read_b64_tr_b16 v[22:23], v1 offset:18944\n"
    "buffer_load_dwordx4 v13, s[44:47], s74 offen offset:3072 lds\n"
    "v_mfma_f32_32x32x16_bf16 v[64:79], v[28:31], a[248:251], v[64:79]\n"
    "ds_read_b64_tr_b16 v[24:25], v1 offset:20480\n"
    "s_mul_i32 s74, s66, 0x700\n"
    "s_add_u32 m0, s80, 0x10000\n"
    "s_nop 0\n"
    "buffer_load_dwordx4 v12, s[40:43], s74 offen lds\n"
    "v_xor_b32 v197, s72, v193\n"
    "v_bitop3_b32 v197, v197, v195, s72 bitop3:0xf6\n"
    "v_cmp_ne_u32 s[88:89], 0, v197\n"
    "s_cmp_lg_u64 s[88:89], 0\n"
    "s_cbranch_scc1 LB_slow_1%=\n"
    "LB_back_2%=:\n"
    "v_and_b32 v166, 0xffff0000, v192\n"
    "v_and_b32 v192, 0xffff, v192\n"
    "v_rcp_f32 v166, v166\n"
    "v_lshl_or_b32 v192, v192, 16, v192\n"
    "v_xor_b32 v160, 0x80008000, v192\n"
    "v_mov_b32 v161, 0\n"
    "v_mul_f32 v164, 0x3fb8aa3b, v166\n"
    "v_and_b32 v167, 0xffff0000, v194\n"
    "v_and_b32 v194, 0xffff, v194\n"
    "v_rcp_f32 v167, v167\n"
    "v_lshl_or_b32 v194, v194, 16, v194\n"
    "v_xor_b32 v162, 0x80008000, v194\n"
    "v_mov_b32 v163, 0\n"
    "v_mul_f32 v165, 0x3fb8aa3b, v167\n"
    "s_mul_i32 s34, s82, 12\n"
    "s_lshl_b32 s35, s76, 1\n"
    "s_add_u32 s34, s34, s35\n"
    "s_add_u32 s34, s34, s75\n"
    "s_add_u32 s34, s34, 0\n"
    "s_lshl_b32 s34, s34, 12\n"
    "s_add_u32 s34, s34, 0x990000\n"
    "v_and_b32 v184, 31, v0\n"
    "v_lshlrev_b32 v184, 3, v184\n"
    "v_lshrrev_b32 v185, 5, v0\n"
    "v_lshl_add_u32 v15, v185, 11, v184\n"
    "buffer_load_dwordx2 v[168:169], v15, s[48:51], s34 offen sc1\n"
    "buffer_load_dwordx2 v[170:171], v15, s[48:51], s34 offen offset:256 sc1\n"
    "buffer_load_dwordx2 v[172:173], v15, s[48:51], s34 offen offset:512 sc1\n"
    "buffer_load_dwordx2 v[174:175], v15, s[48:51], s34 offen offset:768 sc1\n"
    "buffer_load_dwordx2 v[176:177], v15, s[48:51], s34 offen offset:1024 sc1\n"
    "buffer_load_dwordx2 v[178:179], v15, s[48:51], s34 offen offset:1280 sc1\n"
    "buffer_load_dwordx2 v[180:181], v15, s[48:51], s34 offen offset:1536 sc1\n"
    "buffer_load_dwordx2 v[182:183], v15, s[48:51], s34 offen offset:1792 sc1\n"
    "s_waitcnt vmcnt(7)\n"
    "v_xor_b32 v185, s72, v169\n"
    "s_waitcnt vmcnt(6)\n"
    "v_bitop3_b32 v185, v185, v171, s72 bitop3:0xf6\n"
    "s_waitcnt vmcnt(5)\n"
    "v_bitop3_b32 v185, v185, v173, s72 bitop3:0xf6\n"
    "s_waitcnt vmcnt(4)\n"
    "v_bitop3_b32 v185, v185, v175, s72 bitop3:0xf6\n"
    "s_waitcnt vmcnt(3)\n"
    "v_bitop3_b32 v185, v185, v177, s72 bitop3:0xf6\n"
    "s_waitcnt vmcnt(2)\n"
    "v_bitop3_b32 v185, v185, v179, s72 bitop3:0xf6\n"
    "s_waitcnt vmcnt(1)\n"
    "v_bitop3_b32 v185, v185, v181, s72 bitop3:0xf6\n"
    "s_waitcnt vmcnt(0)\n"
    "v_bitop3_b32 v185, v185, v183, s72 bitop3:0xf6\n"
    "v_cmp_ne_u32 s[88:89], 0, v185\n"
    "s_cmp_lg_u64 s[88:89], 0\n"
    "s_cbranch_scc1 LB_slow_3%=\n"
    "LB_back_4%=:\n"
    "v_add_f32 v168, v168, v170\n"
    "v_add_f32 v172, v172, v174\n"
    "v_add_f32 v176, v176, v178\n"
    "v_add_f32 v180, v180, v182\n"
    "v_add_f32 v168, v168, v172\n"
    "v_add_f32 v176, v176, v180\n"
    "v_add_f32 v168, v168, v176\n"
    "v_cvt_pk_bf16_f32 v185, v168, v168\n"
    "v_and_b32 v185, 0xffff0000, v185\n"
    "v_sub_f32 v185, v168, v185\n"
    "v_cvt_pk_bf16_f32 v185, v168, v185\n"
    "s_lshl_b32 s35, s76, 9\n"
    "s_add_u32 s35, s35, 0x27000\n"
    "v_lshl_add_u32 v184, v0, 2, s35\n"
    "s_lshl_b32 s35, s75, 8\n"
    "v_add_u32 v15, s35, v184\n"
    "ds_write_b32 v15, v185\n"
    "s_lshl_b32 s35, s75, 6\n"
    "v_xor_b32 v184, s35, v184\n"
    "s_waitcnt lgkmcnt(0)\n"
    "s_barrier\n"
    "ds_read_b32 v188, v184\n"
    "ds_read_b32 v190, v184 offset:256\n"
    "v_mov_b32 v189, 0\n"
    "v_mov_b32 v191, 0\n"
    "v_mfma_f32_32x32x16_bf16 v[96:111], v[16:19], a[0:3], 0\n"
    "ds_read_b64_tr_b16 v[26:27], v1 offset:20992\n"
    "buffer_load_dwordx4 v13, s[40:43], s74 offen offset:1024 lds\n"
    "v_bfe_u32 v180, v0, 2, 2\n"
    "v_and_b32 v181, 3, v0\n"
    "v_bfe_u32 v182, v0, 4, 1\n"
    "v_mfma_f32_32x32x16_bf16 v[112:127], v[16:19], a[4:7], 0\n"
    "ds_read_b64_tr_b16 v[28:29], v1 offset:22528\n"
    "buffer_load_dwordx4 v12, s[44:47], s74 offen offset:2048 lds\n"
    "v_lshrrev_b32 v183, 5, v0\n"
    "s_lshr_b32 s34, s70, 3\n"
    "s_mul_i32 s34, s34, 0x2000\n"
    "v_mfma_f32_32x32x16_bf16 v[144:159], v[20:23], a[4:7], 0\n"
    "ds_read_b64_tr_b16 v[30:31], v1 offset:23040\n"
    "buffer_load_dwordx4 v13, s[44:47], s74 offen offset:3072 lds\n"
    "s_add_u32 s34, s34, 0x1c000\n"
    "s_lshl_b32 s35, s75, 11\n"
    "s_add_u32 s34, s34, s35\n"
    "v_mfma_f32_32x32x16_bf16 v[128:143], v[20:23], a[0:3], 0\n"
    "ds_read_b64_tr_b16 v[16:17], v1 offset:24576\n"
    "v_lshlrev_b32 v183, 12, v183\n"
    "v_lshl_add_u32 v183, v181, 3, v183\n"
    "v_lshl_add_u32 v182, v182, 3, v180\n"
    "v_lshl_add_u32 v183, v182, 7, v183\n"
    "s_waitcnt lgkmcnt(5)\n"
    "v_mfma_f32_32x32x8_bf16 v[32:47], v[188:189], v[160:161], v[32:47]\n"
    "v_lshrrev_b32 v180, 1, v180\n"
    "v_xor_b32 v180, s76, v180\n"
    "v_lshl_add_u32 v183, v180, 6, v183\n"
    "v_add_u32 v15, s34, v183\n"
    "ds_read_b64_tr_b16 v[168:169], v15\n"
    "v_mfma_f32_32x32x8_bf16 v[48:63], v[188:189], v[162:163], v[48:63]\n"
    "ds_read_b64_tr_b16 v[170:171], v15 offset:512\n"
    "ds_read_b64_tr_b16 v[172:173], v15 offset:32\n"
    "ds_read_b64_tr_b16 v[174:175], v15 offset:544\n"
    "v_mov_b32 v176, 0x3f803f80\n"
    "v_mov_b32 v177, 0x3f803f80\n"
    "s_waitcnt lgkmcnt(8)\n"
    "v_mfma_f32_32x32x8_bf16 v[64:79], v[190:191], v[160:161], v[64:79]\n"
    "v_mov_b32 v178, 0x3f803f80\n"
    "v_mov_b32 v179, 0x3f803f80\n"
    "s_mul_i32 s34, s82, 12\n"
    "s_lshl_b32 s35, s76, 1\n"
    "s_add_u32 s34, s34, s35\n"
    "v_mfma_f32_32x32x8_bf16 v[80:95], v[190:191], v[162:163], v[80:95]\n"
    "s_add_u32 s34, s34, s75\n"
    "s_add_u32 s34, s34, 4\n"
    "s_lshl_b32 s34, s34, 12\n"
    "s_add_u32 s34, s34, 0x990000\n"
    "s_and_b32 s35, s70, 7\n"
    "s_waitcnt lgkmcnt(7)\n"
    "v_mfma_f32_32x32x16_bf16 v[96:111], v[24:27], a[8:11], v[96:111]\n"
    "ds_read_b64_tr_b16 v[18:19], v1 offset:25088\n"
    "s_lshl_b32 s35, s35, 9\n"
    "s_add_u32 s34, s34, s35\n"
    "s_lshr_b32 s35, s70, 3\n"
    "s_lshl_b32 s35, s35, 8\n"
    "v_mfma_f32_32x32x16_bf16 v[112:127], v[24:27], a[12:15], v[112:127]\n"
    "ds_read_b64_tr_b16 v[20:21], v1 offset:26624\n"
    "s_add_u32 s34, s34, s35\n"
    "v_and_b32 v15, 31, v0\n"
    "v_lshlrev_b32 v15, 3, v15\n"
    "s_waitcnt lgkmcnt(4)\n"
    "v_mfma_f32_16x16x32_bf16 v[180:183], v[176:179], v[168:171], 0\n"
    "s_waitcnt lgkmcnt(2)\n"
    "v_mfma_f32_16x16x32_bf16 v[184:187], v[176:179], v[172:175], 0\n"
    "v_bfe_u32 v4, v0, 2, 2\n"
    "v_mfma_f32_32x32x16_bf16 v[144:159], v[28:31], a[12:15], v[144:159]\n"
    "ds_read_b64_tr_b16 v[22:23], v1 offset:27136\n"
    "v_lshrrev_b32 v5, 5, v0\n"
    "v_lshl_add_u32 v4, v5, 2, v4\n"
    "v_and_b32 v5, 3, v0\n"
    "v_lshlrev_b32 v5, 3, v5\n"
    "v_mfma_f32_32x32x16_bf16 v[128:143], v[28:31], a[8:11], v[128:143]\n"
    "ds_read_b64_tr_b16 v[24:25], v1 offset:28672\n"
    "v_cndmask_b32 v180, v180, v184, s[92:93]\n"
    "v_mov_b32 v181, s72\n"
    "buffer_store_dwordx2 v[180:181], v15, s[48:51], s34 offen sc1\n"
    "v_lshl_add_u32 v4, v4, 5, v5\n"
    "s_waitcnt lgkmcnt(3)\n"
    "v_mfma_f32_32x32x16_bf16 v[96:111], v[16:19], a[16:19], v[96:111]\n"
    "ds_read_b64_tr_b16 v[26:27], v1 offset:29184\n"
    "s_mul_i32 s11, s69, 0xc00\n"
    "s_add_u32 s11, s11, 0x24000\n"
    "v_add_u32 v4, s11, v4\n"
    "s_lshl_b32 s11, s75, 9\n"
    "v_mfma_f32_32x32x16_bf16 v[112:127], v[16:19], a[20:23], v[112:127]\n"
    "ds_read_b64_tr_b16 v[28:29], v1 offset:30720\n"
    "v_add_u32 v5, s11, v4\n"
    "ds_read_b64_tr_b16 v[192:193], v5\n"
    "ds_read_b64_tr_b16 v[194:195], v5 offset:256\n"
    "s_xor_b32 s11, s11, 0x200\n"
    "s_waitcnt lgkmcnt(5)\n"
    "v_mfma_f32_32x32x16_bf16 v[144:159], v[20:23], a[20:23], v[144:159]\n"
    "ds_read_b64_tr_b16 v[30:31], v1 offset:31232\n"
    "v_add_u32 v5, s11, v4\n"
    "ds_read_b64_tr_b16 v[196:197], v5\n"
    "ds_read_b64_tr_b16 v[198:199], v5 offset:256\n"
    "v_mov_b32 v4, 0x80008000\n"
    "v_mfma_f32_32x32x16_bf16 v[128:143], v[20:23], a[16:19], v[128:143]\n"
    "s_barrier\n"
    "ds_read_b64_tr_b16 v[16:17], v1 offset:32768\n"
    "s_mul_i32 s74, s67, 0x700\n"
    "s_add_u32 m0, s80, 0x14000\n"
    "s_nop 0\n"
    "buffer_load_dwordx4 v12, s[40:43], s74 offen lds\n"
    "s_waitcnt lgkmcnt(7)\n"
    "v_mfma_f32_32x32x16_bf16 v[96:111], v[24:27], a[24:27], v[96:111]\n"
    "ds_read_b64_tr_b16 v[18:19], v1 offset:33280\n"
    "buffer_load_dwordx4 v13, s[40:43], s74 offen offset:1024 lds\n"
    "v_cndmask_b32 v5, 0, v4, s[92:93]\n"
    "s_mul_i32 s11, s69, 0xc00\n"
    "s_add_u32 s11, s11, 0x24000\n"
    "v_mfma_f32_32x32x16_bf16 v[112:127], v[24:27], a[28:31], v[112:127]\n"
    "ds_read_b64_tr_b16 v[20:21], v1 offset:34816\n"
    "buffer_load_dwordx4 v12, s[44:47], s74 offen offset:2048 lds\n"
    "s_waitcnt lgkmcnt(7)\n"
    "v_xor_b32 v192, v5, v192\n"
    "v_xor_b32 v193, v5, v193\n"
    "s_waitcnt lgkmcnt(6)\n"
    "v_xor_b32 v194, v5, v194\n"
    "s_waitcnt lgkmcnt(5)\n"
    "v_mfma_f32_32x32x16_bf16 v[144:159], v[28:31], a[28:31], v[144:159]\n"
    "ds_read_b64_tr_b16 v[22:23], v1 offset:35328\n"
    "buffer_load_dwordx4 v13, s[44:47], s74 offen offset:3072 lds\n"
    "v_xor_b32 v195, v5, v195\n"
    "s_waitcnt lgkmcnt(5)\n"
    "v_xor_b32 v196, v5, v196\n"
    "v_xor_b32 v197, v5, v197\n"
    "v_mfma_f32_32x32x16_bf16 v[128:143], v[28:31], a[24:27], v[128:143]\n"
    "ds_read_b64_tr_b16 v[24:25], v1 offset:36864\n"
    "v_lshrrev_b32 v8, 2, v0\n"
    "s_mul_i32 s30, s69, 0xe000\n"
    "s_mov_b32 s31, 0xe00\n"
    "v_mul_lo_u32 v8, v8, s31\n"
    "v_and_b32 v251, 3, v0\n"
    "v_add_u32 v8, s30, v8\n"
    "v_lshl_add_u32 v8, v251, 4, v8\n"
    "v_xor_b32 v9, 32, v8\n"
    "s_lshl_b32 s30, s71, 6\n"
    "s_add_u32 s30, s30, 0xc00\n"
    "s_add_u32 s40, s20, s30\n"
    "s_addc_u32 s41, s21, 0\n"
    "s_sub_u32 s42, 0x1c0000, s30\n"
    "s_mov_b32 s43, 0x20000\n"
    "s_add_u32 s44, s22, s30\n"
    "s_addc_u32 s45, s23, 0\n"
    "s_sub_u32 s44, s44, 0x400\n"
    "s_subb_u32 s45, s45, 0\n"
    "s_sub_u32 s46, 0x1c0400, s30\n"
    "s_mov_b32 s47, 0x20000\n"
    "s_mul_i32 s74, s60, 0x700\n"
    "s_add_u32 m0, s81, 0x18000\n"
    "s_nop 0\n"
    "buffer_load_dwordx4 v8, s[40:43], s74 offen lds\n"
    "s_waitcnt lgkmcnt(3)\n"
    "v_mfma_f32_32x32x16_bf16 v[96:111], v[16:19], a[32:35], v[96:111]\n"
    "ds_read_b64_tr_b16 v[26:27], v1 offset:37376\n"
    "buffer_load_dwordx4 v9, s[44:47], s74 offen offset:1024 lds\n"
    "v_xor_b32 v198, v5, v198\n"
    "v_xor_b32 v199, v5, v199\n"
    "v_and_b32 v4, 31, v0\n"
    "v_mfma_f32_32x32x16_bf16 v[112:127], v[16:19], a[36:39], v[112:127]\n"
    "ds_read_b64_tr_b16 v[28:29], v1 offset:38912\n"
    "s_mul_i32 s74, s61, 0x700\n"
    "s_add_u32 m0, s81, 0x1a000\n"
    "s_nop 0\n"
    "buffer_load_dwordx4 v8, s[40:43], s74 offen lds\n"
    "v_mul_u32_u24 v4, 0x48, v4\n"
    "s_waitcnt lgkmcnt(3)\n"
    "v_mfma_f32_32x32x16_bf16 v[144:159], v[20:23], a[36:39], v[144:159]\n"
    "ds_read_b64_tr_b16 v[30:31], v1 offset:39424\n"
    "buffer_load_dwordx4 v9, s[44:47], s74 offen offset:1024 lds\n"
    "v_lshrrev_b32 v5, 5, v0\n"
    "v_lshl_add_u32 v4, v5, 3, v4\n"
    "v_add_u32 v2, s11, v4\n"
    "v_mfma_f32_32x32x16_bf16 v[128:143], v[20:23], a[32:35], v[128:143]\n"
    "ds_read_b64_tr_b16 v[16:17], v1 offset:40960\n"
    "v_bfe_u32 v4, v0, 2, 2\n"
    "v_lshl_add_u32 v4, v5, 3, v4\n"
    "v_mul_u32_u24 v4, 0x48, v4\n"
    "v_and_b32 v5, 3, v0\n"
    "s_waitcnt lgkmcnt(3)\n"
    "v_mfma_f32_32x32x16_bf16 v[96:111], v[24:27], a[40:43], v[96:111]\n"
    "ds_read_b64_tr_b16 v[18:19], v1 offset:41472\n"
    "v_lshl_add_u32 v4, v5, 3, v4\n"
    "v_bfe_u32 v5, v0, 4, 1\n"
    "v_lshl_add_u32 v4, v5, 5, v4\n"
    "v_add_u32 v3, s11, v4\n"
    "v_mfma_f32_32x32x16_bf16 v[112:127], v[24:27], a[44:47], v[112:127]\n"
    "ds_read_b64_tr_b16 v[20:21], v1 offset:43008\n"
    "v_cvt_pk_bf16_f32 v32, v32, v33\n"
    "v_cvt_pk_bf16_f32 v33, v34, v35\n"
    "v_cvt_pk_bf16_f32 v34, v36, v37\n"
    "v_cvt_pk_bf16_f32 v35, v38, v39\n"
    "s_waitcnt lgkmcnt(3)\n"
    "v_mfma_f32_32x32x16_bf16 v[144:159], v[28:31], a[44:47], v[144:159]\n"
    "ds_read_b64_tr_b16 v[22:23], v1 offset:43520\n"
    "v_cvt_pk_bf16_f32 v36, v40, v41\n"
    "v_cvt_pk_bf16_f32 v37, v42, v43\n"
    "v_cvt_pk_bf16_f32 v38, v44, v45\n"
    "v_cvt_pk_bf16_f32 v39, v46, v47\n"
    "v_mfma_f32_32x32x16_bf16 v[128:143], v[28:31], a[40:43], v[128:143]\n"
    "ds_read_b64_tr_b16 v[24:25], v1 offset:45056\n"
    "v_cvt_pk_bf16_f32 v48, v48, v49\n"
    "v_cvt_pk_bf16_f32 v49, v50, v51\n"
    "v_cvt_pk_bf16_f32 v50, v52, v53\n"
    "v_cvt_pk_bf16_f32 v51, v54, v55\n"
    "s_waitcnt lgkmcnt(3)\n"
    "v_mfma_f32_32x32x16_bf16 v[96:111], v[16:19], a[48:51], v[96:111]\n"
    "ds_read_b64_tr_b16 v[26:27], v1 offset:45568\n"
    "v_cvt_pk_bf16_f32 v52, v56, v57\n"
    "v_cvt_pk_bf16_f32 v53, v58, v59\n"
    "v_cvt_pk_bf16_f32 v54, v60, v61\n"
    "v_cvt_pk_bf16_f32 v55, v62, v63\n"
    "v_mfma_f32_32x32x16_bf16 v[112:127], v[16:19], a[52:55], v[112:127]\n"
    "ds_read_b64_tr_b16 v[28:29], v1 offset:47104\n"
    "v_mul_f32 v64, v64, v166\n"
    "v_mul_f32 v65, v65, v166\n"
    "v_mul_f32 v66, v66, v166\n"
    "v_mul_f32 v67, v67, v166\n"
    "s_waitcnt lgkmcnt(3)\n"
    "v_mfma_f32_32x32x16_bf16 v[144:159], v[20:23], a[52:55], v[144:159]\n"
    "ds_read_b64_tr_b16 v[30:31], v1 offset:47616\n"
    "v_mul_f32 v68, v68, v166\n"
    "v_mul_f32 v69, v69, v166\n"
    "v_mul_f32 v70, v70, v166\n"
    "v_mul_f32 v71, v71, v166\n"
    "v_mfma_f32_32x32x16_bf16 v[128:143], v[20:23], a[48:51], v[128:143]\n"
    "s_barrier\n"
    "v_add_u32 v1, 0xfffe8000, v1\n"
    "ds_read_b64_tr_b16 v[16:17], v1\n"
    "s_mul_i32 s74, s62, 0x700\n"
    "s_add_u32 m0, s81, 0x1c000\n"
    "s_nop 0\n"
    "buffer_load_dwordx4 v8, s[40:43], s74 offen lds\n"
    "s_waitcnt lgkmcnt(3)\n"
    "v_mfma_f32_32x32x16_bf16 v[96:111], v[24:27], a[56:59], v[96:111]\n"
    "ds_read_b64_tr_b16 v[18:19], v1 offset:512\n"
    "buffer_load_dwordx4 v9, s[44:47], s74 offen offset:1024 lds\n"
    "v_mul_f32 v72, v72, v166\n"
    "v_mul_f32 v73, v73, v166\n"
    "v_mul_f32 v74, v74, v166\n"
    "v_mfma_f32_32x32x16_bf16 v[112:127], v[24:27], a[60:63], v[112:127]\n"
    "ds_read_b64_tr_b16 v[20:21], v1 offset:2048\n"
    "s_mul_i32 s74, s63, 0x700\n"
    "s_add_u32 m0, s81, 0x1e000\n"
    "s_nop 0\n"
    "buffer_load_dwordx4 v8, s[40:43], s74 offen lds\n"
    "v_mul_f32 v75, v75, v166\n"
    "s_waitcnt lgkmcnt(3)\n"
    "v_mfma_f32_32x32x16_bf16 v[144:159], v[28:31], a[60:63], v[144:159]\n"
    "ds_read_b64_tr_b16 v[22:23], v1 offset:2560\n"
    "buffer_load_dwordx4 v9, s[44:47], s74 offen offset:1024 lds\n"
    "v_mul_f32 v76, v76, v166\n"
    "v_mul_f32 v77, v77, v166\n"
    "v_mul_f32 v78, v78, v166\n"
    "v_mfma_f32_32x32x16_bf16 v[128:143], v[28:31], a[56:59], v[128:143]\n"
    "ds_read_b64_tr_b16 v[24:25], v1 offset:4096\n"
    "v_mul_f32 v79, v79, v166\n"
    "v_cvt_pk_bf16_f32 v64, v64, v65\n"
    "v_cvt_pk_bf16_f32 v65, v66, v67\n"
    "v_cvt_pk_bf16_f32 v66, v68, v69\n"
    "v_mfma_f32_32x32x16_bf16 v[200:215], v[192:195], v[32:35], 0\n"
    "v_cvt_pk_bf16_f32 v67, v70, v71\n"
    "v_cvt_pk_bf16_f32 v68, v72, v73\n"
    "v_cvt_pk_bf16_f32 v69, v74, v75\n"
    "v_cvt_pk_bf16_f32 v70, v76, v77\n"
    "v_cvt_pk_bf16_f32 v71, v78, v79\n"
    "s_waitcnt lgkmcnt(3)\n"
    "v_mfma_f32_32x32x16_bf16 v[96:111], v[16:19], a[64:67], v[96:111]\n"
    "ds_read_b64_tr_b16 v[26:27], v1 offset:4608\n"
    "ds_write_b64 v2, v[64:65]\n"
    "ds_write_b64 v2, v[66:67] offset:16\n"
    "ds_write_b64 v2, v[68:69] offset:32\n"
    "ds_write_b64 v2, v[70:71] offset:48\n"
    "v_mfma_f32_32x32x16_bf16 v[112:127], v[16:19], a[68:71], v[112:127]\n"
    "ds_read_b64_tr_b16 v[28:29], v1 offset:6144\n"
    "ds_read_b64_tr_b16 v[232:233], v3\n"
    "ds_read_b64_tr_b16 v[234:235], v3 offset:288\n"
    "ds_read_b64_tr_b16 v[236:237], v3 offset:1152\n"
    "ds_read_b64_tr_b16 v[238:239], v3 offset:1440\n"
    "s_waitcnt lgkmcnt(11)\n"
    "v_mfma_f32_32x32x16_bf16 v[144:159], v[20:23], a[68:71], v[144:159]\n"
    "ds_read_b64_tr_b16 v[30:31], v1 offset:6656\n"
    "v_mul_f32 v80, v80, v167\n"
    "v_mul_f32 v81, v81, v167\n"
    "v_mul_f32 v82, v82, v167\n"
    "v_mul_f32 v83, v83, v167\n"
    "v_mfma_f32_32x32x16_bf16 v[128:143], v[20:23], a[64:67], v[128:143]\n"
    "ds_read_b64_tr_b16 v[16:17], v1 offset:8192\n"
    "v_mul_f32 v84, v84, v167\n"
    "v_mul_f32 v85, v85, v167\n"
    "v_mul_f32 v86, v86, v167\n"
    "v_mul_f32 v87, v87, v167\n"
    "s_waitcnt lgkmcnt(11)\n"
    "v_mfma_f32_32x32x16_bf16 v[96:111], v[24:27], a[72:75], v[96:111]\n"
    "ds_read_b64_tr_b16 v[18:19], v1 offset:8704\n"
    "v_mul_f32 v88, v88, v167\n"
    "v_mul_f32 v89, v89, v167\n"
    "v_mul_f32 v90, v90, v167\n"
    "v_mul_f32 v91, v91, v167\n"
    "v_mfma_f32_32x32x16_bf16 v[112:127], v[24:27], a[76:79], v[112:127]\n"
    "ds_read_b64_tr_b16 v[20:21], v1 offset:10240\n"
    "v_mul_f32 v92, v92, v167\n"
    "v_mul_f32 v93, v93, v167\n"
    "v_mul_f32 v94, v94, v167\n"
    "v_mul_f32 v95, v95, v167\n"
    "s_waitcnt lgkmcnt(3)\n"
    "v_mfma_f32_32x32x16_bf16 v[144:159], v[28:31], a[76:79], v[144:159]\n"
    "ds_read_b64_tr_b16 v[22:23], v1 offset:10752\n"
    "v_cvt_pk_bf16_f32 v80, v80, v81\n"
    "v_cvt_pk_bf16_f32 v81, v82, v83\n"
    "v_cvt_pk_bf16_f32 v82, v84, v85\n"
    "v_cvt_pk_bf16_f32 v83, v86, v87\n"
    "v_mfma_f32_32x32x16_bf16 v[128:143], v[28:31], a[72:75], v[128:143]\n"
    "ds_read_b64_tr_b16 v[24:25], v1 offset:12288\n"
    "v_cvt_pk_bf16_f32 v84, v88, v89\n"
    "v_cvt_pk_bf16_f32 v85, v90, v91\n"
    "v_cvt_pk_bf16_f32 v86, v92, v93\n"
    "v_cvt_pk_bf16_f32 v87, v94, v95\n"
    "s_waitcnt lgkmcnt(3)\n"
    "v_mfma_f32_32x32x16_bf16 v[96:111], v[16:19], a[80:83], v[96:111]\n"
    "ds_read_b64_tr_b16 v[26:27], v1 offset:12800\n"
    "ds_write_b64 v2, v[80:81]\n"
    "ds_write_b64 v2, v[82:83] offset:16\n"
    "ds_write_b64 v2, v[84:85] offset:32\n"
    "ds_write_b64 v2, v[86:87] offset:48\n"
    "v_mfma_f32_32x32x16_bf16 v[112:127], v[16:19], a[84:87], v[112:127]\n"
    "ds_read_b64_tr_b16 v[28:29], v1 offset:14336\n"
    "ds_read_b64_tr_b16 v[240:241], v3\n"
    "ds_read_b64_tr_b16 v[242:243], v3 offset:288\n"
    "ds_read_b64_tr_b16 v[244:245], v3 offset:1152\n"
    "ds_read_b64_tr_b16 v[246:247], v3 offset:1440\n"
    "s_waitcnt lgkmcnt(11)\n"
    "v_mfma_f32_32x32x16_bf16 v[144:159], v[20:23], a[84:87], v[144:159]\n"
    "ds_read_b64_tr_b16 v[30:31], v1 offset:14848\n"
    "v_mov_b32 v4, 0x3f803f80\n"
    "v_max3_f32 v248, v200, v201, v202\n"
    "v_max3_f32 v248, v248, v203, v204\n"
    "v_max3_f32 v248, v248, v205, v206\n"
    "v_mfma_f32_32x32x16_bf16 v[128:143], v[20:23], a[80:83], v[128:143]\n"
    "s_barrier\n"
    "ds_read_b64_tr_b16 v[16:17], v1 offset:16384\n"
    "s_mul_i32 s74, s64, 0x700\n"
    "s_add_u32 m0, s81, 0x20000\n"
    "s_nop 0\n"
    "buffer_load_dwordx4 v8, s[40:43], s74 offen lds\n"
    "s_waitcnt lgkmcnt(11)\n"
    "v_mfma_f32_32x32x16_bf16 v[96:111], v[24:27], a[88:91], v[96:111]\n"
    "ds_read_b64_tr_b16 v[18:19], v1 offset:16896\n"
    "buffer_load_dwordx4 v9, s[44:47], s74 offen offset:1024 lds\n"
    "v_max_f32 v248, v248, v207\n"
    "v_max3_f32 v249, v208, v209, v210\n"
    "v_max3_f32 v249, v249, v211, v212\n"
    "v_mfma_f32_32x32x16_bf16 v[112:127], v[24:27], a[92:95], v[112:127]\n"
    "ds_read_b64_tr_b16 v[20:21], v1 offset:18432\n"
    "s_mul_i32 s74, s65, 0x700\n"
    "s_add_u32 m0, s81, 0x22000\n"
    "s_nop 0\n"
    "buffer_load_dwordx4 v8, s[40:43], s74 offen lds\n"
    "v_max3_f32 v249, v249, v213, v214\n"
    "s_waitcnt lgkmcnt(3)\n"
    "v_mfma_f32_32x32x16_bf16 v[144:159], v[28:31], a[92:95], v[144:159]\n"
    "ds_read_b64_tr_b16 v[22:23], v1 offset:18944\n"
    "buffer_load_dwordx4 v9, s[44:47], s74 offen offset:1024 lds\n"
    "v_max_f32 v249, v249, v215\n"
    "v_mfma_f32_32x32x16_bf16 v[128:143], v[28:31], a[88:91], v[128:143]\n"
    "ds_read_b64_tr_b16 v[24:25], v1 offset:20480\n"
    "v_permlane32_swap_b32 v248, v249\n"
    "v_max_f32 v248, v248, v249\n"
    "v_mul_f32 v248, -v248, v164\n"
    "v_mov_b32 v249, v248\n"
    "s_waitcnt lgkmcnt(3)\n"
    "v_mfma_f32_32x32x16_bf16 v[96:111], v[16:19], a[96:99], v[96:111]\n"
    "ds_read_b64_tr_b16 v[26:27], v1 offset:20992\n"
    "v_permlane32_swap_b32 v248, v249\n"
    "v_fma_f32 v200, v200, v164, v248\n"
    "v_fma_f32 v201, v201, v164, v248\n"
    "v_fma_f32 v202, v202, v164, v248\n"
    "v_mfma_f32_32x32x16_bf16 v[112:127], v[16:19], a[100:103], v[112:127]\n"
    "ds_read_b64_tr_b16 v[28:29], v1 offset:22528\n"
    "v_fma_f32 v203, v203, v164, v248\n"
    "v_fma_f32 v204, v204, v164, v248\n"
    "v_fma_f32 v205, v205, v164, v248\n"
    "v_fma_f32 v206, v206, v164, v248\n"
    "s_waitcnt lgkmcnt(3)\n"
    "v_mfma_f32_32x32x16_bf16 v[144:159], v[20:23], a[100:103], v[144:159]\n"
    "ds_read_b64_tr_b16 v[30:31], v1 offset:23040\n"
    "v_fma_f32 v207, v207, v164, v248\n"
    "v_fma_f32 v208, v208, v164, v249\n"
    "v_fma_f32 v209, v209, v164, v249\n"
    "v_fma_f32 v210, v210, v164, v249\n"
    "v_mfma_f32_32x32x16_bf16 v[128:143], v[20:23], a[96:99], v[128:143]\n"
    "ds_read_b64_tr_b16 v[16:17], v1 offset:24576\n"
    "v_fma_f32 v211, v211, v164, v249\n"
    "v_fma_f32 v212, v212, v164, v249\n"
    "v_fma_f32 v213, v213, v164, v249\n"
    "v_fma_f32 v214, v214, v164, v249\n"
    "s_waitcnt lgkmcnt(3)\n"
    "v_mfma_f32_32x32x16_bf16 v[96:111], v[24:27], a[104:107], v[96:111]\n"
    "ds_read_b64_tr_b16 v[18:19], v1 offset:25088\n"
    "v_fma_f32 v215, v215, v164, v249\n"
    "v_exp_f32 v200, v200\n"
    "v_mfma_f32_32x32x16_bf16 v[112:127], v[24:27], a[108:111], v[112:127]\n"
    "ds_read_b64_tr_b16 v[20:21], v1 offset:26624\n"
    "v_exp_f32 v201, v201\n"
    "v_exp_f32 v202, v202\n"
    "s_waitcnt lgkmcnt(3)\n"
    "v_mfma_f32_32x32x16_bf16 v[144:159], v[28:31], a[108:111], v[144:159]\n"
    "ds_read_b64_tr_b16 v[22:23], v1 offset:27136\n"
    "v_exp_f32 v203, v203\n"
    "v_exp_f32 v204, v204\n"
    "v_mfma_f32_32x32x16_bf16 v[128:143], v[28:31], a[104:107], v[128:143]\n"
    "ds_read_b64_tr_b16 v[24:25], v1 offset:28672\n"
    "v_exp_f32 v205, v205\n"
    "v_exp_f32 v206, v206\n"
    "s_waitcnt lgkmcnt(3)\n"
    "v_mfma_f32_32x32x16_bf16 v[96:111], v[16:19], a[112:115], v[96:111]\n"
    "ds_read_b64_tr_b16 v[26:27], v1 offset:29184\n"
    "v_exp_f32 v207, v207\n"
    "v_exp_f32 v208, v208\n"
    "v_mfma_f32_32x32x16_bf16 v[112:127], v[16:19], a[116:119], v[112:127]\n"
    "ds_read_b64_tr_b16 v[28:29], v1 offset:30720\n"
    "v_exp_f32 v209, v209\n"
    "v_exp_f32 v210, v210\n"
    "s_waitcnt lgkmcnt(3)\n"
    "v_mfma_f32_32x32x16_bf16 v[144:159], v[20:23], a[116:119], v[144:159]\n"
    "ds_read_b64_tr_b16 v[30:31], v1 offset:31232\n"
    "v_exp_f32 v211, v211\n"
    "v_exp_f32 v212, v212\n"
    "v_mfma_f32_32x32x16_bf16 v[128:143], v[20:23], a[112:115], v[128:143]\n"
    "s_barrier\n"
    "ds_read_b64_tr_b16 v[16:17], v1 offset:32768\n"
    "s_mul_i32 s74, s66, 0x700\n"
    "s_add_u32 m0, s81, 0\n"
    "s_nop 0\n"
    "buffer_load_dwordx4 v8, s[40:43], s74 offen lds\n"
    "s_waitcnt lgkmcnt(3)\n"
    "v_mfma_f32_32x32x16_bf16 v[96:111], v[24:27], a[120:123], v[96:111]\n"
    "ds_read_b64_tr_b16 v[18:19], v1 offset:33280\n"
    "buffer_load_dwordx4 v9, s[44:47], s74 offen offset:1024 lds\n"
    "v_exp_f32 v213, v213\n"
    "v_add_f32 v248, v200, v201\n"
    "v_mfma_f32_32x32x16_bf16 v[112:127], v[24:27], a[124:127], v[112:127]\n"
    "ds_read_b64_tr_b16 v[20:21], v1 offset:34816\n"
    "s_mul_i32 s74, s67, 0x700\n"
    "s_add_u32 m0, s81, 0x2000\n"
    "s_nop 0\n"
    "buffer_load_dwordx4 v8, s[40:43], s74 offen lds\n"
    "v_add_f32 v248, v248, v202\n"
    "s_waitcnt lgkmcnt(3)\n"
    "v_mfma_f32_32x32x16_bf16 v[144:159], v[28:31], a[124:127], v[144:159]\n"
    "ds_read_b64_tr_b16 v[22:23], v1 offset:35328\n"
    "buffer_load_dwordx4 v9, s[44:47], s74 offen offset:1024 lds\n"
    "v_exp_f32 v214, v214\n"
    "v_add_f32 v248, v248, v203\n"
    "v_mfma_f32_32x32x16_bf16 v[128:143], v[28:31], a[120:123], v[128:143]\n"
    "ds_read_b64_tr_b16 v[24:25], v1 offset:36864\n"
    "v_exp_f32 v215, v215\n"
    "v_add_f32 v248, v248, v204\n"
    "v_add_f32 v248, v248, v205\n"
    "s_waitcnt lgkmcnt(3)\n"
    "v_mfma_f32_32x32x16_bf16 v[96:111], v[16:19], a[128:131], v[96:111]\n"
    "ds_read_b64_tr_b16 v[26:27], v1 offset:37376\n"
    "v_add_f32 v248, v248, v206\n"
    "v_add_f32 v248, v248, v207\n"
    "v_add_f32 v249, v208, v209\n"
    "v_add_f32 v249, v249, v210\n"
    "v_mfma_f32_32x32x16_bf16 v[112:127], v[16:19], a[132:135], v[112:127]\n"
    "ds_read_b64_tr_b16 v[28:29], v1 offset:38912\n"
    "v_add_f32 v249, v249, v211\n"
    "v_add_f32 v249, v249, v212\n"
    "v_add_f32 v249, v249, v213\n"
    "v_add_f32 v249, v249, v214\n"
    "s_waitcnt lgkmcnt(3)\n"
    "v_mfma_f32_32x32x16_bf16 v[144:159], v[20:23], a[132:135], v[144:159]\n"
    "ds_read_b64_tr_b16 v[30:31], v1 offset:39424\n"
    "v_add_f32 v249, v249, v215\n"
    "v_cndmask_b32 v40, v232, v4, s[92:93]\n"
    "v_cndmask_b32 v41, v233, v4, s[92:93]\n"
    "v_permlane32_swap_b32 v248, v249\n"
    "v_mfma_f32_32x32x16_bf16 v[216:231], v[196:199], v[36:39], 0\n"
    "v_add_f32 v248, v248, v249\n"
    "v_rcp_f32 v248, v248\n"
    "v_cndmask_b32 v42, v234, v4, s[92:93]\n"
    "v_mov_b32 v249, v248\n"
    "v_mfma_f32_32x32x16_bf16 v[128:143], v[20:23], a[128:131], v[128:143]\n"
    "ds_read_b64_tr_b16 v[16:17], v1 offset:40960\n"
    "v_permlane32_swap_b32 v248, v249\n"
    "v_mul_f32 v200, v200, v248\n"
    "v_mul_f32 v201, v201, v248\n"
    "v_mul_f32 v202, v202, v248\n"
    "s_waitcnt lgkmcnt(3)\n"
    "v_mfma_f32_32x32x16_bf16 v[96:111], v[24:27], a[136:139], v[96:111]\n"
    "ds_read_b64_tr_b16 v[18:19], v1 offset:41472\n"
    "v_mul_f32 v203, v203, v248\n"
    "v_mul_f32 v204, v204, v248\n"
    "v_mul_f32 v205, v205, v248\n"
    "v_mul_f32 v206, v206, v248\n"
    "v_mfma_f32_32x32x16_bf16 v[112:127], v[24:27], a[140:143], v[112:127]\n"
    "ds_read_b64_tr_b16 v[20:21], v1 offset:43008\n"
    "v_mul_f32 v207, v207, v248\n"
    "v_mul_f32 v208, v208, v249\n"
    "v_mul_f32 v209, v209, v249\n"
    "v_mul_f32 v210, v210, v249\n"
    "s_waitcnt lgkmcnt(3)\n"
    "v_mfma_f32_32x32x16_bf16 v[144:159], v[28:31], a[140:143], v[144:159]\n"
    "ds_read_b64_tr_b16 v[22:23], v1 offset:43520\n"
    "v_mul_f32 v211, v211, v249\n"
    "v_mul_f32 v212, v212, v249\n"
    "v_mul_f32 v213, v213, v249\n"
    "v_mul_f32 v214, v214, v249\n"
    "v_mfma_f32_32x32x16_bf16 v[128:143], v[28:31], a[136:139], v[128:143]\n"
    "ds_read_b64_tr_b16 v[24:25], v1 offset:45056\n"
    "v_mul_f32 v215, v215, v249\n"
    "v_cvt_pk_bf16_f32 v200, v200, v201\n"
    "v_cvt_pk_bf16_f32 v201, v202, v203\n"
    "v_cvt_pk_bf16_f32 v202, v204, v205\n"
    "s_waitcnt lgkmcnt(3)\n"
    "v_mfma_f32_32x32x16_bf16 v[96:111], v[16:19], a[144:147], v[96:111]\n"
    "ds_read_b64_tr_b16 v[26:27], v1 offset:45568\n"
    "v_cvt_pk_bf16_f32 v203, v206, v207\n"
    "v_cvt_pk_bf16_f32 v204, v208, v209\n"
    "v_cvt_pk_bf16_f32 v205, v210, v211\n"
    "v_cvt_pk_bf16_f32 v206, v212, v213\n"
    "v_mfma_f32_32x32x16_bf16 v[112:127], v[16:19], a[148:151], v[112:127]\n"
    "ds_read_b64_tr_b16 v[28:29], v1 offset:47104\n"
    "v_cvt_pk_bf16_f32 v207, v214, v215\n"
    "ds_write_b64 v2, v[200:201]\n"
    "ds_write_b64 v2, v[202:203] offset:16\n"
    "ds_write_b64 v2, v[204:205] offset:32\n"
    "s_waitcnt lgkmcnt(6)\n"
    "v_mfma_f32_32x32x16_bf16 v[144:159], v[20:23], a[148:151], v[144:159]\n"
    "ds_read_b64_tr_b16 v[30:31], v1 offset:47616\n"
    "ds_write_b64 v2, v[206:207] offset:48\n"
    "ds_read_b64_tr_b16 v[208:209], v3\n"
    "ds_read_b64_tr_b16 v[210:211], v3 offset:288\n"
    "ds_read_b64_tr_b16 v[212:213], v3 offset:1152\n"
    "v_mfma_f32_32x32x16_bf16 v[128:143], v[20:23], a[144:147], v[128:143]\n"
    "s_barrier\n"
    "ds_read_b64_tr_b16 v[16:17], v1 offset:49152\n"
    "v_lshrrev_b32 v6, 3, v0\n"
    "s_mul_i32 s30, s69, 0x4000\n"
    "s_mov_b32 s31, 0x400\n"
    "v_mul_lo_u32 v6, v6, s31\n"
    "v_bfe_u32 v251, v0, 4, 2\n"
    "v_lshlrev_b32 v251, 1, v251\n"
    "v_bitop3_b32 v251, v0, 7, v251 bitop3:0x6a\n"
    "v_lshl_add_u32 v6, v251, 4, v6\n"
    "v_add_u32 v6, s30, v6\n"
    "v_add_u32 v7, 0x1c00, v6\n"
    "s_lshl_b32 s30, s79, 7\n"
    "s_add_u32 s52, s24, s30\n"
    "s_addc_u32 s53, s25, 0\n"
    "s_sub_u32 s54, 0x1c0000, s30\n"
    "s_mov_b32 s55, 0x20000\n"
    "s_mul_i32 s74, s77, 0x80000\n"
    "s_add_u32 s74, s74, 0\n"
    "s_add_u32 m0, s81, 0x4000\n"
    "s_nop 0\n"
    "buffer_load_dwordx4 v6, s[52:55], s74 offen lds\n"
    "s_waitcnt lgkmcnt(10)\n"
    "v_mfma_f32_32x32x16_bf16 v[96:111], v[24:27], a[152:155], v[96:111]\n"
    "ds_read_b64_tr_b16 v[18:19], v1 offset:49664\n"
    "buffer_load_dwordx4 v7, s[52:55], s74 offen offset:1024 lds\n"
    "ds_read_b64_tr_b16 v[214:215], v3 offset:1440\n"
    "v_cndmask_b32 v43, v235, v4, s[92:93]\n"
    "v_cndmask_b32 v44, v236, v4, s[92:93]\n"
    "v_mfma_f32_32x32x16_bf16 v[112:127], v[24:27], a[156:159], v[112:127]\n"
    "ds_read_b64_tr_b16 v[20:21], v1 offset:51200\n"
    "s_mul_i32 s74, s77, 0x80000\n"
    "s_add_u32 s74, s74, 0x20000\n"
    "s_add_u32 m0, s81, 0x6000\n"
    "s_nop 0\n"
    "buffer_load_dwordx4 v6, s[52:55], s74 offen lds\n"
    "s_waitcnt lgkmcnt(5)\n"
    "v_mfma_f32_32x32x16_bf16 v[64:79], v[208:211], v[40:43], 0\n"
    "v_cndmask_b32 v45, v237, v4, s[92:93]\n"
    "v_cndmask_b32 v46, v238, v4, s[92:93]\n"
    "v_cndmask_b32 v47, v239, v4, s[92:93]\n"
    "v_max3_f32 v250, v216, v217, v218\n"
    "v_max3_f32 v250, v250, v219, v220\n"
    "v_mfma_f32_32x32x16_bf16 v[144:159], v[28:31], a[156:159], v[144:159]\n"
    "ds_read_b64_tr_b16 v[22:23], v1 offset:51712\n"
    "buffer_load_dwordx4 v7, s[52:55], s74 offen offset:1024 lds\n"
    "v_max3_f32 v250, v250, v221, v222\n"
    "v_max_f32 v250, v250, v223\n"
    "v_max3_f32 v5, v224, v225, v226\n"
    "v_mfma_f32_32x32x16_bf16 v[128:143], v[28:31], a[152:155], v[128:143]\n"
    "ds_read_b64_tr_b16 v[24:25], v1 offset:53248\n"
    "v_max3_f32 v5, v5, v227, v228\n"
    "v_max3_f32 v5, v5, v229, v230\n"
    "v_max_f32 v5, v5, v231\n"
    "s_waitcnt lgkmcnt(3)\n"
    "v_mfma_f32_32x32x16_bf16 v[64:79], v[212:215], v[44:47], v[64:79]\n"
    "s_mul_i32 s34, s82, 12\n"
    "v_permlane32_swap_b32 v250, v5\n"
    "v_max_f32 v250, v250, v5\n"
    "v_mul_f32 v250, -v250, v164\n"
    "v_mov_b32 v5, v250\n"
    "v_mfma_f32_32x32x16_bf16 v[96:111], v[16:19], a[160:163], v[96:111]\n"
    "ds_read_b64_tr_b16 v[26:27], v1 offset:53760\n"
    "v_permlane32_swap_b32 v250, v5\n"
    "v_fma_f32 v216, v216, v164, v250\n"
    "v_fma_f32 v217, v217, v164, v250\n"
    "v_fma_f32 v218, v218, v164, v250\n"
    "v_mfma_f32_32x32x16_bf16 v[112:127], v[16:19], a[164:167], v[112:127]\n"
    "ds_read_b64_tr_b16 v[28:29], v1 offset:55296\n"
    "v_fma_f32 v219, v219, v164, v250\n"
    "v_fma_f32 v220, v220, v164, v250\n"
    "v_fma_f32 v221, v221, v164, v250\n"
    "v_fma_f32 v222, v222, v164, v250\n"
    "s_waitcnt lgkmcnt(3)\n"
    "v_mfma_f32_32x32x16_bf16 v[144:159], v[20:23], a[164:167], v[144:159]\n"
    "ds_read_b64_tr_b16 v[30:31], v1 offset:55808\n"
    "v_fma_f32 v223, v223, v164, v250\n"
    "v_fma_f32 v224, v224, v164, v5\n"
    "v_fma_f32 v225, v225, v164, v5\n"
    "v_fma_f32 v226, v226, v164, v5\n"
    "v_mfma_f32_32x32x16_bf16 v[128:143], v[20:23], a[160:163], v[128:143]\n"
    "ds_read_b64_tr_b16 v[16:17], v1 offset:57344\n"
    "v_fma_f32 v227, v227, v164, v5\n"
    "v_fma_f32 v228, v228, v164, v5\n"
    "v_fma_f32 v229, v229, v164, v5\n"
    "v_fma_f32 v230, v230, v164, v5\n"
    "s_waitcnt lgkmcnt(3)\n"
    "v_mfma_f32_32x32x16_bf16 v[96:111], v[24:27], a[168:171], v[96:111]\n"
    "ds_read_b64_tr_b16 v[18:19], v1 offset:57856\n"
    "v_fma_f32 v231, v231, v164, v5\n"
    "v_exp_f32 v216, v216\n"
    "s_lshl_b32 s35, s76, 1\n"
    "v_mfma_f32_32x32x16_bf16 v[112:127], v[24:27], a[172:175], v[112:127]\n"
    "ds_read_b64_tr_b16 v[20:21], v1 offset:59392\n"
    "v_exp_f32 v217, v217\n"
    "v_exp_f32 v218, v218\n"
    "s_waitcnt lgkmcnt(3)\n"
    "v_mfma_f32_32x32x16_bf16 v[144:159], v[28:31], a[172:175], v[144:159]\n"
    "ds_read_b64_tr_b16 v[22:23], v1 offset:59904\n"
    "v_exp_f32 v219, v219\n"
    "v_exp_f32 v220, v220\n"
    "v_mfma_f32_32x32x16_bf16 v[128:143], v[28:31], a[168:171], v[128:143]\n"
    "ds_read_b64_tr_b16 v[24:25], v1 offset:61440\n"
    "v_exp_f32 v221, v221\n"
    "v_exp_f32 v222, v222\n"
    "s_waitcnt lgkmcnt(3)\n"
    "v_mfma_f32_32x32x16_bf16 v[96:111], v[16:19], a[176:179], v[96:111]\n"
    "ds_read_b64_tr_b16 v[26:27], v1 offset:61952\n"
    "v_exp_f32 v223, v223\n"
    "v_exp_f32 v224, v224\n"
    "v_mfma_f32_32x32x16_bf16 v[112:127], v[16:19], a[180:183], v[112:127]\n"
    "ds_read_b64_tr_b16 v[28:29], v1 offset:63488\n"
    "v_exp_f32 v225, v225\n"
    "v_exp_f32 v226, v226\n"
    "s_waitcnt lgkmcnt(3)\n"
    "v_mfma_f32_32x32x16_bf16 v[144:159], v[20:23], a[180:183], v[144:159]\n"
    "ds_read_b64_tr_b16 v[30:31], v1 offset:64000\n"
    "v_exp_f32 v227, v227\n"
    "v_exp_f32 v228, v228\n"
    "v_mfma_f32_32x32x16_bf16 v[128:143], v[20:23], a[176:179], v[128:143]\n"
    "s_waitcnt vmcnt(25)\n"
    "s_barrier\n"
    "v_add_u32 v1, 0x10000, v1\n"
    "ds_read_b64_tr_b16 v[16:17], v1\n"
    "s_mul_i32 s74, s77, 0x80000\n"
    "s_add_u32 s74, s74, 0x40000\n"
    "s_add_u32 m0, s81, 0x8000\n"
    "s_nop 0\n"
    "buffer_load_dwordx4 v6, s[52:55], s74 offen lds\n"
    "s_waitcnt lgkmcnt(3)\n"
    "v_mfma_f32_32x32x16_bf16 v[96:111], v[24:27], a[184:187], v[96:111]\n"
    "ds_read_b64_tr_b16 v[18:19], v1 offset:512\n"
    "buffer_load_dwordx4 v7, s[52:55], s74 offen offset:1024 lds\n"
    "v_exp_f32 v229, v229\n"
    "v_add_f32 v250, v216, v217\n"
    "v_mfma_f32_32x32x16_bf16 v[112:127], v[24:27], a[188:191], v[112:127]\n"
    "ds_read_b64_tr_b16 v[20:21], v1 offset:2048\n"
    "s_mul_i32 s74, s77, 0x80000\n"
    "s_add_u32 s74, s74, 0x60000\n"
    "s_add_u32 m0, s81, 0xa000\n"
    "s_nop 0\n"
    "buffer_load_dwordx4 v6, s[52:55], s74 offen lds\n"
    "s_waitcnt lgkmcnt(3)\n"
    "v_mfma_f32_32x32x16_bf16 v[144:159], v[28:31], a[188:191], v[144:159]\n"
    "ds_read_b64_tr_b16 v[22:23], v1 offset:2560\n"
    "buffer_load_dwordx4 v7, s[52:55], s74 offen offset:1024 lds\n"
    "v_exp_f32 v230, v230\n"
    "v_add_f32 v250, v250, v218\n"
    "v_mfma_f32_32x32x16_bf16 v[128:143], v[28:31], a[184:187], v[128:143]\n"
    "ds_read_b64_tr_b16 v[24:25], v1 offset:4096\n"
    "v_exp_f32 v231, v231\n"
    "v_add_f32 v250, v250, v219\n"
    "v_add_f32 v250, v250, v220\n"
    "s_waitcnt lgkmcnt(3)\n"
    "v_mfma_f32_32x32x16_bf16 v[96:111], v[16:19], a[192:195], v[96:111]\n"
    "ds_read_b64_tr_b16 v[26:27], v1 offset:4608\n"
    "v_add_f32 v250, v250, v221\n"
    "v_add_f32 v250, v250, v222\n"
    "v_add_f32 v250, v250, v223\n"
    "v_add_f32 v5, v224, v225\n"
    "v_mfma_f32_32x32x16_bf16 v[112:127], v[16:19], a[196:199], v[112:127]\n"
    "ds_read_b64_tr_b16 v[28:29], v1 offset:6144\n"
    "v_add_f32 v5, v5, v226\n"
    "v_add_f32 v5, v5, v227\n"
    "v_add_f32 v5, v5, v228\n"
    "v_add_f32 v5, v5, v229\n"
    "s_waitcnt lgkmcnt(3)\n"
    "v_mfma_f32_32x32x16_bf16 v[144:159], v[20:23], a[196:199], v[144:159]\n"
    "ds_read_b64_tr_b16 v[30:31], v1 offset:6656\n"
    "v_add_f32 v5, v5, v230\n"
    "v_add_f32 v5, v5, v231\n"
    "v_cndmask_b32 v56, v4, v232, s[92:93]\n"
    "v_cndmask_b32 v57, v4, v233, s[92:93]\n"
    "v_mfma_f32_32x32x16_bf16 v[200:215], v[192:195], v[48:51], 0\n"
    "v_permlane32_swap_b32 v250, v5\n"
    "v_add_f32 v250, v250, v5\n"
    "v_rcp_f32 v250, v250\n"
    "v_cndmask_b32 v58, v4, v234, s[92:93]\n"
    "v_mfma_f32_32x32x16_bf16 v[128:143], v[20:23], a[192:195], v[128:143]\n"
    "ds_read_b64_tr_b16 v[16:17], v1 offset:8192\n"
    "v_mov_b32 v5, v250\n"
    "v_cndmask_b32 v59, v4, v235, s[92:93]\n"
    "v_cndmask_b32 v60, v4, v236, s[92:93]\n"
    "v_permlane32_swap_b32 v250, v5\n"
    "s_waitcnt lgkmcnt(3)\n"
    "v_mfma_f32_32x32x16_bf16 v[96:111], v[24:27], a[200:203], v[96:111]\n"
    "ds_read_b64_tr_b16 v[18:19], v1 offset:8704\n"
    "v_mul_f32 v216, v216, v250\n"
    "v_mul_f32 v217, v217, v250\n"
    "v_mul_f32 v218, v218, v250\n"
    "v_mul_f32 v219, v219, v250\n"
    "v_mfma_f32_32x32x16_bf16 v[112:127], v[24:27], a[204:207], v[112:127]\n"
    "ds_read_b64_tr_b16 v[20:21], v1 offset:10240\n"
    "v_mul_f32 v220, v220, v250\n"
    "v_mul_f32 v221, v221, v250\n"
    "v_mul_f32 v222, v222, v250\n"
    "v_mul_f32 v223, v223, v250\n"
    "s_waitcnt lgkmcnt(3)\n"
    "v_mfma_f32_32x32x16_bf16 v[144:159], v[28:31], a[204:207], v[144:159]\n"
    "ds_read_b64_tr_b16 v[22:23], v1 offset:10752\n"
    "v_mul_f32 v224, v224, v5\n"
    "v_mul_f32 v225, v225, v5\n"
    "v_mul_f32 v226, v226, v5\n"
    "v_mul_f32 v227, v227, v5\n"
    "v_mfma_f32_32x32x16_bf16 v[128:143], v[28:31], a[200:203], v[128:143]\n"
    "ds_read_b64_tr_b16 v[24:25], v1 offset:12288\n"
    "v_mul_f32 v228, v228, v5\n"
    "v_mul_f32 v229, v229, v5\n"
    "v_mul_f32 v230, v230, v5\n"
    "v_mul_f32 v231, v231, v5\n"
    "s_waitcnt lgkmcnt(3)\n"
    "v_mfma_f32_32x32x16_bf16 v[96:111], v[16:19], a[208:211], v[96:111]\n"
    "ds_read_b64_tr_b16 v[26:27], v1 offset:12800\n"
    "v_cvt_pk_bf16_f32 v216, v216, v217\n"
    "v_cvt_pk_bf16_f32 v217, v218, v219\n"
    "v_cvt_pk_bf16_f32 v218, v220, v221\n"
    "v_cvt_pk_bf16_f32 v219, v222, v223\n"
    "v_mfma_f32_32x32x16_bf16 v[112:127], v[16:19], a[212:215], v[112:127]\n"
    "ds_read_b64_tr_b16 v[28:29], v1 offset:14336\n"
    "v_cvt_pk_bf16_f32 v220, v224, v225\n"
    "v_cvt_pk_bf16_f32 v221, v226, v227\n"
    "v_cvt_pk_bf16_f32 v222, v228, v229\n"
    "v_cvt_pk_bf16_f32 v223, v230, v231\n"
    "s_waitcnt lgkmcnt(3)\n"
    "v_mfma_f32_32x32x16_bf16 v[144:159], v[20:23], a[212:215], v[144:159]\n"
    "ds_read_b64_tr_b16 v[30:31], v1 offset:14848\n"
    "ds_write_b64 v2, v[216:217]\n"
    "ds_write_b64 v2, v[218:219] offset:16\n"
    "ds_write_b64 v2, v[220:221] offset:32\n"
    "ds_write_b64 v2, v[222:223] offset:48\n"
    "v_mfma_f32_32x32x16_bf16 v[128:143], v[20:23], a[208:211], v[128:143]\n"
    "s_waitcnt vmcnt(24)\n"
    "s_barrier\n"
    "ds_read_b64_tr_b16 v[16:17], v1 offset:16384\n"
    "s_mul_i32 s74, s77, 0x40000\n"
    "s_add_u32 s74, s74, 0x100000\n"
    "s_add_u32 m0, s81, 0xc000\n"
    "s_nop 0\n"
    "buffer_load_dwordx4 v6, s[52:55], s74 offen lds\n"
    "s_waitcnt lgkmcnt(7)\n"
    "v_mfma_f32_32x32x16_bf16 v[96:111], v[24:27], a[216:219], v[96:111]\n"
    "ds_read_b64_tr_b16 v[18:19], v1 offset:16896\n"
    "buffer_load_dwordx4 v7, s[52:55], s74 offen offset:1024 lds\n"
    "ds_read_b64_tr_b16 v[224:225], v3\n"
    "ds_read_b64_tr_b16 v[226:227], v3 offset:288\n"
    "ds_read_b64_tr_b16 v[228:229], v3 offset:1152\n"
    "v_mfma_f32_32x32x16_bf16 v[112:127], v[24:27], a[220:223], v[112:127]\n"
    "ds_read_b64_tr_b16 v[20:21], v1 offset:18432\n"
    "s_mul_i32 s74, s77, 0x40000\n"
    "s_add_u32 s74, s74, 0x110000\n"
    "s_add_u32 m0, s81, 0xe000\n"
    "s_nop 0\n"
    "buffer_load_dwordx4 v6, s[52:55], s74 offen lds\n"
    "s_waitcnt lgkmcnt(10)\n"
    "v_mfma_f32_32x32x16_bf16 v[144:159], v[28:31], a[220:223], v[144:159]\n"
    "ds_read_b64_tr_b16 v[22:23], v1 offset:18944\n"
    "buffer_load_dwordx4 v7, s[52:55], s74 offen offset:1024 lds\n"
    "ds_read_b64_tr_b16 v[230:231], v3 offset:1440\n"
    "v_cndmask_b32 v61, v4, v237, s[92:93]\n"
    "v_cndmask_b32 v62, v4, v238, s[92:93]\n"
    "v_mfma_f32_32x32x16_bf16 v[128:143], v[28:31], a[216:219], v[128:143]\n"
    "ds_read_b64_tr_b16 v[24:25], v1 offset:20480\n"
    "v_cndmask_b32 v63, v4, v239, s[92:93]\n"
    "v_max3_f32 v248, v200, v201, v202\n"
    "v_max3_f32 v248, v248, v203, v204\n"
    "v_max3_f32 v248, v248, v205, v206\n"
    "s_waitcnt lgkmcnt(7)\n"
    "v_mfma_f32_32x32x16_bf16 v[96:111], v[16:19], a[224:227], v[96:111]\n"
    "ds_read_b64_tr_b16 v[26:27], v1 offset:20992\n"
    "v_max_f32 v248, v248, v207\n"
    "v_max3_f32 v249, v208, v209, v210\n"
    "v_max3_f32 v249, v249, v211, v212\n"
    "s_waitcnt lgkmcnt(6)\n"
    "v_mfma_f32_32x32x16_bf16 v[80:95], v[224:227], v[56:59], 0\n"
    "v_max3_f32 v249, v249, v213, v214\n"
    "v_max_f32 v249, v249, v215\n"
    "s_add_u32 s34, s34, s35\n"
    "s_add_u32 s34, s34, s75\n"
    "v_permlane32_swap_b32 v248, v249\n"
    "v_mfma_f32_32x32x16_bf16 v[112:127], v[16:19], a[228:231], v[112:127]\n"
    "ds_read_b64_tr_b16 v[28:29], v1 offset:22528\n"
    "v_max_f32 v248, v248, v249\n"
    "v_mul_f32 v248, -v248, v165\n"
    "v_mov_b32 v249, v248\n"
    "s_waitcnt lgkmcnt(3)\n"
    "v_mfma_f32_32x32x16_bf16 v[80:95], v[228:231], v[60:63], v[80:95]\n"
    "s_add_u32 s34, s34, 4\n"
    "v_permlane32_swap_b32 v248, v249\n"
    "v_fma_f32 v200, v200, v165, v248\n"
    "v_fma_f32 v201, v201, v165, v248\n"
    "v_fma_f32 v202, v202, v165, v248\n"
    "v_mfma_f32_32x32x16_bf16 v[144:159], v[20:23], a[228:231], v[144:159]\n"
    "ds_read_b64_tr_b16 v[30:31], v1 offset:23040\n"
    "v_fma_f32 v203, v203, v165, v248\n"
    "v_fma_f32 v204, v204, v165, v248\n"
    "v_fma_f32 v205, v205, v165, v248\n"
    "v_fma_f32 v206, v206, v165, v248\n"
    "v_mfma_f32_32x32x16_bf16 v[128:143], v[20:23], a[224:227], v[128:143]\n"
    "ds_read_b64_tr_b16 v[16:17], v1 offset:24576\n"
    "v_fma_f32 v207, v207, v165, v248\n"
    "v_fma_f32 v208, v208, v165, v249\n"
    "v_fma_f32 v209, v209, v165, v249\n"
    "v_fma_f32 v210, v210, v165, v249\n"
    "s_waitcnt lgkmcnt(3)\n"
    "v_mfma_f32_32x32x16_bf16 v[96:111], v[24:27], a[232:235], v[96:111]\n"
    "ds_read_b64_tr_b16 v[18:19], v1 offset:25088\n"
    "v_fma_f32 v211, v211, v165, v249\n"
    "v_fma_f32 v212, v212, v165, v249\n"
    "v_fma_f32 v213, v213, v165, v249\n"
    "v_fma_f32 v214, v214, v165, v249\n"
    "v_mfma_f32_32x32x16_bf16 v[112:127], v[24:27], a[236:239], v[112:127]\n"
    "ds_read_b64_tr_b16 v[20:21], v1 offset:26624\n"
    "v_fma_f32 v215, v215, v165, v249\n"
    "v_exp_f32 v200, v200\n"
    "s_lshl_b32 s34, s34, 12\n"
    "s_waitcnt lgkmcnt(3)\n"
    "v_mfma_f32_32x32x16_bf16 v[144:159], v[28:31], a[236:239], v[144:159]\n"
    "ds_read_b64_tr_b16 v[22:23], v1 offset:27136\n"
    "v_exp_f32 v201, v201\n"
    "v_exp_f32 v202, v202\n"
    "v_mfma_f32_32x32x16_bf16 v[128:143], v[28:31], a[232:235], v[128:143]\n"
    "ds_read_b64_tr_b16 v[24:25], v1 offset:28672\n"
    "v_exp_f32 v203, v203\n"
    "v_exp_f32 v204, v204\n"
    "s_waitcnt lgkmcnt(3)\n"
    "v_mfma_f32_32x32x16_bf16 v[96:111], v[16:19], a[240:243], v[96:111]\n"
    "ds_read_b64_tr_b16 v[26:27], v1 offset:29184\n"
    "v_exp_f32 v205, v205\n"
    "v_exp_f32 v206, v206\n"
    "v_mfma_f32_32x32x16_bf16 v[112:127], v[16:19], a[244:247], v[112:127]\n"
    "ds_read_b64_tr_b16 v[28:29], v1 offset:30720\n"
    "v_exp_f32 v207, v207\n"
    "v_exp_f32 v208, v208\n"
    "s_waitcnt lgkmcnt(3)\n"
    "v_mfma_f32_32x32x16_bf16 v[144:159], v[20:23], a[244:247], v[144:159]\n"
    "ds_read_b64_tr_b16 v[30:31], v1 offset:31232\n"
    "v_exp_f32 v209, v209\n"
    "v_exp_f32 v210, v210\n"
    "v_mfma_f32_32x32x16_bf16 v[128:143], v[20:23], a[240:243], v[128:143]\n"
    "s_waitcnt vmcnt(24)\n"
    "s_barrier\n"
    "v_bfe_u32 v251, v0, 4, 1\n"
    "v_xor_b32 v1, s76, v251\n"
    "v_lshlrev_b32 v1, 5, v1\n"
    "v_lshl_add_u32 v1, v251, 10, v1\n"
    "v_and_b32 v251, 3, v0\n"
    "v_lshl_add_u32 v1, v251, 3, v1\n"
    "v_bfe_u32 v251, v0, 2, 2\n"
    "v_lshl_add_u32 v1, v251, 6, v1\n"
    "v_lshrrev_b32 v251, 5, v0\n"
    "v_lshl_add_u32 v1, v251, 9, v1\n"
    "v_add_u32 v1, 0x18000, v1\n"
    "ds_read_b64_tr_b16 v[16:17], v1\n"
    "s_mul_i32 s74, s77, 0x40000\n"
    "s_add_u32 s74, s74, 0x120000\n"
    "s_add_u32 m0, s81, 0x10000\n"
    "s_nop 0\n"
    "buffer_load_dwordx4 v6, s[52:55], s74 offen lds\n"
    "s_waitcnt lgkmcnt(3)\n"
    "v_mfma_f32_32x32x16_bf16 v[96:111], v[24:27], a[248:251], v[96:111]\n"
    "ds_read_b64_tr_b16 v[18:19], v1 offset:256\n"
    "buffer_load_dwordx4 v7, s[52:55], s74 offen offset:1024 lds\n"
    "v_exp_f32 v211, v211\n"
    "v_add_f32 v248, v200, v201\n"
    "v_mfma_f32_32x32x16_bf16 v[112:127], v[24:27], a[252:255], v[112:127]\n"
    "ds_read_b64_tr_b16 v[20:21], v1 offset:2048\n"
    "s_mul_i32 s74, s77, 0x40000\n"
    "s_add_u32 s74, s74, 0x130000\n"
    "s_add_u32 m0, s81, 0x12000\n"
    "s_nop 0\n"
    "buffer_load_dwordx4 v6, s[52:55], s74 offen lds\n"
    "s_waitcnt lgkmcnt(3)\n"
    "v_mfma_f32_32x32x16_bf16 v[144:159], v[28:31], a[252:255], v[144:159]\n"
    "ds_read_b64_tr_b16 v[22:23], v1 offset:2304\n"
    "buffer_load_dwordx4 v7, s[52:55], s74 offen offset:1024 lds\n"
    "v_exp_f32 v212, v212\n"
    "v_add_f32 v248, v248, v202\n"
    "v_mfma_f32_32x32x16_bf16 v[128:143], v[28:31], a[248:251], v[128:143]\n"
    "ds_read_b64_tr_b16 v[24:25], v1 offset:4096\n"
    "v_exp_f32 v213, v213\n"
    "v_exp_f32 v214, v214\n"
    "v_exp_f32 v215, v215\n"
    "v_add_f32 v248, v248, v203\n"
    "v_add_f32 v248, v248, v204\n"
    "v_add_f32 v248, v248, v205\n"
    "v_add_f32 v248, v248, v206\n"
    "v_add_f32 v248, v248, v207\n"
    "v_add_f32 v249, v208, v209\n"
    "v_add_f32 v249, v249, v210\n"
    "v_add_f32 v249, v249, v211\n"
    "v_add_f32 v249, v249, v212\n"
    "v_add_f32 v249, v249, v213\n"
    "v_add_f32 v249, v249, v214\n"
    "v_add_f32 v249, v249, v215\n"
    "v_cndmask_b32 v40, v240, v4, s[92:93]\n"
    "v_cndmask_b32 v41, v241, v4, s[92:93]\n"
    "v_permlane32_swap_b32 v248, v249\n"
    "v_add_f32 v248, v248, v249\n"
    "v_rcp_f32 v248, v248\n"
    "v_cndmask_b32 v42, v242, v4, s[92:93]\n"
    "v_mov_b32 v249, v248\n"
    "v_cndmask_b32 v43, v243, v4, s[92:93]\n"
    "v_cndmask_b32 v44, v244, v4, s[92:93]\n"
    "v_permlane32_swap_b32 v248, v249\n"
    "v_mul_f32 v200, v200, v248\n"
    "v_mul_f32 v201, v201, v248\n"
    "v_mul_f32 v202, v202, v248\n"
    "v_mul_f32 v203, v203, v248\n"
    "v_mul_f32 v204, v204, v248\n"
    "v_mul_f32 v205, v205, v248\n"
    "v_mul_f32 v206, v206, v248\n"
    "v_mul_f32 v207, v207, v248\n"
    "v_mul_f32 v208, v208, v249\n"
    "v_mul_f32 v209, v209, v249\n"
    "v_mul_f32 v210, v210, v249\n"
    "v_mul_f32 v211, v211, v249\n"
    "v_mul_f32 v212, v212, v249\n"
    "v_mul_f32 v213, v213, v249\n"
    "v_mul_f32 v214, v214, v249\n"
    "v_mul_f32 v215, v215, v249\n"
    "v_cvt_pk_bf16_f32 v200, v200, v201\n"
    "v_cvt_pk_bf16_f32 v201, v202, v203\n"
    "v_cvt_pk_bf16_f32 v202, v204, v205\n"
    "v_cvt_pk_bf16_f32 v203, v206, v207\n"
    "v_cvt_pk_bf16_f32 v204, v208, v209\n"
    "v_cvt_pk_bf16_f32 v205, v210, v211\n"
    "v_cvt_pk_bf16_f32 v206, v212, v213\n"
    "v_cvt_pk_bf16_f32 v207, v214, v215\n"
    "ds_write_b64 v2, v[200:201]\n"
    "ds_write_b64 v2, v[202:203] offset:16\n"
    "ds_write_b64 v2, v[204:205] offset:32\n"
    "ds_write_b64 v2, v[206:207] offset:48\n"
    "ds_read_b64_tr_b16 v[208:209], v3\n"
    "ds_read_b64_tr_b16 v[210:211], v3 offset:288\n"
    "ds_read_b64_tr_b16 v[212:213], v3 offset:1152\n"
    "ds_read_b64_tr_b16 v[214:215], v3 offset:1440\n"
    "v_cndmask_b32 v45, v245, v4, s[92:93]\n"
    "v_cndmask_b32 v46, v246, v4, s[92:93]\n"
    "v_cndmask_b32 v47, v247, v4, s[92:93]\n"
    "v_mfma_f32_32x32x16_bf16 v[216:231], v[196:199], v[52:55], 0\n"
    "s_nop 11\n"
    "v_max3_f32 v250, v216, v217, v218\n"
    "v_max3_f32 v250, v250, v219, v220\n"
    "v_max3_f32 v250, v250, v221, v222\n"
    "v_max_f32 v250, v250, v223\n"
    "v_max3_f32 v5, v224, v225, v226\n"
    "v_max3_f32 v5, v5, v227, v228\n"
    "v_max3_f32 v5, v5, v229, v230\n"
    "v_max_f32 v5, v5, v231\n"
    "s_nop 1\n"
    "v_permlane32_swap_b32 v250, v5\n"
    "v_max_f32 v250, v250, v5\n"
    "v_mul_f32 v250, -v250, v165\n"
    "v_mov_b32 v5, v250\n"
    "s_waitcnt lgkmcnt(2)\n"
    "v_mfma_f32_32x32x16_bf16 v[64:79], v[208:211], v[40:43], v[64:79]\n"
    "v_permlane32_swap_b32 v250, v5\n"
    "v_fma_f32 v216, v216, v165, v250\n"
    "v_fma_f32 v217, v217, v165, v250\n"
    "v_fma_f32 v218, v218, v165, v250\n"
    "v_fma_f32 v219, v219, v165, v250\n"
    "v_fma_f32 v220, v220, v165, v250\n"
    "v_fma_f32 v221, v221, v165, v250\n"
    "v_fma_f32 v222, v222, v165, v250\n"
    "v_fma_f32 v223, v223, v165, v250\n"
    "v_fma_f32 v224, v224, v165, v5\n"
    "v_fma_f32 v225, v225, v165, v5\n"
    "v_fma_f32 v226, v226, v165, v5\n"
    "s_waitcnt lgkmcnt(0)\n"
    "v_mfma_f32_32x32x16_bf16 v[64:79], v[212:215], v[44:47], v[64:79]\n"
    "v_fma_f32 v227, v227, v165, v5\n"
    "v_fma_f32 v228, v228, v165, v5\n"
    "v_fma_f32 v229, v229, v165, v5\n"
    "v_fma_f32 v230, v230, v165, v5\n"
    "v_fma_f32 v231, v231, v165, v5\n"
    "v_exp_f32 v216, v216\n"
    "v_exp_f32 v217, v217\n"
    "v_exp_f32 v218, v218\n"
    "v_exp_f32 v219, v219\n"
    "v_exp_f32 v220, v220\n"
    "v_exp_f32 v221, v221\n"
    "v_exp_f32 v222, v222\n"
    "v_exp_f32 v223, v223\n"
    "v_exp_f32 v224, v224\n"
    "v_exp_f32 v225, v225\n"
    "v_exp_f32 v226, v226\n"
    "v_exp_f32 v227, v227\n"
    "v_exp_f32 v228, v228\n"
    "v_exp_f32 v229, v229\n"
    "v_exp_f32 v230, v230\n"
    "v_exp_f32 v231, v231\n"
    "v_add_f32 v250, v216, v217\n"
    "v_add_f32 v250, v250, v218\n"
    "v_add_f32 v250, v250, v219\n"
    "v_add_f32 v250, v250, v220\n"
    "v_add_f32 v250, v250, v221\n"
    "v_add_f32 v250, v250, v222\n"
    "v_add_f32 v250, v250, v223\n"
    "v_add_f32 v5, v224, v225\n"
    "v_add_f32 v5, v5, v226\n"
    "v_add_f32 v5, v5, v227\n"
    "v_add_f32 v5, v5, v228\n"
    "v_add_f32 v5, v5, v229\n"
    "v_add_f32 v5, v5, v230\n"
    "v_add_f32 v5, v5, v231\n"
    "v_cndmask_b32 v56, v4, v240, s[92:93]\n"
    "v_cndmask_b32 v57, v4, v241, s[92:93]\n"
    "v_permlane32_swap_b32 v250, v5\n"
    "v_add_f32 v250, v250, v5\n"
    "v_rcp_f32 v250, v250\n"
    "v_cndmask_b32 v58, v4, v242, s[92:93]\n"
    "v_mov_b32 v5, v250\n"
    "v_cndmask_b32 v59, v4, v243, s[92:93]\n"
    "v_cndmask_b32 v60, v4, v244, s[92:93]\n"
    "v_permlane32_swap_b32 v250, v5\n"
    "v_mul_f32 v216, v216, v250\n"
    "v_mul_f32 v217, v217, v250\n"
    "v_mul_f32 v218, v218, v250\n"
    "v_mul_f32 v219, v219, v250\n"
    "v_mul_f32 v220, v220, v250\n"
    "v_mul_f32 v221, v221, v250\n"
    "v_mul_f32 v222, v222, v250\n"
    "v_mul_f32 v223, v223, v250\n"
    "v_mul_f32 v224, v224, v5\n"
    "v_mul_f32 v225, v225, v5\n"
    "v_mul_f32 v226, v226, v5\n"
    "v_mul_f32 v227, v227, v5\n"
    "v_mul_f32 v228, v228, v5\n"
    "v_mul_f32 v229, v229, v5\n"
    "v_mul_f32 v230, v230, v5\n"
    "v_mul_f32 v231, v231, v5\n"
    "v_cvt_pk_bf16_f32 v216, v216, v217\n"
    "v_cvt_pk_bf16_f32 v217, v218, v219\n"
    "v_cvt_pk_bf16_f32 v218, v220, v221\n"
    "v_cvt_pk_bf16_f32 v219, v222, v223\n"
    "v_cvt_pk_bf16_f32 v220, v224, v225\n"
    "v_cvt_pk_bf16_f32 v221, v226, v227\n"
    "v_cvt_pk_bf16_f32 v222, v228, v229\n"
    "v_cvt_pk_bf16_f32 v223, v230, v231\n"
    "ds_write_b64 v2, v[216:217]\n"
    "ds_write_b64 v2, v[218:219] offset:16\n"
    "ds_write_b64 v2, v[220:221] offset:32\n"
    "ds_write_b64 v2, v[222:223] offset:48\n"
    "ds_read_b64_tr_b16 v[224:225], v3\n"
    "ds_read_b64_tr_b16 v[226:227], v3 offset:288\n"
    "ds_read_b64_tr_b16 v[228:229], v3 offset:1152\n"
    "ds_read_b64_tr_b16 v[230:231], v3 offset:1440\n"
    "v_cndmask_b32 v61, v4, v245, s[92:93]\n"
    "v_cndmask_b32 v62, v4, v246, s[92:93]\n"
    "v_cndmask_b32 v63, v4, v247, s[92:93]\n"
    "v_and_b32 v192, 15, v0\n"
    "v_lshrrev_b32 v193, 5, v0\n"
    "v_lshl_add_u32 v192, v193, 4, v192\n"
    "v_add_u32 v194, 32, v193\n"
    "v_add_u32 v195, 34, v193\n"
    "v_and_b32 v196, 31, v0\n"
    "v_cmp_eq_u32 vcc, 0, v196\n"
    "s_mul_i32 s11, s69, 0xc00\n"
    "s_add_u32 s11, s11, 0x24000\n"
    "v_cndmask_b32 v195, v195, v194, vcc\n"
    "v_cndmask_b32 v195, v195, v192, s[92:93]\n"
    "v_lshl_add_u32 v2, v195, 4, s11\n"
    "v_cndmask_b32 v194, v192, v194, s[92:93]\n"
    "s_xor_b32 s10, s69, 2\n"
    "s_mul_i32 s10, s10, 0xc00\n"
    "s_add_u32 s10, s10, 0x24000\n"
    "v_lshl_add_u32 v3, v194, 4, s10\n"
    "v_and_b32 v192, 15, v0\n"
    "v_lshlrev_b32 v192, 5, v192\n"
    "v_lshl_add_u32 v192, v193, 4, v192\n"
    "v_mov_b32 v194, 0x7ff00000\n"
    "v_cndmask_b32 v4, v192, v194, s[92:93]\n"
    "v_lshlrev_b32 v195, 4, v193\n"
    "v_add_u32 v195, 0x400, v195\n"
    "v_cmp_eq_u32 vcc, 16, v196\n"
    "s_lshl_b32 s10, s71, 2\n"
    "s_waitcnt lgkmcnt(2)\n"
    "v_mfma_f32_32x32x16_bf16 v[80:95], v[224:227], v[56:59], v[80:95]\n"
    "v_cndmask_b32 v4, v4, v195, vcc\n"
    "v_add_u32 v5, 0x200, v4\n"
    "v_add_u32 v195, 32, v195\n"
    "v_cndmask_b32 v5, v5, v195, vcc\n"
    "s_lshl_b32 s11, s76, 1\n"
    "s_add_u32 s10, s10, s11\n"
    "s_add_u32 s10, s10, s75\n"
    "s_lshl_b32 s10, s10, 4\n"
    "s_add_u32 s87, s10, s70\n"
    "s_mul_i32 s10, s87, 0x480\n"
    "s_add_u32 s10, s10, 0x900000\n"
    "s_waitcnt lgkmcnt(0)\n"
    "v_mfma_f32_32x32x16_bf16 v[80:95], v[228:231], v[60:63], v[80:95]\n"
    "s_nop 11\n"
    "ds_write_b128 v2, v[80:83]\n"
    "ds_write_b128 v2, v[84:87] offset:768\n"
    "ds_write_b128 v2, v[88:91] offset:1536\n"
    "ds_write_b128 v2, v[92:95] offset:2304\n"
    "s_waitcnt lgkmcnt(0)\n"
    "s_barrier\n"
    "ds_read_b128 v[200:203], v3\n"
    "ds_read_b128 v[204:207], v3 offset:768\n"
    "ds_read_b128 v[208:211], v3 offset:1536\n"
    "ds_read_b128 v[212:215], v3 offset:2304\n"
    "s_lshl_b32 s87, s87, 2\n"
    "s_add_u32 s87, s87, 0xa58000\n"
    "v_mov_b32 v249, 0\n"
    "v_mov_b32 v250, s72\n"
    "s_waitcnt lgkmcnt(3)\n"
    "v_add_f32 v64, v64, v200\n"
    "v_add_f32 v65, v65, v201\n"
    "v_add_f32 v66, v66, v202\n"
    "v_add_f32 v67, v67, v203\n"
    "s_waitcnt lgkmcnt(2)\n"
    "v_add_f32 v68, v68, v204\n"
    "v_add_f32 v69, v69, v205\n"
    "v_add_f32 v70, v70, v206\n"
    "v_add_f32 v71, v71, v207\n"
    "s_waitcnt lgkmcnt(1)\n"
    "v_add_f32 v72, v72, v208\n"
    "v_add_f32 v73, v73, v209\n"
    "v_add_f32 v74, v74, v210\n"
    "v_add_f32 v75, v75, v211\n"
    "s_waitcnt lgkmcnt(0)\n"
    "v_add_f32 v76, v76, v212\n"
    "v_add_f32 v77, v77, v213\n"
    "v_add_f32 v78, v78, v214\n"
    "v_add_f32 v79, v79, v215\n"
    "v_cvt_pk_bf16_f32 v64, v64, v65\n"
    "v_cvt_pk_bf16_f32 v65, v66, v67\n"
    "v_cvt_pk_bf16_f32 v66, v68, v69\n"
    "v_cvt_pk_bf16_f32 v67, v70, v71\n"
    "v_cvt_pk_bf16_f32 v68, v72, v73\n"
    "v_cvt_pk_bf16_f32 v69, v74, v75\n"
    "v_cvt_pk_bf16_f32 v70, v76, v77\n"
    "v_cvt_pk_bf16_f32 v71, v78, v79\n"
    "v_permlane32_swap_b32 v64, v66\n"
    "v_permlane32_swap_b32 v65, v67\n"
    "buffer_store_dwordx4 v[64:67], v4, s[48:51], s10 offen sc1\n"
    "v_permlane32_swap_b32 v68, v70\n"
    "v_permlane32_swap_b32 v69, v71\n"
    "buffer_store_dwordx4 v[68:71], v5, s[48:51], s10 offen sc1\n"
    "s_waitcnt vmcnt(0)\n"
    "buffer_store_dword v250, v249, s[48:51], s87 offen sc1\n"
    "s_add_u32 s34, s34, 0x990000\n"
    "v_and_b32 v184, 31, v0\n"
    "v_lshlrev_b32 v184, 3, v184\n"
    "v_lshrrev_b32 v185, 5, v0\n"
    "v_lshl_add_u32 v15, v185, 11, v184\n"
    "buffer_load_dwordx2 v[168:169], v15, s[48:51], s34 offen sc1\n"
    "buffer_load_dwordx2 v[170:171], v15, s[48:51], s34 offen offset:256 sc1\n"
    "buffer_load_dwordx2 v[172:173], v15, s[48:51], s34 offen offset:512 sc1\n"
    "buffer_load_dwordx2 v[174:175], v15, s[48:51], s34 offen offset:768 sc1\n"
    "buffer_load_dwordx2 v[176:177], v15, s[48:51], s34 offen offset:1024 sc1\n"
    "buffer_load_dwordx2 v[178:179], v15, s[48:51], s34 offen offset:1280 sc1\n"
    "buffer_load_dwordx2 v[180:181], v15, s[48:51], s34 offen offset:1536 sc1\n"
    "buffer_load_dwordx2 v[182:183], v15, s[48:51], s34 offen offset:1792 sc1\n"
    "s_waitcnt vmcnt(7)\n"
    "v_xor_b32 v185, s72, v169\n"
    "s_waitcnt vmcnt(6)\n"
    "v_bitop3_b32 v185, v185, v171, s72 bitop3:0xf6\n"
    "s_waitcnt vmcnt(5)\n"
    "v_bitop3_b32 v185, v185, v173, s72 bitop3:0xf6\n"
    "s_waitcnt vmcnt(4)\n"
    "v_bitop3_b32 v185, v185, v175, s72 bitop3:0xf6\n"
    "s_waitcnt vmcnt(3)\n"
    "v_bitop3_b32 v185, v185, v177, s72 bitop3:0xf6\n"
    "s_waitcnt vmcnt(2)\n"
    "v_bitop3_b32 v185, v185, v179, s72 bitop3:0xf6\n"
    "s_waitcnt vmcnt(1)\n"
    "v_bitop3_b32 v185, v185, v181, s72 bitop3:0xf6\n"
    "s_waitcnt vmcnt(0)\n"
    "v_bitop3_b32 v185, v185, v183, s72 bitop3:0xf6\n"
    "v_cmp_ne_u32 s[88:89], 0, v185\n"
    "s_cmp_lg_u64 s[88:89], 0\n"
    "s_cbranch_scc1 LB_slow_5%=\n"
    "LB_back_6%=:\n"
    "v_add_f32 v168, v168, v170\n"
    "v_add_f32 v172, v172, v174\n"
    "v_add_f32 v176, v176, v178\n"
    "v_add_f32 v180, v180, v182\n"
    "v_add_f32 v168, v168, v172\n"
    "v_add_f32 v176, v176, v180\n"
    "v_add_f32 v168, v168, v176\n"
    "v_cvt_pk_bf16_f32 v185, v168, v168\n"
    "v_and_b32 v185, 0xffff0000, v185\n"
    "v_sub_f32 v185, v168, v185\n"
    "v_cvt_pk_bf16_f32 v185, v168, v185\n"
    "s_lshl_b32 s35, s76, 9\n"
    "s_add_u32 s35, s35, 0x27400\n"
    "v_lshl_add_u32 v184, v0, 2, s35\n"
    "s_lshl_b32 s35, s75, 8\n"
    "v_add_u32 v15, s35, v184\n"
    "ds_write_b32 v15, v185\n"
    "s_waitcnt lgkmcnt(0)\n"
    "s_barrier\n"
    "ds_read_b32 v188, v184\n"
    "ds_read_b32 v190, v184 offset:256\n"
    "v_mov_b32 v189, 0\n"
    "v_mov_b32 v191, 0\n"
    "v_mfma_f32_32x32x16_bf16 v[32:47], v[16:19], a[0:3], 0\n"
    "ds_read_b64_tr_b16 v[26:27], v1 offset:4352\n"
    "v_bfe_u32 v180, v0, 2, 2\n"
    "v_and_b32 v181, 3, v0\n"
    "v_bfe_u32 v182, v0, 4, 1\n"
    "v_lshrrev_b32 v183, 5, v0\n"
    "v_mfma_f32_32x32x16_bf16 v[48:63], v[16:19], a[4:7], 0\n"
    "ds_read_b64_tr_b16 v[28:29], v1 offset:6144\n"
    "s_lshr_b32 s34, s70, 3\n"
    "s_mul_i32 s34, s34, 0x1000\n"
    "s_add_u32 s34, s34, 0x18000\n"
    "v_lshlrev_b32 v183, 11, v183\n"
    "v_mfma_f32_32x32x16_bf16 v[32:47], v[20:23], a[8:11], v[32:47]\n"
    "ds_read_b64_tr_b16 v[30:31], v1 offset:6400\n"
    "v_lshl_add_u32 v183, v181, 3, v183\n"
    "v_lshl_add_u32 v182, v182, 3, v180\n"
    "v_lshl_add_u32 v183, v182, 6, v183\n"
    "s_lshl_b32 s35, s76, 5\n"
    "v_mfma_f32_32x32x16_bf16 v[48:63], v[20:23], a[12:15], v[48:63]\n"
    "ds_read_b64_tr_b16 v[16:17], v1 offset:8192\n"
    "v_add_u32 v183, s34, v183\n"
    "v_add_u32 v15, s35, v183\n"
    "s_xor_b32 s35, s35, 32\n"
    "v_add_u32 v183, s35, v183\n"
    "s_waitcnt lgkmcnt(5)\n"
    "v_mfma_f32_32x32x8_bf16 v[96:111], v[188:189], v[160:161], v[96:111]\n"
    "ds_read_b64_tr_b16 v[168:169], v15\n"
    "ds_read_b64_tr_b16 v[170:171], v15 offset:256\n"
    "ds_read_b64_tr_b16 v[172:173], v183 offset:1024\n"
    "ds_read_b64_tr_b16 v[174:175], v183 offset:1280\n"
    "v_mov_b32 v176, 0x3f803f80\n"
    "v_mfma_f32_32x32x8_bf16 v[112:127], v[188:189], v[162:163], v[112:127]\n"
    "v_mov_b32 v177, 0x3f803f80\n"
    "v_mov_b32 v178, 0x3f803f80\n"
    "v_mov_b32 v179, 0x3f803f80\n"
    "s_mul_i32 s34, s82, 12\n"
    "s_lshl_b32 s35, s76, 1\n"
    "s_waitcnt lgkmcnt(8)\n"
    "v_mfma_f32_32x32x8_bf16 v[128:143], v[190:191], v[160:161], v[128:143]\n"
    "s_add_u32 s34, s34, s35\n"
    "s_add_u32 s34, s34, 8\n"
    "s_lshl_b32 s34, s34, 12\n"
    "s_add_u32 s34, s34, 0x990000\n"
    "s_and_b32 s35, s70, 7\n"
    "v_mfma_f32_32x32x8_bf16 v[144:159], v[190:191], v[162:163], v[144:159]\n"
    "s_lshl_b32 s35, s35, 9\n"
    "s_add_u32 s34, s34, s35\n"
    "s_lshr_b32 s35, s70, 3\n"
    "s_lshl_b32 s35, s35, 8\n"
    "s_add_u32 s34, s34, s35\n"
    "s_waitcnt lgkmcnt(7)\n"
    "v_mfma_f32_32x32x16_bf16 v[32:47], v[24:27], a[16:19], v[32:47]\n"
    "ds_read_b64_tr_b16 v[18:19], v1 offset:8448\n"
    "v_and_b32 v15, 31, v0\n"
    "v_lshlrev_b32 v15, 3, v15\n"
    "s_mul_i32 s10, s71, 0x800\n"
    "s_waitcnt lgkmcnt(3)\n"
    "v_mfma_f32_16x16x32_bf16 v[180:183], v[176:179], v[168:171], 0\n"
    "s_waitcnt lgkmcnt(1)\n"
    "v_mfma_f32_16x16x32_bf16 v[184:187], v[176:179], v[172:175], 0\n"
    "s_lshl_b32 s11, s76, 10\n"
    "v_mfma_f32_32x32x16_bf16 v[48:63], v[24:27], a[20:23], v[48:63]\n"
    "ds_read_b64_tr_b16 v[20:21], v1 offset:10240\n"
    "s_add_u32 s10, s10, s11\n"
    "s_add_u32 s10, s10, s83\n"
    "s_add_u32 s10, s10, 0x8000\n"
    "s_add_u32 s11, s10, 0x24000\n"
    "v_mfma_f32_32x32x16_bf16 v[32:47], v[28:31], a[24:27], v[32:47]\n"
    "ds_read_b64_tr_b16 v[22:23], v1 offset:10496\n"
    "v_cndmask_b32 v180, v180, v184, s[92:93]\n"
    "v_mov_b32 v181, s72\n"
    "buffer_store_dwordx2 v[180:181], v15, s[48:51], s34 offen sc1\n"
    "v_mul_f32 v96, v96, v128\n"
    "v_mfma_f32_32x32x16_bf16 v[48:63], v[28:31], a[28:31], v[48:63]\n"
    "ds_read_b64_tr_b16 v[24:25], v1 offset:12288\n"
    "v_mul_f32 v97, v97, v129\n"
    "v_mul_f32 v98, v98, v130\n"
    "v_mul_f32 v99, v99, v131\n"
    "v_mul_f32 v100, v100, v132\n"
    "s_waitcnt lgkmcnt(3)\n"
    "v_mfma_f32_32x32x16_bf16 v[32:47], v[16:19], a[32:35], v[32:47]\n"
    "ds_read_b64_tr_b16 v[26:27], v1 offset:12544\n"
    "v_mul_f32 v101, v101, v133\n"
    "v_mul_f32 v102, v102, v134\n"
    "v_mul_f32 v103, v103, v135\n"
    "v_mul_f32 v104, v104, v136\n"
    "v_mfma_f32_32x32x16_bf16 v[48:63], v[16:19], a[36:39], v[48:63]\n"
    "ds_read_b64_tr_b16 v[28:29], v1 offset:14336\n"
    "v_mul_f32 v105, v105, v137\n"
    "v_mul_f32 v106, v106, v138\n"
    "v_mul_f32 v107, v107, v139\n"
    "v_mul_f32 v108, v108, v140\n"
    "s_waitcnt lgkmcnt(3)\n"
    "v_mfma_f32_32x32x16_bf16 v[32:47], v[20:23], a[40:43], v[32:47]\n"
    "ds_read_b64_tr_b16 v[30:31], v1 offset:14592\n"
    "v_mul_f32 v109, v109, v141\n"
    "v_mul_f32 v110, v110, v142\n"
    "v_mul_f32 v111, v111, v143\n"
    "v_cvt_pk_bf16_f32 v128, v96, v97\n"
    "v_mfma_f32_32x32x16_bf16 v[48:63], v[20:23], a[44:47], v[48:63]\n"
    "s_barrier\n"
    "ds_read_b64_tr_b16 v[16:17], v1 offset:16384\n"
    "s_mul_i32 s74, s77, 0x80000\n"
    "s_add_u32 s74, s74, 0x10000\n"
    "s_add_u32 m0, s81, 0x14000\n"
    "s_nop 0\n"
    "buffer_load_dwordx4 v6, s[52:55], s74 offen lds\n"
    "s_waitcnt lgkmcnt(3)\n"
    "v_mfma_f32_32x32x16_bf16 v[32:47], v[24:27], a[48:51], v[32:47]\n"
    "ds_read_b64_tr_b16 v[18:19], v1 offset:16640\n"
    "buffer_load_dwordx4 v7, s[52:55], s74 offen offset:1024 lds\n"
    "v_cvt_pk_bf16_f32 v129, v98, v99\n"
    "v_cvt_pk_bf16_f32 v130, v100, v101\n"
    "v_cvt_pk_bf16_f32 v131, v102, v103\n"
    "v_mfma_f32_32x32x16_bf16 v[48:63], v[24:27], a[52:55], v[48:63]\n"
    "ds_read_b64_tr_b16 v[20:21], v1 offset:18432\n"
    "s_mul_i32 s74, s77, 0x80000\n"
    "s_add_u32 s74, s74, 0x30000\n"
    "s_add_u32 m0, s81, 0x16000\n"
    "s_nop 0\n"
    "buffer_load_dwordx4 v6, s[52:55], s74 offen lds\n"
    "s_waitcnt lgkmcnt(3)\n"
    "v_mfma_f32_32x32x16_bf16 v[32:47], v[28:31], a[56:59], v[32:47]\n"
    "ds_read_b64_tr_b16 v[22:23], v1 offset:18688\n"
    "buffer_load_dwordx4 v7, s[52:55], s74 offen offset:1024 lds\n"
    "buffer_store_dwordx4 v[128:131], v14, s[48:51], s10 offen sc1\n"
    "v_cvt_pk_bf16_f32 v132, v104, v105\n"
    "v_cvt_pk_bf16_f32 v133, v106, v107\n"
    "v_mfma_f32_32x32x16_bf16 v[48:63], v[28:31], a[60:63], v[48:63]\n"
    "ds_read_b64_tr_b16 v[24:25], v1 offset:20480\n"
    "s_mul_i32 s74, s77, 0x80000\n"
    "s_add_u32 s74, s74, 0x50000\n"
    "s_add_u32 m0, s81, 0x18000\n"
    "s_nop 0\n"
    "buffer_load_dwordx4 v6, s[52:55], s74 offen lds\n"
    "s_waitcnt lgkmcnt(3)\n"
    "v_mfma_f32_32x32x16_bf16 v[32:47], v[16:19], a[64:67], v[32:47]\n"
    "ds_read_b64_tr_b16 v[26:27], v1 offset:20736\n"
    "buffer_load_dwordx4 v7, s[52:55], s74 offen offset:1024 lds\n"
    "v_cvt_pk_bf16_f32 v134, v108, v109\n"
    "v_cvt_pk_bf16_f32 v135, v110, v111\n"
    "buffer_store_dwordx4 v[132:135], v14, s[48:51], s10 offen offset:512 sc1\n"
    "v_mfma_f32_32x32x16_bf16 v[48:63], v[16:19], a[68:71], v[48:63]\n"
    "ds_read_b64_tr_b16 v[28:29], v1 offset:22528\n"
    "v_mul_f32 v112, v112, v144\n"
    "v_mul_f32 v113, v113, v145\n"
    "v_mul_f32 v114, v114, v146\n"
    "v_mul_f32 v115, v115, v147\n"
    "s_waitcnt lgkmcnt(3)\n"
    "v_mfma_f32_32x32x16_bf16 v[32:47], v[20:23], a[72:75], v[32:47]\n"
    "ds_read_b64_tr_b16 v[30:31], v1 offset:22784\n"
    "v_mul_f32 v116, v116, v148\n"
    "v_mul_f32 v117, v117, v149\n"
    "v_mul_f32 v118, v118, v150\n"
    "v_mul_f32 v119, v119, v151\n"
    "v_mfma_f32_32x32x16_bf16 v[48:63], v[20:23], a[76:79], v[48:63]\n"
    "ds_read_b64_tr_b16 v[16:17], v1 offset:24576\n"
    "v_mul_f32 v120, v120, v152\n"
    "v_mul_f32 v121, v121, v153\n"
    "v_mul_f32 v122, v122, v154\n"
    "v_mul_f32 v123, v123, v155\n"
    "s_waitcnt lgkmcnt(3)\n"
    "v_mfma_f32_32x32x16_bf16 v[32:47], v[24:27], a[80:83], v[32:47]\n"
    "ds_read_b64_tr_b16 v[18:19], v1 offset:24832\n"
    "v_mul_f32 v124, v124, v156\n"
    "v_mul_f32 v125, v125, v157\n"
    "v_mul_f32 v126, v126, v158\n"
    "v_mul_f32 v127, v127, v159\n"
    "v_mfma_f32_32x32x16_bf16 v[48:63], v[24:27], a[84:87], v[48:63]\n"
    "ds_read_b64_tr_b16 v[20:21], v1 offset:26624\n"
    "v_cvt_pk_bf16_f32 v144, v112, v113\n"
    "v_cvt_pk_bf16_f32 v145, v114, v115\n"
    "v_cvt_pk_bf16_f32 v146, v116, v117\n"
    "v_cvt_pk_bf16_f32 v147, v118, v119\n"
    "s_waitcnt lgkmcnt(3)\n"
    "v_mfma_f32_32x32x16_bf16 v[32:47], v[28:31], a[88:91], v[32:47]\n"
    "ds_read_b64_tr_b16 v[22:23], v1 offset:26880\n"
    "buffer_store_dwordx4 v[144:147], v14, s[48:51], s11 offen sc1\n"
    "v_cvt_pk_bf16_f32 v148, v120, v121\n"
    "v_cvt_pk_bf16_f32 v149, v122, v123\n"
    "v_cvt_pk_bf16_f32 v150, v124, v125\n"
    "v_mfma_f32_32x32x16_bf16 v[48:63], v[28:31], a[92:95], v[48:63]\n"
    "ds_read_b64_tr_b16 v[24:25], v1 offset:28672\n"
    "v_cvt_pk_bf16_f32 v151, v126, v127\n"
    "buffer_store_dwordx4 v[148:151], v14, s[48:51], s11 offen offset:512 sc1\n"
    "v_mov_b32 v249, 0\n"
    "v_mov_b32 v250, s72\n"
    "s_waitcnt lgkmcnt(3)\n"
    "v_mfma_f32_32x32x16_bf16 v[32:47], v[16:19], a[96:99], v[32:47]\n"
    "ds_read_b64_tr_b16 v[26:27], v1 offset:28928\n"
    "v_mfma_f32_32x32x16_bf16 v[48:63], v[16:19], a[100:103], v[48:63]\n"
    "ds_read_b64_tr_b16 v[28:29], v1 offset:30720\n"
    "s_waitcnt lgkmcnt(3)\n"
    "v_mfma_f32_32x32x16_bf16 v[32:47], v[20:23], a[104:107], v[32:47]\n"
    "ds_read_b64_tr_b16 v[30:31], v1 offset:30976\n"
    "v_mfma_f32_32x32x16_bf16 v[48:63], v[20:23], a[108:111], v[48:63]\n"
    "s_barrier\n"
    "ds_read_b64_tr_b16 v[16:17], v1 offset:32768\n"
    "s_mul_i32 s74, s77, 0x80000\n"
    "s_add_u32 s74, s74, 0x70000\n"
    "s_add_u32 m0, s81, 0x1a000\n"
    "s_nop 0\n"
    "buffer_load_dwordx4 v6, s[52:55], s74 offen lds\n"
    "s_waitcnt lgkmcnt(3)\n"
    "v_mfma_f32_32x32x16_bf16 v[32:47], v[24:27], a[112:115], v[32:47]\n"
    "ds_read_b64_tr_b16 v[18:19], v1 offset:33024\n"
    "buffer_load_dwordx4 v7, s[52:55], s74 offen offset:1024 lds\n"
    "v_mfma_f32_32x32x16_bf16 v[48:63], v[24:27], a[116:119], v[48:63]\n"
    "ds_read_b64_tr_b16 v[20:21], v1 offset:34816\n"
    "s_mul_i32 s74, s77, 0x20000\n"
    "s_add_u32 s74, s74, 0x180000\n"
    "s_add_u32 m0, s81, 0x1c000\n"
    "s_nop 0\n"
    "buffer_load_dwordx4 v6, s[52:55], s74 offen lds\n"
    "s_waitcnt lgkmcnt(3)\n"
    "v_mfma_f32_32x32x16_bf16 v[32:47], v[28:31], a[120:123], v[32:47]\n"
    "ds_read_b64_tr_b16 v[22:23], v1 offset:35072\n"
    "buffer_load_dwordx4 v7, s[52:55], s74 offen offset:1024 lds\n"
    "v_mfma_f32_32x32x16_bf16 v[48:63], v[28:31], a[124:127], v[48:63]\n"
    "ds_read_b64_tr_b16 v[24:25], v1 offset:36864\n"
    "s_waitcnt lgkmcnt(3)\n"
    "v_mfma_f32_32x32x16_bf16 v[32:47], v[16:19], a[128:131], v[32:47]\n"
    "ds_read_b64_tr_b16 v[26:27], v1 offset:37120\n"
    "v_mfma_f32_32x32x16_bf16 v[48:63], v[16:19], a[132:135], v[48:63]\n"
    "ds_read_b64_tr_b16 v[28:29], v1 offset:38912\n"
    "s_waitcnt lgkmcnt(3)\n"
    "v_mfma_f32_32x32x16_bf16 v[32:47], v[20:23], a[136:139], v[32:47]\n"
    "ds_read_b64_tr_b16 v[30:31], v1 offset:39168\n"
    "s_mul_i32 s34, s82, 12\n"
    "s_lshl_b32 s35, s76, 1\n"
    "s_add_u32 s34, s34, s35\n"
    "s_add_u32 s34, s34, 8\n"
    "v_mfma_f32_32x32x16_bf16 v[48:63], v[20:23], a[140:143], v[48:63]\n"
    "ds_read_b64_tr_b16 v[16:17], v1 offset:40960\n"
    "s_lshl_b32 s34, s34, 12\n"
    "s_add_u32 s34, s34, 0x990000\n"
    "v_and_b32 v184, 31, v0\n"
    "v_lshlrev_b32 v184, 3, v184\n"
    "s_waitcnt lgkmcnt(3)\n"
    "v_mfma_f32_32x32x16_bf16 v[32:47], v[24:27], a[144:147], v[32:47]\n"
    "ds_read_b64_tr_b16 v[18:19], v1 offset:41216\n"
    "v_lshrrev_b32 v185, 5, v0\n"
    "v_lshl_add_u32 v15, v185, 11, v184\n"
    "buffer_load_dwordx2 v[168:169], v15, s[48:51], s34 offen sc1\n"
    "buffer_load_dwordx2 v[170:171], v15, s[48:51], s34 offen offset:256 sc1\n"
    "v_mfma_f32_32x32x16_bf16 v[48:63], v[24:27], a[148:151], v[48:63]\n"
    "ds_read_b64_tr_b16 v[20:21], v1 offset:43008\n"
    "buffer_load_dwordx2 v[172:173], v15, s[48:51], s34 offen offset:512 sc1\n"
    "buffer_load_dwordx2 v[174:175], v15, s[48:51], s34 offen offset:768 sc1\n"
    "buffer_load_dwordx2 v[176:177], v15, s[48:51], s34 offen offset:1024 sc1\n"
    "buffer_load_dwordx2 v[178:179], v15, s[48:51], s34 offen offset:1280 sc1\n"
    "s_waitcnt lgkmcnt(3)\n"
    "v_mfma_f32_32x32x16_bf16 v[32:47], v[28:31], a[152:155], v[32:47]\n"
    "ds_read_b64_tr_b16 v[22:23], v1 offset:43264\n"
    "buffer_load_dwordx2 v[180:181], v15, s[48:51], s34 offen offset:1536 sc1\n"
    "buffer_load_dwordx2 v[182:183], v15, s[48:51], s34 offen offset:1792 sc1\n"
    "v_mfma_f32_32x32x16_bf16 v[48:63], v[28:31], a[156:159], v[48:63]\n"
    "ds_read_b64_tr_b16 v[24:25], v1 offset:45056\n"
    "s_waitcnt lgkmcnt(3)\n"
    "v_mfma_f32_32x32x16_bf16 v[32:47], v[16:19], a[160:163], v[32:47]\n"
    "ds_read_b64_tr_b16 v[26:27], v1 offset:45312\n"
    "v_mfma_f32_32x32x16_bf16 v[48:63], v[16:19], a[164:167], v[48:63]\n"
    "ds_read_b64_tr_b16 v[28:29], v1 offset:47104\n"
    "s_waitcnt lgkmcnt(3)\n"
    "v_mfma_f32_32x32x16_bf16 v[32:47], v[20:23], a[168:171], v[32:47]\n"
    "ds_read_b64_tr_b16 v[30:31], v1 offset:47360\n"
    "v_mfma_f32_32x32x16_bf16 v[48:63], v[20:23], a[172:175], v[48:63]\n"
    "s_barrier\n"
    "v_add_u32 v1, 0xfffe8000, v1\n"
    "ds_read_b64_tr_b16 v[16:17], v1\n"
    "s_mul_i32 s74, s77, 0x20000\n"
    "s_add_u32 s74, s74, 0x190000\n"
    "s_add_u32 m0, s81, 0x1e000\n"
    "s_nop 0\n"
    "buffer_load_dwordx4 v6, s[52:55], s74 offen lds\n"
    "s_waitcnt lgkmcnt(3)\n"
    "v_mfma_f32_32x32x16_bf16 v[32:47], v[24:27], a[176:179], v[32:47]\n"
    "ds_read_b64_tr_b16 v[18:19], v1 offset:256\n"
    "buffer_load_dwordx4 v7, s[52:55], s74 offen offset:1024 lds\n"
    "v_mfma_f32_32x32x16_bf16 v[48:63], v[24:27], a[180:183], v[48:63]\n"
    "ds_read_b64_tr_b16 v[20:21], v1 offset:2048\n"
    "s_waitcnt vmcnt(14)\n"
    "buffer_store_dword v250, v249, s[48:51], s84 offen offset:8 sc1\n"
    "s_waitcnt lgkmcnt(3)\n"
    "v_mfma_f32_32x32x16_bf16 v[32:47], v[28:31], a[184:187], v[32:47]\n"
    "ds_read_b64_tr_b16 v[22:23], v1 offset:2304\n"
    "v_mfma_f32_32x32x16_bf16 v[48:63], v[28:31], a[188:191], v[48:63]\n"
    "ds_read_b64_tr_b16 v[24:25], v1 offset:4096\n"
    "s_waitcnt lgkmcnt(3)\n"
    "v_mfma_f32_32x32x16_bf16 v[32:47], v[16:19], a[192:195], v[32:47]\n"
    "ds_read_b64_tr_b16 v[26:27], v1 offset:4352\n"
    "v_mfma_f32_32x32x16_bf16 v[48:63], v[16:19], a[196:199], v[48:63]\n"
    "ds_read_b64_tr_b16 v[28:29], v1 offset:6144\n"
    "s_waitcnt lgkmcnt(3)\n"
    "v_mfma_f32_32x32x16_bf16 v[32:47], v[20:23], a[200:203], v[32:47]\n"
    "ds_read_b64_tr_b16 v[30:31], v1 offset:6400\n"
    "v_mfma_f32_32x32x16_bf16 v[48:63], v[20:23], a[204:207], v[48:63]\n"
    "ds_read_b64_tr_b16 v[16:17], v1 offset:8192\n"
    "s_waitcnt lgkmcnt(3)\n"
    "v_mfma_f32_32x32x16_bf16 v[32:47], v[24:27], a[208:211], v[32:47]\n"
    "ds_read_b64_tr_b16 v[18:19], v1 offset:8448\n"
    "v_mfma_f32_32x32x16_bf16 v[48:63], v[24:27], a[212:215], v[48:63]\n"
    "ds_read_b64_tr_b16 v[20:21], v1 offset:10240\n"
    "s_waitcnt lgkmcnt(3)\n"
    "v_mfma_f32_32x32x16_bf16 v[32:47], v[28:31], a[216:219], v[32:47]\n"
    "ds_read_b64_tr_b16 v[22:23], v1 offset:10496\n"
    "v_mfma_f32_32x32x16_bf16 v[48:63], v[28:31], a[220:223], v[48:63]\n"
    "ds_read_b64_tr_b16 v[24:25], v1 offset:12288\n"
    "s_waitcnt lgkmcnt(3)\n"
    "v_mfma_f32_32x32x16_bf16 v[32:47], v[16:19], a[224:227], v[32:47]\n"
    "ds_read_b64_tr_b16 v[26:27], v1 offset:12544\n"
    "v_mfma_f32_32x32x16_bf16 v[48:63], v[16:19], a[228:231], v[48:63]\n"
    "ds_read_b64_tr_b16 v[28:29], v1 offset:14336\n"
    "s_waitcnt lgkmcnt(3)\n"
    "v_mfma_f32_32x32x16_bf16 v[32:47], v[20:23], a[232:235], v[32:47]\n"
    "ds_read_b64_tr_b16 v[30:31], v1 offset:14592\n"
    "v_mfma_f32_32x32x16_bf16 v[48:63], v[20:23], a[236:239], v[48:63]\n"
    "s_waitcnt lgkmcnt(2)\n"
    "v_mfma_f32_32x32x16_bf16 v[32:47], v[24:27], a[240:243], v[32:47]\n"
    "v_mfma_f32_32x32x16_bf16 v[48:63], v[24:27], a[244:247], v[48:63]\n"
    "s_waitcnt lgkmcnt(0)\n"
    "v_mfma_f32_32x32x16_bf16 v[32:47], v[28:31], a[248:251], v[32:47]\n"
    "v_mfma_f32_32x32x16_bf16 v[48:63], v[28:31], a[252:255], v[48:63]\n"
    "s_waitcnt vmcnt(10)\n"
    "v_xor_b32 v185, s72, v169\n"
    "s_waitcnt vmcnt(9)\n"
    "v_bitop3_b32 v185, v185, v171, s72 bitop3:0xf6\n"
    "s_waitcnt vmcnt(8)\n"
    "v_bitop3_b32 v185, v185, v173, s72 bitop3:0xf6\n"
    "s_waitcnt vmcnt(7)\n"
    "v_bitop3_b32 v185, v185, v175, s72 bitop3:0xf6\n"
    "s_waitcnt vmcnt(6)\n"
    "v_bitop3_b32 v185, v185, v177, s72 bitop3:0xf6\n"
    "s_waitcnt vmcnt(5)\n"
    "v_bitop3_b32 v185, v185, v179, s72 bitop3:0xf6\n"
    "s_waitcnt vmcnt(4)\n"
    "v_bitop3_b32 v185, v185, v181, s72 bitop3:0xf6\n"
    "s_waitcnt vmcnt(3)\n"
    "v_bitop3_b32 v185, v185, v183, s72 bitop3:0xf6\n"
    "v_cmp_ne_u32 s[88:89], 0, v185\n"
    "s_cmp_lg_u64 s[88:89], 0\n"
    "s_cbranch_scc1 LB_slow_7%=\n"
    "LB_back_8%=:\n"
    "v_add_f32 v168, v168, v170\n"
    "v_add_f32 v172, v172, v174\n"
    "v_add_f32 v176, v176, v178\n"
    "v_add_f32 v180, v180, v182\n"
    "v_add_f32 v168, v168, v172\n"
    "v_add_f32 v176, v176, v180\n"
    "v_add_f32 v168, v168, v176\n"
    "v_cvt_pk_bf16_f32 v185, v168, v168\n"
    "v_and_b32 v185, 0xffff0000, v185\n"
    "v_sub_f32 v185, v168, v185\n"
    "v_cvt_pk_bf16_f32 v188, v168, v185\n"
    "v_mov_b32 v189, 0\n"
    "s_lshl_b32 s10, s71, 10\n"
    "s_lshl_b32 s11, s76, 9\n"
    "v_mfma_f32_32x32x8_bf16 v[32:47], v[188:189], v[160:161], v[32:47]\n"
    "v_mfma_f32_32x32x8_bf16 v[48:63], v[188:189], v[162:163], v[48:63]\n"
    "s_add_u32 s10, s10, s11\n"
    "s_add_u32 s10, s10, s83\n"
    "s_add_u32 s10, s10, 0xc000\n"
    "s_add_u32 s11, s10, 0x24000\n"
    "v_bfe_u32 v236, v0, 2, 2\n"
    "v_and_b32 v237, 3, v0\n"
    "v_bfe_u32 v238, v0, 4, 1\n"
    "v_lshrrev_b32 v239, 5, v0\n"
    "v_lshl_add_u32 v240, v238, 2, v236\n"
    "v_lshl_add_u32 v240, v239, 4, v240\n"
    "v_lshlrev_b32 v240, 7, v240\n"
    "v_mul_f32 v32, v32, v40\n"
    "v_mul_f32 v33, v33, v41\n"
    "v_mul_f32 v34, v34, v42\n"
    "v_mul_f32 v35, v35, v43\n"
    "v_mul_f32 v36, v36, v44\n"
    "v_mul_f32 v37, v37, v45\n"
    "v_mul_f32 v38, v38, v46\n"
    "v_mul_f32 v39, v39, v47\n"
    "v_cvt_pk_bf16_f32 v40, v32, v33\n"
    "v_cvt_pk_bf16_f32 v41, v34, v35\n"
    "v_cvt_pk_bf16_f32 v42, v36, v37\n"
    "v_cvt_pk_bf16_f32 v43, v38, v39\n"
    "buffer_store_dwordx4 v[40:43], v14, s[48:51], s10 offen sc1\n"
    "v_mul_f32 v48, v48, v56\n"
    "v_mul_f32 v49, v49, v57\n"
    "v_mul_f32 v50, v50, v58\n"
    "v_mul_f32 v51, v51, v59\n"
    "v_mul_f32 v52, v52, v60\n"
    "v_mul_f32 v53, v53, v61\n"
    "v_mul_f32 v54, v54, v62\n"
    "v_mul_f32 v55, v55, v63\n"
    "v_cvt_pk_bf16_f32 v56, v48, v49\n"
    "v_cvt_pk_bf16_f32 v57, v50, v51\n"
    "v_cvt_pk_bf16_f32 v58, v52, v53\n"
    "v_cvt_pk_bf16_f32 v59, v54, v55\n"
    "buffer_store_dwordx4 v[56:59], v14, s[48:51], s11 offen sc1\n"
    "v_lshl_add_u32 v240, v237, 3, v240\n"
    "v_lshrrev_b32 v236, 1, v236\n"
    "v_lshl_add_u32 v241, v238, 1, v236\n"
    "v_xor_b32 v237, 0, v241\n"
    "v_lshl_add_u32 v2, v237, 5, v240\n"
    "v_xor_b32 v237, 1, v241\n"
    "v_lshl_add_u32 v3, v237, 5, v240\n"
    "v_xor_b32 v237, 2, v241\n"
    "v_lshl_add_u32 v4, v237, 5, v240\n"
    "v_xor_b32 v237, 3, v241\n"
    "v_lshl_add_u32 v5, v237, 5, v240\n"
    "v_lshlrev_b32 v8, 4, v0\n"
    "v_lshlrev_b32 v246, 3, v0\n"
    "s_xor_b32 s85, s77, s75\n"
    "s_lshl_b32 s86, s78, 1\n"
    "s_add_u32 s87, s86, s85\n"
    "s_lshl_b32 s8, s87, 9\n"
    "s_add_u32 s8, s8, 0xa54000\n"
    "s_lshl_b32 s87, s87, 1\n"
    "s_add_u32 s87, s87, s76\n"
    "s_mul_i32 s98, s87, 0x24000\n"
    "s_add_u32 s87, s86, s77\n"
    "s_lshl_b32 s9, s87, 9\n"
    "s_add_u32 s9, s9, 0xa54000\n"
    "s_lshl_b32 s87, s87, 1\n"
    "s_add_u32 s87, s87, s76\n"
    "s_mul_i32 s99, s87, 0x24000\n"
    "s_lshl_b32 s85, s75, 13\n"
    "s_add_u32 s99, s99, s85\n"
    "s_add_u32 s99, s99, 0xe000\n"
    "buffer_load_dwordx2 v[244:245], v246, s[48:51], s8 offen sc1\n"
    "v_mov_b32 v249, 0\n"
    "v_mov_b32 v250, s72\n"
    "s_waitcnt vmcnt(1)\n"
    "buffer_store_dword v250, v249, s[48:51], s84 offen offset:16 sc1\n"
    "s_lshl_b32 s30, s77, 4\n"
    "s_lshl_b32 s94, 0x1111, s30\n"
    "s_mov_b32 s95, 0\n"
    "s_waitcnt vmcnt(1)\n"
    "v_cmp_ne_u32 s[88:89], s72, v244\n"
    "v_cmp_ne_u32 s[90:91], s72, v245\n"
    "s_or_b64 s[88:89], s[88:89], s[90:91]\n"
    "s_and_b64 s[88:89], s[88:89], s[94:95]\n"
    "s_cbranch_scc1 LB_slow_9%=\n"
    "LB_back_10%=:\n"
    "s_mul_i32 s30, s77, 0x4000\n"
    "s_add_u32 s30, s30, s98\n"
    "s_add_u32 s30, s30, 0\n"
    "s_add_u32 s31, s30, 0x12000\n"
    "buffer_load_dwordx4 a[0:3], v8, s[48:51], s30 offen sc1\n"
    "buffer_load_dwordx4 a[4:7], v8, s[48:51], s31 offen sc1\n"
    "buffer_load_dwordx4 a[8:11], v8, s[48:51], s30 offen offset:1024 sc1\n"
    "buffer_load_dwordx4 a[12:15], v8, s[48:51], s31 offen offset:1024 sc1\n"
    "s_mul_i32 s30, s77, 0x4000\n"
    "s_add_u32 s30, s30, s98\n"
    "s_add_u32 s30, s30, 0x1000\n"
    "s_add_u32 s31, s30, 0x12000\n"
    "buffer_load_dwordx4 a[16:19], v8, s[48:51], s30 offen sc1\n"
    "buffer_load_dwordx4 a[20:23], v8, s[48:51], s31 offen sc1\n"
    "buffer_load_dwordx4 a[24:27], v8, s[48:51], s30 offen offset:1024 sc1\n"
    "buffer_load_dwordx4 a[28:31], v8, s[48:51], s31 offen offset:1024 sc1\n"
    "s_mul_i32 s30, s77, 0x4000\n"
    "s_add_u32 s30, s30, s98\n"
    "s_add_u32 s30, s30, 0x2000\n"
    "s_add_u32 s31, s30, 0x12000\n"
    "buffer_load_dwordx4 a[32:35], v8, s[48:51], s30 offen sc1\n"
    "buffer_load_dwordx4 a[36:39], v8, s[48:51], s31 offen sc1\n"
    "buffer_load_dwordx4 a[40:43], v8, s[48:51], s30 offen offset:1024 sc1\n"
    "buffer_load_dwordx4 a[44:47], v8, s[48:51], s31 offen offset:1024 sc1\n"
    "s_mul_i32 s30, s77, 0x4000\n"
    "s_add_u32 s30, s30, s98\n"
    "s_add_u32 s30, s30, 0x3000\n"
    "s_add_u32 s31, s30, 0x12000\n"
    "buffer_load_dwordx4 a[48:51], v8, s[48:51], s30 offen sc1\n"
    "buffer_load_dwordx4 a[52:55], v8, s[48:51], s31 offen sc1\n"
    "buffer_load_dwordx4 a[56:59], v8, s[48:51], s30 offen offset:1024 sc1\n"
    "buffer_load_dwordx4 a[60:63], v8, s[48:51], s31 offen offset:1024 sc1\n"
    "s_lshl_b32 s30, s77, 4\n"
    "s_lshl_b32 s95, 0x2222, s30\n"
    "s_mov_b32 s94, 0\n"
    "v_cmp_ne_u32 s[88:89], s72, v244\n"
    "v_cmp_ne_u32 s[90:91], s72, v245\n"
    "s_or_b64 s[88:89], s[88:89], s[90:91]\n"
    "s_and_b64 s[88:89], s[88:89], s[94:95]\n"
    "s_cbranch_scc1 LB_slow_11%=\n"
    "LB_back_12%=:\n"
    "s_mul_i32 s30, s77, 0x2000\n"
    "s_add_u32 s30, s30, s98\n"
    "s_add_u32 s30, s30, 0x8000\n"
    "s_add_u32 s31, s30, 0x12000\n"
    "buffer_load_dwordx4 a[64:67], v8, s[48:51], s30 offen sc1\n"
    "buffer_load_dwordx4 a[68:71], v8, s[48:51], s31 offen sc1\n"
    "buffer_load_dwordx4 a[72:75], v8, s[48:51], s30 offen offset:1024 sc1\n"
    "buffer_load_dwordx4 a[76:79], v8, s[48:51], s31 offen offset:1024 sc1\n"
    "s_mul_i32 s30, s77, 0x2000\n"
    "s_add_u32 s30, s30, s98\n"
    "s_add_u32 s30, s30, 0x8800\n"
    "s_add_u32 s31, s30, 0x12000\n"
    "buffer_load_dwordx4 a[80:83], v8, s[48:51], s30 offen sc1\n"
    "buffer_load_dwordx4 a[84:87], v8, s[48:51], s31 offen sc1\n"
    "buffer_load_dwordx4 a[88:91], v8, s[48:51], s30 offen offset:1024 sc1\n"
    "buffer_load_dwordx4 a[92:95], v8, s[48:51], s31 offen offset:1024 sc1\n"
    "s_mul_i32 s30, s77, 0x2000\n"
    "s_add_u32 s30, s30, s98\n"
    "s_add_u32 s30, s30, 0x9000\n"
    "s_add_u32 s31, s30, 0x12000\n"
    "buffer_load_dwordx4 a[96:99], v8, s[48:51], s30 offen sc1\n"
    "buffer_load_dwordx4 a[100:103], v8, s[48:51], s31 offen sc1\n"
    "buffer_load_dwordx4 a[104:107], v8, s[48:51], s30 offen offset:1024 sc1\n"
    "buffer_load_dwordx4 a[108:111], v8, s[48:51], s31 offen offset:1024 sc1\n"
    "s_mul_i32 s30, s77, 0x2000\n"
    "s_add_u32 s30, s30, s98\n"
    "s_add_u32 s30, s30, 0x9800\n"
    "s_add_u32 s31, s30, 0x12000\n"
    "buffer_load_dwordx4 a[112:115], v8, s[48:51], s30 offen sc1\n"
    "buffer_load_dwordx4 a[116:119], v8, s[48:51], s31 offen sc1\n"
    "buffer_load_dwordx4 a[120:123], v8, s[48:51], s30 offen offset:1024 sc1\n"
    "buffer_load_dwordx4 a[124:127], v8, s[48:51], s31 offen offset:1024 sc1\n"
    "s_barrier\n"
    "ds_read_b64_tr_b16 v[16:17], v2 offset:16384\n"
    "ds_read_b64_tr_b16 v[18:19], v2 offset:17408\n"
    "ds_read_b64_tr_b16 v[20:21], v3 offset:16384\n"
    "ds_read_b64_tr_b16 v[22:23], v3 offset:17408\n"
    "ds_read_b64_tr_b16 v[24:25], v4 offset:16384\n"
    "ds_read_b64_tr_b16 v[26:27], v4 offset:17408\n"
    "ds_read_b64_tr_b16 v[28:29], v5 offset:16384\n"
    "ds_read_b64_tr_b16 v[30:31], v5 offset:17408\n"
    "ds_read_b64_tr_b16 v[168:169], v2 offset:20480\n"
    "s_waitcnt vmcnt(31) lgkmcnt(7)\n"
    "v_mfma_f32_16x16x32_bf16 v[32:35], v[16:19], a[0:3], 0\n"
    "ds_read_b64_tr_b16 v[170:171], v2 offset:21504\n"
    "s_waitcnt vmcnt(30)\n"
    "v_mfma_f32_16x16x32_bf16 v[48:51], v[16:19], a[4:7], 0\n"
    "ds_read_b64_tr_b16 v[172:173], v3 offset:20480\n"
    "s_lshl_b32 s30, s79, 7\n"
    "s_add_u32 s52, s18, s30\n"
    "s_addc_u32 s53, s19, 0\n"
    "s_sub_u32 s54, 0x80000, s30\n"
    "s_mov_b32 s55, 0x20000\n"
    "s_mov_b32 s74, 0\n"
    "s_add_u32 m0, s81, 0x20000\n"
    "s_nop 0\n"
    "buffer_load_dwordx4 v6, s[52:55], s74 offen lds\n"
    "s_waitcnt lgkmcnt(7)\n"
    "v_mfma_f32_16x16x32_bf16 v[52:55], v[20:23], a[4:7], 0\n"
    "ds_read_b64_tr_b16 v[174:175], v3 offset:21504\n"
    "v_mfma_f32_16x16x32_bf16 v[36:39], v[20:23], a[0:3], 0\n"
    "ds_read_b64_tr_b16 v[176:177], v4 offset:20480\n"
    "buffer_load_dwordx4 v7, s[52:55], s74 offen offset:1024 lds\n"
    "s_waitcnt lgkmcnt(7)\n"
    "v_mfma_f32_16x16x32_bf16 v[40:43], v[24:27], a[0:3], 0\n"
    "ds_read_b64_tr_b16 v[178:179], v4 offset:21504\n"
    "v_mfma_f32_16x16x32_bf16 v[56:59], v[24:27], a[4:7], 0\n"
    "ds_read_b64_tr_b16 v[180:181], v5 offset:20480\n"
    "s_mov_b32 s74, 0x40000\n"
    "s_add_u32 m0, s81, 0x22000\n"
    "s_nop 0\n"
    "buffer_load_dwordx4 v6, s[52:55], s74 offen lds\n"
    "s_waitcnt lgkmcnt(7)\n"
    "v_mfma_f32_16x16x32_bf16 v[60:63], v[28:31], a[4:7], 0\n"
    "ds_read_b64_tr_b16 v[182:183], v5 offset:21504\n"
    "v_mfma_f32_16x16x32_bf16 v[44:47], v[28:31], a[0:3], 0\n"
    "ds_read_b64_tr_b16 v[16:17], v2 offset:24576\n"
    "buffer_load_dwordx4 v7, s[52:55], s74 offen offset:1024 lds\n"
    "s_waitcnt vmcnt(33) lgkmcnt(7)\n"
    "v_mfma_f32_16x16x32_bf16 v[32:35], v[168:171], a[8:11], v[32:35]\n"
    "ds_read_b64_tr_b16 v[18:19], v2 offset:25600\n"
    "s_waitcnt vmcnt(32)\n"
    "v_mfma_f32_16x16x32_bf16 v[48:51], v[168:171], a[12:15], v[48:51]\n"
    "ds_read_b64_tr_b16 v[20:21], v3 offset:24576\n"
    "s_mov_b32 s74, 0x10000\n"
    "s_add_u32 m0, s81, 0\n"
    "s_nop 0\n"
    "buffer_load_dwordx4 v6, s[52:55], s74 offen lds\n"
    "s_waitcnt lgkmcnt(7)\n"
    "v_mfma_f32_16x16x32_bf16 v[52:55], v[172:175], a[12:15], v[52:55]\n"
    "ds_read_b64_tr_b16 v[22:23], v3 offset:25600\n"
    "v_mfma_f32_16x16x32_bf16 v[36:39], v[172:175], a[8:11], v[36:39]\n"
    "ds_read_b64_tr_b16 v[24:25], v4 offset:24576\n"
    "buffer_load_dwordx4 v7, s[52:55], s74 offen offset:1024 lds\n"
    "s_waitcnt lgkmcnt(7)\n"
    "v_mfma_f32_16x16x32_bf16 v[40:43], v[176:179], a[8:11], v[40:43]\n"
    "ds_read_b64_tr_b16 v[26:27], v4 offset:25600\n"
    "v_mfma_f32_16x16x32_bf16 v[56:59], v[176:179], a[12:15], v[56:59]\n"
    "ds_read_b64_tr_b16 v[28:29], v5 offset:24576\n"
    "s_mov_b32 s74, 0x50000\n"
    "s_add_u32 m0, s81, 0x2000\n"
    "s_nop 0\n"
    "buffer_load_dwordx4 v6, s[52:55], s74 offen lds\n"
    "s_waitcnt lgkmcnt(7)\n"
    "v_mfma_f32_16x16x32_bf16 v[60:63], v[180:183], a[12:15], v[60:63]\n"
    "ds_read_b64_tr_b16 v[30:31], v5 offset:25600\n"
    "v_mfma_f32_16x16x32_bf16 v[44:47], v[180:183], a[8:11], v[44:47]\n"
    "ds_read_b64_tr_b16 v[168:169], v2 offset:28672\n"
    "buffer_load_dwordx4 v7, s[52:55], s74 offen offset:1024 lds\n"
    "buffer_load_dwordx2 v[244:245], v246, s[48:51], s8 offen sc1\n"
    "s_waitcnt vmcnt(36) lgkmcnt(7)\n"
    "v_mfma_f32_16x16x32_bf16 v[32:35], v[16:19], a[16:19], v[32:35]\n"
    "ds_read_b64_tr_b16 v[170:171], v2 offset:29696\n"
    "s_waitcnt vmcnt(35)\n"
    "v_mfma_f32_16x16x32_bf16 v[48:51], v[16:19], a[20:23], v[48:51]\n"
    "ds_read_b64_tr_b16 v[172:173], v3 offset:28672\n"
    "s_waitcnt lgkmcnt(7)\n"
    "v_mfma_f32_16x16x32_bf16 v[52:55], v[20:23], a[20:23], v[52:55]\n"
    "ds_read_b64_tr_b16 v[174:175], v3 offset:29696\n"
    "v_mfma_f32_16x16x32_bf16 v[36:39], v[20:23], a[16:19], v[36:39]\n"
    "ds_read_b64_tr_b16 v[176:177], v4 offset:28672\n"
    "s_waitcnt lgkmcnt(7)\n"
    "v_mfma_f32_16x16x32_bf16 v[40:43], v[24:27], a[16:19], v[40:43]\n"
    "ds_read_b64_tr_b16 v[178:179], v4 offset:29696\n"
    "v_mfma_f32_16x16x32_bf16 v[56:59], v[24:27], a[20:23], v[56:59]\n"
    "ds_read_b64_tr_b16 v[180:181], v5 offset:28672\n"
    "s_waitcnt lgkmcnt(7)\n"
    "v_mfma_f32_16x16x32_bf16 v[60:63], v[28:31], a[20:23], v[60:63]\n"
    "ds_read_b64_tr_b16 v[182:183], v5 offset:29696\n"
    "v_mfma_f32_16x16x32_bf16 v[44:47], v[28:31], a[16:19], v[44:47]\n"
    "ds_read_b64_tr_b16 v[16:17], v2 offset:32768\n"
    "s_waitcnt vmcnt(34) lgkmcnt(7)\n"
    "v_mfma_f32_16x16x32_bf16 v[32:35], v[168:171], a[24:27], v[32:35]\n"
    "ds_read_b64_tr_b16 v[18:19], v2 offset:33792\n"
    "s_waitcnt vmcnt(33)\n"
    "v_mfma_f32_16x16x32_bf16 v[48:51], v[168:171], a[28:31], v[48:51]\n"
    "ds_read_b64_tr_b16 v[20:21], v3 offset:32768\n"
    "s_waitcnt lgkmcnt(7)\n"
    "v_mfma_f32_16x16x32_bf16 v[52:55], v[172:175], a[28:31], v[52:55]\n"
    "ds_read_b64_tr_b16 v[22:23], v3 offset:33792\n"
    "v_mfma_f32_16x16x32_bf16 v[36:39], v[172:175], a[24:27], v[36:39]\n"
    "ds_read_b64_tr_b16 v[24:25], v4 offset:32768\n"
    "s_waitcnt lgkmcnt(7)\n"
    "v_mfma_f32_16x16x32_bf16 v[40:43], v[176:179], a[24:27], v[40:43]\n"
    "ds_read_b64_tr_b16 v[26:27], v4 offset:33792\n"
    "v_mfma_f32_16x16x32_bf16 v[56:59], v[176:179], a[28:31], v[56:59]\n"
    "ds_read_b64_tr_b16 v[28:29], v5 offset:32768\n"
    "s_waitcnt lgkmcnt(7)\n"
    "v_mfma_f32_16x16x32_bf16 v[60:63], v[180:183], a[28:31], v[60:63]\n"
    "ds_read_b64_tr_b16 v[30:31], v5 offset:33792\n"
    "v_mfma_f32_16x16x32_bf16 v[44:47], v[180:183], a[24:27], v[44:47]\n"
    "ds_read_b64_tr_b16 v[168:169], v2 offset:36864\n"
    "s_waitcnt vmcnt(32) lgkmcnt(7)\n"
    "v_mfma_f32_16x16x32_bf16 v[32:35], v[16:19], a[32:35], v[32:35]\n"
    "ds_read_b64_tr_b16 v[170:171], v2 offset:37888\n"
    "s_waitcnt vmcnt(31)\n"
    "v_mfma_f32_16x16x32_bf16 v[48:51], v[16:19], a[36:39], v[48:51]\n"
    "ds_read_b64_tr_b16 v[172:173], v3 offset:36864\n"
    "s_waitcnt lgkmcnt(7)\n"
    "v_mfma_f32_16x16x32_bf16 v[52:55], v[20:23], a[36:39], v[52:55]\n"
    "ds_read_b64_tr_b16 v[174:175], v3 offset:37888\n"
    "v_mfma_f32_16x16x32_bf16 v[36:39], v[20:23], a[32:35], v[36:39]\n"
    "ds_read_b64_tr_b16 v[176:177], v4 offset:36864\n"
    "s_waitcnt lgkmcnt(7)\n"
    "v_mfma_f32_16x16x32_bf16 v[40:43], v[24:27], a[32:35], v[40:43]\n"
    "ds_read_b64_tr_b16 v[178:179], v4 offset:37888\n"
    "v_mfma_f32_16x16x32_bf16 v[56:59], v[24:27], a[36:39], v[56:59]\n"
    "ds_read_b64_tr_b16 v[180:181], v5 offset:36864\n"
    "s_waitcnt lgkmcnt(7)\n"
    "v_mfma_f32_16x16x32_bf16 v[60:63], v[28:31], a[36:39], v[60:63]\n"
    "ds_read_b64_tr_b16 v[182:183], v5 offset:37888\n"
    "v_mfma_f32_16x16x32_bf16 v[44:47], v[28:31], a[32:35], v[44:47]\n"
    "ds_read_b64_tr_b16 v[16:17], v2 offset:40960\n"
    "s_waitcnt vmcnt(30) lgkmcnt(7)\n"
    "v_mfma_f32_16x16x32_bf16 v[32:35], v[168:171], a[40:43], v[32:35]\n"
    "ds_read_b64_tr_b16 v[18:19], v2 offset:41984\n"
    "s_waitcnt vmcnt(29)\n"
    "v_mfma_f32_16x16x32_bf16 v[48:51], v[168:171], a[44:47], v[48:51]\n"
    "ds_read_b64_tr_b16 v[20:21], v3 offset:40960\n"
    "s_waitcnt lgkmcnt(7)\n"
    "v_mfma_f32_16x16x32_bf16 v[52:55], v[172:175], a[44:47], v[52:55]\n"
    "ds_read_b64_tr_b16 v[22:23], v3 offset:41984\n"
    "v_mfma_f32_16x16x32_bf16 v[36:39], v[172:175], a[40:43], v[36:39]\n"
    "ds_read_b64_tr_b16 v[24:25], v4 offset:40960\n"
    "s_waitcnt lgkmcnt(7)\n"
    "v_mfma_f32_16x16x32_bf16 v[40:43], v[176:179], a[40:43], v[40:43]\n"
    "ds_read_b64_tr_b16 v[26:27], v4 offset:41984\n"
    "v_mfma_f32_16x16x32_bf16 v[56:59], v[176:179], a[44:47], v[56:59]\n"
    "ds_read_b64_tr_b16 v[28:29], v5 offset:40960\n"
    "s_waitcnt lgkmcnt(7)\n"
    "v_mfma_f32_16x16x32_bf16 v[60:63], v[180:183], a[44:47], v[60:63]\n"
    "ds_read_b64_tr_b16 v[30:31], v5 offset:41984\n"
    "v_mfma_f32_16x16x32_bf16 v[44:47], v[180:183], a[40:43], v[44:47]\n"
    "ds_read_b64_tr_b16 v[168:169], v2 offset:45056\n"
    "s_waitcnt vmcnt(28) lgkmcnt(7)\n"
    "v_mfma_f32_16x16x32_bf16 v[32:35], v[16:19], a[48:51], v[32:35]\n"
    "ds_read_b64_tr_b16 v[170:171], v2 offset:46080\n"
    "s_waitcnt vmcnt(27)\n"
    "v_mfma_f32_16x16x32_bf16 v[48:51], v[16:19], a[52:55], v[48:51]\n"
    "ds_read_b64_tr_b16 v[172:173], v3 offset:45056\n"
    "s_waitcnt lgkmcnt(7)\n"
    "v_mfma_f32_16x16x32_bf16 v[52:55], v[20:23], a[52:55], v[52:55]\n"
    "ds_read_b64_tr_b16 v[174:175], v3 offset:46080\n"
    "v_mfma_f32_16x16x32_bf16 v[36:39], v[20:23], a[48:51], v[36:39]\n"
    "ds_read_b64_tr_b16 v[176:177], v4 offset:45056\n"
    "s_waitcnt lgkmcnt(7)\n"
    "v_mfma_f32_16x16x32_bf16 v[40:43], v[24:27], a[48:51], v[40:43]\n"
    "ds_read_b64_tr_b16 v[178:179], v4 offset:46080\n"
    "v_mfma_f32_16x16x32_bf16 v[56:59], v[24:27], a[52:55], v[56:59]\n"
    "ds_read_b64_tr_b16 v[180:181], v5 offset:45056\n"
    "s_waitcnt lgkmcnt(7)\n"
    "v_mfma_f32_16x16x32_bf16 v[60:63], v[28:31], a[52:55], v[60:63]\n"
    "ds_read_b64_tr_b16 v[182:183], v5 offset:46080\n"
    "v_mfma_f32_16x16x32_bf16 v[44:47], v[28:31], a[48:51], v[44:47]\n"
    "s_barrier\n"
    "ds_read_b64_tr_b16 v[16:17], v2 offset:49152\n"
    "s_mov_b32 s74, 0x20000\n"
    "s_add_u32 m0, s81, 0x4000\n"
    "s_nop 0\n"
    "buffer_load_dwordx4 v6, s[52:55], s74 offen lds\n"
    "s_waitcnt vmcnt(27) lgkmcnt(7)\n"
    "v_mfma_f32_16x16x32_bf16 v[32:35], v[168:171], a[56:59], v[32:35]\n"
    "ds_read_b64_tr_b16 v[18:19], v2 offset:50176\n"
    "s_waitcnt vmcnt(26)\n"
    "v_mfma_f32_16x16x32_bf16 v[48:51], v[168:171], a[60:63], v[48:51]\n"
    "ds_read_b64_tr_b16 v[20:21], v3 offset:49152\n"
    "buffer_load_dwordx4 v7, s[52:55], s74 offen offset:1024 lds\n"
    "s_waitcnt lgkmcnt(7)\n"
    "v_mfma_f32_16x16x32_bf16 v[52:55], v[172:175], a[60:63], v[52:55]\n"
    "ds_read_b64_tr_b16 v[22:23], v3 offset:50176\n"
    "v_mfma_f32_16x16x32_bf16 v[36:39], v[172:175], a[56:59], v[36:39]\n"
    "ds_read_b64_tr_b16 v[24:25], v4 offset:49152\n"
    "s_mov_b32 s74, 0x60000\n"
    "s_add_u32 m0, s81, 0x6000\n"
    "s_nop 0\n"
    "buffer_load_dwordx4 v6, s[52:55], s74 offen lds\n"
    "s_waitcnt lgkmcnt(7)\n"
    "v_mfma_f32_16x16x32_bf16 v[40:43], v[176:179], a[56:59], v[40:43]\n"
    "ds_read_b64_tr_b16 v[26:27], v4 offset:50176\n"
    "v_mfma_f32_16x16x32_bf16 v[56:59], v[176:179], a[60:63], v[56:59]\n"
    "ds_read_b64_tr_b16 v[28:29], v5 offset:49152\n"
    "buffer_load_dwordx4 v7, s[52:55], s74 offen offset:1024 lds\n"
    "s_waitcnt lgkmcnt(7)\n"
    "v_mfma_f32_16x16x32_bf16 v[60:63], v[180:183], a[60:63], v[60:63]\n"
    "ds_read_b64_tr_b16 v[30:31], v5 offset:50176\n"
    "v_mfma_f32_16x16x32_bf16 v[44:47], v[180:183], a[56:59], v[44:47]\n"
    "ds_read_b64_tr_b16 v[168:169], v2 offset:53248\n"
    "s_lshl_b32 s30, s77, 4\n"
    "s_lshl_b32 s94, 0x4444, s30\n"
    "s_mov_b32 s95, 0\n"
    "s_waitcnt vmcnt(4)\n"
    "v_cmp_ne_u32 s[88:89], s72, v244\n"
    "v_cmp_ne_u32 s[90:91], s72, v245\n"
    "s_or_b64 s[88:89], s[88:89], s[90:91]\n"
    "s_and_b64 s[88:89], s[88:89], s[94:95]\n"
    "s_cbranch_scc1 LB_slow_13%=\n"
    "LB_back_14%=:\n"
    "s_mul_i32 s30, s77, 0x4000\n"
    "s_add_u32 s30, s30, s98\n"
    "s_add_u32 s30, s30, 0x800\n"
    "s_add_u32 s31, s30, 0x12000\n"
    "buffer_load_dwordx4 a[128:131], v8, s[48:51], s30 offen sc1\n"
    "buffer_load_dwordx4 a[132:135], v8, s[48:51], s31 offen sc1\n"
    "buffer_load_dwordx4 a[136:139], v8, s[48:51], s30 offen offset:1024 sc1\n"
    "buffer_load_dwordx4 a[140:143], v8, s[48:51], s31 offen offset:1024 sc1\n"
    "s_mul_i32 s30, s77, 0x4000\n"
    "s_add_u32 s30, s30, s98\n"
    "s_add_u32 s30, s30, 0x1800\n"
    "s_add_u32 s31, s30, 0x12000\n"
    "buffer_load_dwordx4 a[144:147], v8, s[48:51], s30 offen sc1\n"
    "buffer_load_dwordx4 a[148:151], v8, s[48:51], s31 offen sc1\n"
    "buffer_load_dwordx4 a[152:155], v8, s[48:51], s30 offen offset:1024 sc1\n"
    "buffer_load_dwordx4 a[156:159], v8, s[48:51], s31 offen offset:1024 sc1\n"
    "s_mul_i32 s30, s77, 0x4000\n"
    "s_add_u32 s30, s30, s98\n"
    "s_add_u32 s30, s30, 0x2800\n"
    "s_add_u32 s31, s30, 0x12000\n"
    "buffer_load_dwordx4 a[160:163], v8, s[48:51], s30 offen sc1\n"
    "buffer_load_dwordx4 a[164:167], v8, s[48:51], s31 offen sc1\n"
    "buffer_load_dwordx4 a[168:171], v8, s[48:51], s30 offen offset:1024 sc1\n"
    "buffer_load_dwordx4 a[172:175], v8, s[48:51], s31 offen offset:1024 sc1\n"
    "s_mul_i32 s30, s77, 0x4000\n"
    "s_add_u32 s30, s30, s98\n"
    "s_add_u32 s30, s30, 0x3800\n"
    "s_add_u32 s31, s30, 0x12000\n"
    "buffer_load_dwordx4 a[176:179], v8, s[48:51], s30 offen sc1\n"
    "buffer_load_dwordx4 a[180:183], v8, s[48:51], s31 offen sc1\n"
    "buffer_load_dwordx4 a[184:187], v8, s[48:51], s30 offen offset:1024 sc1\n"
    "buffer_load_dwordx4 a[188:191], v8, s[48:51], s31 offen offset:1024 sc1\n"
    "s_lshl_b32 s30, s77, 4\n"
    "s_lshl_b32 s95, 0x4444, s30\n"
    "s_mov_b32 s94, 0\n"
    "v_cmp_ne_u32 s[88:89], s72, v244\n"
    "v_cmp_ne_u32 s[90:91], s72, v245\n"
    "s_or_b64 s[88:89], s[88:89], s[90:91]\n"
    "s_and_b64 s[88:89], s[88:89], s[94:95]\n"
    "s_cbranch_scc1 LB_slow_15%=\n"
    "LB_back_16%=:\n"
    "s_mul_i32 s30, s77, 0x1000\n"
    "s_add_u32 s30, s30, s98\n"
    "s_add_u32 s30, s30, 0xc000\n"
    "s_add_u32 s31, s30, 0x12000\n"
    "buffer_load_dwordx4 a[192:195], v8, s[48:51], s30 offen sc1\n"
    "buffer_load_dwordx4 a[196:199], v8, s[48:51], s31 offen sc1\n"
    "buffer_load_dwordx4 a[200:203], v8, s[48:51], s30 offen offset:1024 sc1\n"
    "buffer_load_dwordx4 a[204:207], v8, s[48:51], s31 offen offset:1024 sc1\n"
    "s_mul_i32 s30, s77, 0x1000\n"
    "s_add_u32 s30, s30, s98\n"
    "s_add_u32 s30, s30, 0xc800\n"
    "s_add_u32 s31, s30, 0x12000\n"
    "buffer_load_dwordx4 a[208:211], v8, s[48:51], s30 offen sc1\n"
    "buffer_load_dwordx4 a[212:215], v8, s[48:51], s31 offen sc1\n"
    "buffer_load_dwordx4 a[216:219], v8, s[48:51], s30 offen offset:1024 sc1\n"
    "buffer_load_dwordx4 a[220:223], v8, s[48:51], s31 offen offset:1024 sc1\n"
    "buffer_load_dwordx2 v[244:245], v246, s[48:51], s9 offen sc1\n"
    "s_waitcnt lgkmcnt(7)\n"
    "v_mfma_f32_16x16x32_bf16 v[32:35], v[16:19], a[64:67], v[32:35]\n"
    "ds_read_b64_tr_b16 v[170:171], v2 offset:54272\n"
    "v_mfma_f32_16x16x32_bf16 v[48:51], v[16:19], a[68:71], v[48:51]\n"
    "ds_read_b64_tr_b16 v[172:173], v3 offset:53248\n"
    "s_waitcnt lgkmcnt(7)\n"
    "v_mfma_f32_16x16x32_bf16 v[52:55], v[20:23], a[68:71], v[52:55]\n"
    "ds_read_b64_tr_b16 v[174:175], v3 offset:54272\n"
    "v_mfma_f32_16x16x32_bf16 v[36:39], v[20:23], a[64:67], v[36:39]\n"
    "ds_read_b64_tr_b16 v[176:177], v4 offset:53248\n"
    "s_waitcnt lgkmcnt(7)\n"
    "v_mfma_f32_16x16x32_bf16 v[40:43], v[24:27], a[64:67], v[40:43]\n"
    "ds_read_b64_tr_b16 v[178:179], v4 offset:54272\n"
    "v_mfma_f32_16x16x32_bf16 v[56:59], v[24:27], a[68:71], v[56:59]\n"
    "ds_read_b64_tr_b16 v[180:181], v5 offset:53248\n"
    "s_waitcnt lgkmcnt(7)\n"
    "v_mfma_f32_16x16x32_bf16 v[60:63], v[28:31], a[68:71], v[60:63]\n"
    "ds_read_b64_tr_b16 v[182:183], v5 offset:54272\n"
    "v_mfma_f32_16x16x32_bf16 v[44:47], v[28:31], a[64:67], v[44:47]\n"
    "ds_read_b64_tr_b16 v[16:17], v2 offset:57344\n"
    "s_waitcnt lgkmcnt(7)\n"
    "v_mfma_f32_16x16x32_bf16 v[32:35], v[168:171], a[72:75], v[32:35]\n"
    "ds_read_b64_tr_b16 v[18:19], v2 offset:58368\n"
    "v_mfma_f32_16x16x32_bf16 v[48:51], v[168:171], a[76:79], v[48:51]\n"
    "ds_read_b64_tr_b16 v[20:21], v3 offset:57344\n"
    "s_waitcnt lgkmcnt(7)\n"
    "v_mfma_f32_16x16x32_bf16 v[52:55], v[172:175], a[76:79], v[52:55]\n"
    "ds_read_b64_tr_b16 v[22:23], v3 offset:58368\n"
    "v_mfma_f32_16x16x32_bf16 v[36:39], v[172:175], a[72:75], v[36:39]\n"
    "ds_read_b64_tr_b16 v[24:25], v4 offset:57344\n"
    "s_waitcnt lgkmcnt(7)\n"
    "v_mfma_f32_16x16x32_bf16 v[40:43], v[176:179], a[72:75], v[40:43]\n"
    "ds_read_b64_tr_b16 v[26:27], v4 offset:58368\n"
    "v_mfma_f32_16x16x32_bf16 v[56:59], v[176:179], a[76:79], v[56:59]\n"
    "ds_read_b64_tr_b16 v[28:29], v5 offset:57344\n"
    "s_waitcnt lgkmcnt(7)\n"
    "v_mfma_f32_16x16x32_bf16 v[60:63], v[180:183], a[76:79], v[60:63]\n"
    "ds_read_b64_tr_b16 v[30:31], v5 offset:58368\n"
    "v_mfma_f32_16x16x32_bf16 v[44:47], v[180:183], a[72:75], v[44:47]\n"
    "ds_read_b64_tr_b16 v[168:169], v2 offset:61440\n"
    "s_waitcnt lgkmcnt(7)\n"
    "v_mfma_f32_16x16x32_bf16 v[32:35], v[16:19], a[80:83], v[32:35]\n"
    "ds_read_b64_tr_b16 v[170:171], v2 offset:62464\n"
    "v_mfma_f32_16x16x32_bf16 v[48:51], v[16:19], a[84:87], v[48:51]\n"
    "ds_read_b64_tr_b16 v[172:173], v3 offset:61440\n"
    "s_waitcnt lgkmcnt(7)\n"
    "v_mfma_f32_16x16x32_bf16 v[52:55], v[20:23], a[84:87], v[52:55]\n"
    "ds_read_b64_tr_b16 v[174:175], v3 offset:62464\n"
    "v_mfma_f32_16x16x32_bf16 v[36:39], v[20:23], a[80:83], v[36:39]\n"
    "ds_read_b64_tr_b16 v[176:177], v4 offset:61440\n"
    "s_waitcnt lgkmcnt(7)\n"
    "v_mfma_f32_16x16x32_bf16 v[40:43], v[24:27], a[80:83], v[40:43]\n"
    "ds_read_b64_tr_b16 v[178:179], v4 offset:62464\n"
    "v_mfma_f32_16x16x32_bf16 v[56:59], v[24:27], a[84:87], v[56:59]\n"
    "ds_read_b64_tr_b16 v[180:181], v5 offset:61440\n"
    "s_waitcnt lgkmcnt(7)\n"
    "v_mfma_f32_16x16x32_bf16 v[60:63], v[28:31], a[84:87], v[60:63]\n"
    "ds_read_b64_tr_b16 v[182:183], v5 offset:62464\n"
    "v_mfma_f32_16x16x32_bf16 v[44:47], v[28:31], a[80:83], v[44:47]\n"
    "v_add_u32 v2, 0x10000, v2\n"
    "v_add_u32 v3, 0x10000, v3\n"
    "v_add_u32 v4, 0x10000, v4\n"
    "v_add_u32 v5, 0x10000, v5\n"
    "ds_read_b64_tr_b16 v[16:17], v2\n"
    "s_waitcnt lgkmcnt(7)\n"
    "v_mfma_f32_16x16x32_bf16 v[32:35], v[168:171], a[88:91], v[32:35]\n"
    "ds_read_b64_tr_b16 v[18:19], v2 offset:1024\n"
    "v_mfma_f32_16x16x32_bf16 v[48:51], v[168:171], a[92:95], v[48:51]\n"
    "ds_read_b64_tr_b16 v[20:21], v3\n"
    "s_waitcnt lgkmcnt(7)\n"
    "v_mfma_f32_16x16x32_bf16 v[52:55], v[172:175], a[92:95], v[52:55]\n"
    "ds_read_b64_tr_b16 v[22:23], v3 offset:1024\n"
    "v_mfma_f32_16x16x32_bf16 v[36:39], v[172:175], a[88:91], v[36:39]\n"
    "ds_read_b64_tr_b16 v[24:25], v4\n"
    "s_waitcnt lgkmcnt(7)\n"
    "v_mfma_f32_16x16x32_bf16 v[40:43], v[176:179], a[88:91], v[40:43]\n"
    "ds_read_b64_tr_b16 v[26:27], v4 offset:1024\n"
    "v_mfma_f32_16x16x32_bf16 v[56:59], v[176:179], a[92:95], v[56:59]\n"
    "ds_read_b64_tr_b16 v[28:29], v5\n"
    "s_waitcnt lgkmcnt(7)\n"
    "v_mfma_f32_16x16x32_bf16 v[60:63], v[180:183], a[92:95], v[60:63]\n"
    "ds_read_b64_tr_b16 v[30:31], v5 offset:1024\n"
    "v_mfma_f32_16x16x32_bf16 v[44:47], v[180:183], a[88:91], v[44:47]\n"
    "ds_read_b64_tr_b16 v[168:169], v2 offset:4096\n"
    "s_waitcnt lgkmcnt(7)\n"
    "v_mfma_f32_16x16x32_bf16 v[32:35], v[16:19], a[96:99], v[32:35]\n"
    "ds_read_b64_tr_b16 v[170:171], v2 offset:5120\n"
    "v_mfma_f32_16x16x32_bf16 v[48:51], v[16:19], a[100:103], v[48:51]\n"
    "ds_read_b64_tr_b16 v[172:173], v3 offset:4096\n"
    "s_waitcnt lgkmcnt(7)\n"
    "v_mfma_f32_16x16x32_bf16 v[52:55], v[20:23], a[100:103], v[52:55]\n"
    "ds_read_b64_tr_b16 v[174:175], v3 offset:5120\n"
    "v_mfma_f32_16x16x32_bf16 v[36:39], v[20:23], a[96:99], v[36:39]\n"
    "ds_read_b64_tr_b16 v[176:177], v4 offset:4096\n"
    "s_waitcnt lgkmcnt(7)\n"
    "v_mfma_f32_16x16x32_bf16 v[40:43], v[24:27], a[96:99], v[40:43]\n"
    "ds_read_b64_tr_b16 v[178:179], v4 offset:5120\n"
    "v_mfma_f32_16x16x32_bf16 v[56:59], v[24:27], a[100:103], v[56:59]\n"
    "ds_read_b64_tr_b16 v[180:181], v5 offset:4096\n"
    "s_waitcnt lgkmcnt(7)\n"
    "v_mfma_f32_16x16x32_bf16 v[60:63], v[28:31], a[100:103], v[60:63]\n"
    "ds_read_b64_tr_b16 v[182:183], v5 offset:5120\n"
    "v_mfma_f32_16x16x32_bf16 v[44:47], v[28:31], a[96:99], v[44:47]\n"
    "ds_read_b64_tr_b16 v[16:17], v2 offset:8192\n"
    "s_waitcnt lgkmcnt(7)\n"
    "v_mfma_f32_16x16x32_bf16 v[32:35], v[168:171], a[104:107], v[32:35]\n"
    "ds_read_b64_tr_b16 v[18:19], v2 offset:9216\n"
    "v_mfma_f32_16x16x32_bf16 v[48:51], v[168:171], a[108:111], v[48:51]\n"
    "ds_read_b64_tr_b16 v[20:21], v3 offset:8192\n"
    "s_waitcnt lgkmcnt(7)\n"
    "v_mfma_f32_16x16x32_bf16 v[52:55], v[172:175], a[108:111], v[52:55]\n"
    "ds_read_b64_tr_b16 v[22:23], v3 offset:9216\n"
    "v_mfma_f32_16x16x32_bf16 v[36:39], v[172:175], a[104:107], v[36:39]\n"
    "ds_read_b64_tr_b16 v[24:25], v4 offset:8192\n"
    "s_waitcnt lgkmcnt(7)\n"
    "v_mfma_f32_16x16x32_bf16 v[40:43], v[176:179], a[104:107], v[40:43]\n"
    "ds_read_b64_tr_b16 v[26:27], v4 offset:9216\n"
    "v_mfma_f32_16x16x32_bf16 v[56:59], v[176:179], a[108:111], v[56:59]\n"
    "ds_read_b64_tr_b16 v[28:29], v5 offset:8192\n"
    "s_waitcnt lgkmcnt(7)\n"
    "v_mfma_f32_16x16x32_bf16 v[60:63], v[180:183], a[108:111], v[60:63]\n"
    "ds_read_b64_tr_b16 v[30:31], v5 offset:9216\n"
    "v_mfma_f32_16x16x32_bf16 v[44:47], v[180:183], a[104:107], v[44:47]\n"
    "ds_read_b64_tr_b16 v[168:169], v2 offset:12288\n"
    "s_waitcnt lgkmcnt(7)\n"
    "v_mfma_f32_16x16x32_bf16 v[32:35], v[16:19], a[112:115], v[32:35]\n"
    "ds_read_b64_tr_b16 v[170:171], v2 offset:13312\n"
    "v_mfma_f32_16x16x32_bf16 v[48:51], v[16:19], a[116:119], v[48:51]\n"
    "ds_read_b64_tr_b16 v[172:173], v3 offset:12288\n"
    "s_waitcnt lgkmcnt(7)\n"
    "v_mfma_f32_16x16x32_bf16 v[52:55], v[20:23], a[116:119], v[52:55]\n"
    "ds_read_b64_tr_b16 v[174:175], v3 offset:13312\n"
    "v_mfma_f32_16x16x32_bf16 v[36:39], v[20:23], a[112:115], v[36:39]\n"
    "ds_read_b64_tr_b16 v[176:177], v4 offset:12288\n"
    "s_waitcnt lgkmcnt(7)\n"
    "v_mfma_f32_16x16x32_bf16 v[40:43], v[24:27], a[112:115], v[40:43]\n"
    "ds_read_b64_tr_b16 v[178:179], v4 offset:13312\n"
    "v_mfma_f32_16x16x32_bf16 v[56:59], v[24:27], a[116:119], v[56:59]\n"
    "ds_read_b64_tr_b16 v[180:181], v5 offset:12288\n"
    "s_waitcnt lgkmcnt(7)\n"
    "v_mfma_f32_16x16x32_bf16 v[60:63], v[28:31], a[116:119], v[60:63]\n"
    "ds_read_b64_tr_b16 v[182:183], v5 offset:13312\n"
    "v_mfma_f32_16x16x32_bf16 v[44:47], v[28:31], a[112:115], v[44:47]\n"
    "s_barrier\n"
    "ds_read_b64_tr_b16 v[16:17], v2 offset:16384\n"
    "s_mov_b32 s74, 0x30000\n"
    "s_add_u32 m0, s81, 0x8000\n"
    "s_nop 0\n"
    "buffer_load_dwordx4 v6, s[52:55], s74 offen lds\n"
    "s_waitcnt lgkmcnt(7)\n"
    "v_mfma_f32_16x16x32_bf16 v[32:35], v[168:171], a[120:123], v[32:35]\n"
    "ds_read_b64_tr_b16 v[18:19], v2 offset:17408\n"
    "v_mfma_f32_16x16x32_bf16 v[48:51], v[168:171], a[124:127], v[48:51]\n"
    "ds_read_b64_tr_b16 v[20:21], v3 offset:16384\n"
    "buffer_load_dwordx4 v7, s[52:55], s74 offen offset:1024 lds\n"
    "s_waitcnt lgkmcnt(7)\n"
    "v_mfma_f32_16x16x32_bf16 v[52:55], v[172:175], a[124:127], v[52:55]\n"
    "ds_read_b64_tr_b16 v[22:23], v3 offset:17408\n"
    "v_mfma_f32_16x16x32_bf16 v[36:39], v[172:175], a[120:123], v[36:39]\n"
    "ds_read_b64_tr_b16 v[24:25], v4 offset:16384\n"
    "s_mov_b32 s74, 0x70000\n"
    "s_add_u32 m0, s81, 0xa000\n"
    "s_nop 0\n"
    "buffer_load_dwordx4 v6, s[52:55], s74 offen lds\n"
    "s_waitcnt lgkmcnt(7)\n"
    "v_mfma_f32_16x16x32_bf16 v[40:43], v[176:179], a[120:123], v[40:43]\n"
    "ds_read_b64_tr_b16 v[26:27], v4 offset:17408\n"
    "v_mfma_f32_16x16x32_bf16 v[56:59], v[176:179], a[124:127], v[56:59]\n"
    "ds_read_b64_tr_b16 v[28:29], v5 offset:16384\n"
    "buffer_load_dwordx4 v7, s[52:55], s74 offen offset:1024 lds\n"
    "s_waitcnt lgkmcnt(7)\n"
    "v_mfma_f32_16x16x32_bf16 v[60:63], v[180:183], a[124:127], v[60:63]\n"
    "ds_read_b64_tr_b16 v[30:31], v5 offset:17408\n"
    "v_mfma_f32_16x16x32_bf16 v[44:47], v[180:183], a[120:123], v[44:47]\n"
    "ds_read_b64_tr_b16 v[168:169], v2 offset:20480\n"
    "s_waitcnt vmcnt(28) lgkmcnt(7)\n"
    "v_mfma_f32_16x16x32_bf16 v[32:35], v[16:19], a[128:131], v[32:35]\n"
    "ds_read_b64_tr_b16 v[170:171], v2 offset:21504\n"
    "s_waitcnt vmcnt(27)\n"
    "v_mfma_f32_16x16x32_bf16 v[48:51], v[16:19], a[132:135], v[48:51]\n"
    "ds_read_b64_tr_b16 v[172:173], v3 offset:20480\n"
    "s_waitcnt lgkmcnt(7)\n"
    "v_mfma_f32_16x16x32_bf16 v[52:55], v[20:23], a[132:135], v[52:55]\n"
    "ds_read_b64_tr_b16 v[174:175], v3 offset:21504\n"
    "v_mfma_f32_16x16x32_bf16 v[36:39], v[20:23], a[128:131], v[36:39]\n"
    "ds_read_b64_tr_b16 v[176:177], v4 offset:20480\n"
    "s_waitcnt lgkmcnt(7)\n"
    "v_mfma_f32_16x16x32_bf16 v[40:43], v[24:27], a[128:131], v[40:43]\n"
    "ds_read_b64_tr_b16 v[178:179], v4 offset:21504\n"
    "v_mfma_f32_16x16x32_bf16 v[56:59], v[24:27], a[132:135], v[56:59]\n"
    "ds_read_b64_tr_b16 v[180:181], v5 offset:20480\n"
    "s_waitcnt lgkmcnt(7)\n"
    "v_mfma_f32_16x16x32_bf16 v[60:63], v[28:31], a[132:135], v[60:63]\n"
    "ds_read_b64_tr_b16 v[182:183], v5 offset:21504\n"
    "v_mfma_f32_16x16x32_bf16 v[44:47], v[28:31], a[128:131], v[44:47]\n"
    "ds_read_b64_tr_b16 v[16:17], v2 offset:24576\n"
    "s_waitcnt vmcnt(26) lgkmcnt(7)\n"
    "v_mfma_f32_16x16x32_bf16 v[32:35], v[168:171], a[136:139], v[32:35]\n"
    "ds_read_b64_tr_b16 v[18:19], v2 offset:25600\n"
    "s_waitcnt vmcnt(25)\n"
    "v_mfma_f32_16x16x32_bf16 v[48:51], v[168:171], a[140:143], v[48:51]\n"
    "ds_read_b64_tr_b16 v[20:21], v3 offset:24576\n"
    "s_waitcnt lgkmcnt(7)\n"
    "v_mfma_f32_16x16x32_bf16 v[52:55], v[172:175], a[140:143], v[52:55]\n"
    "ds_read_b64_tr_b16 v[22:23], v3 offset:25600\n"
    "v_mfma_f32_16x16x32_bf16 v[36:39], v[172:175], a[136:139], v[36:39]\n"
    "ds_read_b64_tr_b16 v[24:25], v4 offset:24576\n"
    "s_waitcnt lgkmcnt(7)\n"
    "v_mfma_f32_16x16x32_bf16 v[40:43], v[176:179], a[136:139], v[40:43]\n"
    "ds_read_b64_tr_b16 v[26:27], v4 offset:25600\n"
    "v_mfma_f32_16x16x32_bf16 v[56:59], v[176:179], a[140:143], v[56:59]\n"
    "ds_read_b64_tr_b16 v[28:29], v5 offset:24576\n"
    "s_waitcnt lgkmcnt(7)\n"
    "v_mfma_f32_16x16x32_bf16 v[60:63], v[180:183], a[140:143], v[60:63]\n"
    "ds_read_b64_tr_b16 v[30:31], v5 offset:25600\n"
    "v_mfma_f32_16x16x32_bf16 v[44:47], v[180:183], a[136:139], v[44:47]\n"
    "ds_read_b64_tr_b16 v[168:169], v2 offset:28672\n"
    "s_waitcnt vmcnt(24) lgkmcnt(7)\n"
    "v_mfma_f32_16x16x32_bf16 v[32:35], v[16:19], a[144:147], v[32:35]\n"
    "ds_read_b64_tr_b16 v[170:171], v2 offset:29696\n"
    "s_waitcnt vmcnt(23)\n"
    "v_mfma_f32_16x16x32_bf16 v[48:51], v[16:19], a[148:151], v[48:51]\n"
    "ds_read_b64_tr_b16 v[172:173], v3 offset:28672\n"
    "s_waitcnt lgkmcnt(7)\n"
    "v_mfma_f32_16x16x32_bf16 v[52:55], v[20:23], a[148:151], v[52:55]\n"
    "ds_read_b64_tr_b16 v[174:175], v3 offset:29696\n"
    "v_mfma_f32_16x16x32_bf16 v[36:39], v[20:23], a[144:147], v[36:39]\n"
    "ds_read_b64_tr_b16 v[176:177], v4 offset:28672\n"
    "s_waitcnt lgkmcnt(7)\n"
    "v_mfma_f32_16x16x32_bf16 v[40:43], v[24:27], a[144:147], v[40:43]\n"
    "ds_read_b64_tr_b16 v[178:179], v4 offset:29696\n"
    "v_mfma_f32_16x16x32_bf16 v[56:59], v[24:27], a[148:151], v[56:59]\n"
    "ds_read_b64_tr_b16 v[180:181], v5 offset:28672\n"
    "s_waitcnt lgkmcnt(7)\n"
    "v_mfma_f32_16x16x32_bf16 v[60:63], v[28:31], a[148:151], v[60:63]\n"
    "ds_read_b64_tr_b16 v[182:183], v5 offset:29696\n"
    "v_mfma_f32_16x16x32_bf16 v[44:47], v[28:31], a[144:147], v[44:47]\n"
    "ds_read_b64_tr_b16 v[16:17], v2 offset:32768\n"
    "s_waitcnt vmcnt(22) lgkmcnt(7)\n"
    "v_mfma_f32_16x16x32_bf16 v[32:35], v[168:171], a[152:155], v[32:35]\n"
    "ds_read_b64_tr_b16 v[18:19], v2 offset:33792\n"
    "s_waitcnt vmcnt(21)\n"
    "v_mfma_f32_16x16x32_bf16 v[48:51], v[168:171], a[156:159], v[48:51]\n"
    "ds_read_b64_tr_b16 v[20:21], v3 offset:32768\n"
    "s_waitcnt lgkmcnt(7)\n"
    "v_mfma_f32_16x16x32_bf16 v[52:55], v[172:175], a[156:159], v[52:55]\n"
    "ds_read_b64_tr_b16 v[22:23], v3 offset:33792\n"
    "v_mfma_f32_16x16x32_bf16 v[36:39], v[172:175], a[152:155], v[36:39]\n"
    "ds_read_b64_tr_b16 v[24:25], v4 offset:32768\n"
    "s_waitcnt lgkmcnt(7)\n"
    "v_mfma_f32_16x16x32_bf16 v[40:43], v[176:179], a[152:155], v[40:43]\n"
    "ds_read_b64_tr_b16 v[26:27], v4 offset:33792\n"
    "v_mfma_f32_16x16x32_bf16 v[56:59], v[176:179], a[156:159], v[56:59]\n"
    "ds_read_b64_tr_b16 v[28:29], v5 offset:32768\n"
    "s_waitcnt lgkmcnt(7)\n"
    "v_mfma_f32_16x16x32_bf16 v[60:63], v[180:183], a[156:159], v[60:63]\n"
    "ds_read_b64_tr_b16 v[30:31], v5 offset:33792\n"
    "v_mfma_f32_16x16x32_bf16 v[44:47], v[180:183], a[152:155], v[44:47]\n"
    "ds_read_b64_tr_b16 v[168:169], v2 offset:36864\n"
    "s_lshl_b32 s30, s75, 4\n"
    "s_lshl_b32 s94, 0x2222, s30\n"
    "s_mov_b32 s95, 0\n"
    "s_waitcnt vmcnt(4)\n"
    "v_cmp_ne_u32 s[88:89], s72, v244\n"
    "v_cmp_ne_u32 s[90:91], s72, v245\n"
    "s_or_b64 s[88:89], s[88:89], s[90:91]\n"
    "s_and_b64 s[88:89], s[88:89], s[94:95]\n"
    "s_cbranch_scc1 LB_slow_17%=\n"
    "LB_back_18%=:\n"
    "s_add_u32 s30, s99, 0\n"
    "s_add_u32 s31, s30, 0x12000\n"
    "buffer_load_dwordx4 a[224:227], v8, s[48:51], s30 offen sc1\n"
    "buffer_load_dwordx4 a[228:231], v8, s[48:51], s31 offen sc1\n"
    "buffer_load_dwordx4 a[232:235], v8, s[48:51], s30 offen offset:1024 sc1\n"
    "buffer_load_dwordx4 a[236:239], v8, s[48:51], s31 offen offset:1024 sc1\n"
    "buffer_load_dwordx4 a[240:243], v8, s[48:51], s30 offen offset:2048 sc1\n"
    "buffer_load_dwordx4 a[244:247], v8, s[48:51], s31 offen offset:2048 sc1\n"
    "buffer_load_dwordx4 a[248:251], v8, s[48:51], s30 offen offset:3072 sc1\n"
    "buffer_load_dwordx4 a[252:255], v8, s[48:51], s31 offen offset:3072 sc1\n"
    "s_add_u32 s30, s99, 0x1000\n"
    "s_add_u32 s31, s30, 0x12000\n"
    "buffer_load_dwordx4 a[0:3], v8, s[48:51], s30 offen sc1\n"
    "buffer_load_dwordx4 a[4:7], v8, s[48:51], s31 offen sc1\n"
    "buffer_load_dwordx4 a[8:11], v8, s[48:51], s30 offen offset:1024 sc1\n"
    "buffer_load_dwordx4 a[12:15], v8, s[48:51], s31 offen offset:1024 sc1\n"
    "buffer_load_dwordx4 a[16:19], v8, s[48:51], s30 offen offset:2048 sc1\n"
    "buffer_load_dwordx4 a[20:23], v8, s[48:51], s31 offen offset:2048 sc1\n"
    "buffer_load_dwordx4 a[24:27], v8, s[48:51], s30 offen offset:3072 sc1\n"
    "buffer_load_dwordx4 a[28:31], v8, s[48:51], s31 offen offset:3072 sc1\n"
    "s_lshl_b32 s30, s78, 1\n"
    "s_add_u32 s30, s30, s77\n"
    "s_lshl_b32 s30, s30, 1\n"
    "s_add_u32 s30, s30, s76\n"
    "s_lshl_b32 s30, s30, 1\n"
    "s_add_u32 s30, s30, s75\n"
    "s_lshl_b32 s31, s30, 7\n"
    "s_add_u32 s31, s31, 0xa50000\n"
    "v_and_b32 v155, 15, v0\n"
    "v_lshlrev_b32 v156, 3, v155\n"
    "buffer_load_dwordx2 v[152:153], v156, s[48:51], s31 offen sc1\n"
    "s_lshl_b32 s30, s30, 14\n"
    "s_lshl_b32 s31, s79, 7\n"
    "s_add_u32 s30, s30, s31\n"
    "s_add_u32 s36, s12, s30\n"
    "s_addc_u32 s37, s13, 0\n"
    "s_sub_u32 s38, 0x200000, s30\n"
    "s_mov_b32 s39, 0x20000\n"
    "s_add_u32 s56, s26, s30\n"
    "s_addc_u32 s57, s27, 0\n"
    "s_sub_u32 s58, 0x200000, s30\n"
    "s_mov_b32 s59, 0x20000\n"
    "v_lshlrev_b32 v155, 10, v155\n"
    "v_bfe_u32 v156, v0, 4, 1\n"
    "v_lshl_add_u32 v155, v156, 5, v155\n"
    "v_lshrrev_b32 v156, 5, v0\n"
    "v_lshl_add_u32 v154, v156, 4, v155\n"
    "buffer_load_dwordx4 v[144:147], v154, s[36:39], 0 offen\n"
    "buffer_load_dwordx4 v[148:151], v154, s[36:39], 0 offen offset:64\n"
    "s_waitcnt lgkmcnt(7)\n"
    "v_mfma_f32_16x16x32_bf16 v[32:35], v[16:19], a[160:163], v[32:35]\n"
    "ds_read_b64_tr_b16 v[170:171], v2 offset:37888\n"
    "v_mfma_f32_16x16x32_bf16 v[48:51], v[16:19], a[164:167], v[48:51]\n"
    "ds_read_b64_tr_b16 v[172:173], v3 offset:36864\n"
    "s_waitcnt lgkmcnt(7)\n"
    "v_mfma_f32_16x16x32_bf16 v[52:55], v[20:23], a[164:167], v[52:55]\n"
    "ds_read_b64_tr_b16 v[174:175], v3 offset:37888\n"
    "v_mfma_f32_16x16x32_bf16 v[36:39], v[20:23], a[160:163], v[36:39]\n"
    "ds_read_b64_tr_b16 v[176:177], v4 offset:36864\n"
    "s_waitcnt lgkmcnt(7)\n"
    "v_mfma_f32_16x16x32_bf16 v[40:43], v[24:27], a[160:163], v[40:43]\n"
    "ds_read_b64_tr_b16 v[178:179], v4 offset:37888\n"
    "v_mfma_f32_16x16x32_bf16 v[56:59], v[24:27], a[164:167], v[56:59]\n"
    "ds_read_b64_tr_b16 v[180:181], v5 offset:36864\n"
    "s_waitcnt lgkmcnt(7)\n"
    "v_mfma_f32_16x16x32_bf16 v[60:63], v[28:31], a[164:167], v[60:63]\n"
    "ds_read_b64_tr_b16 v[182:183], v5 offset:37888\n"
    "v_mfma_f32_16x16x32_bf16 v[44:47], v[28:31], a[160:163], v[44:47]\n"
    "ds_read_b64_tr_b16 v[16:17], v2 offset:40960\n"
    "s_waitcnt lgkmcnt(7)\n"
    "v_mfma_f32_16x16x32_bf16 v[32:35], v[168:171], a[168:171], v[32:35]\n"
    "ds_read_b64_tr_b16 v[18:19], v2 offset:41984\n"
    "v_mfma_f32_16x16x32_bf16 v[48:51], v[168:171], a[172:175], v[48:51]\n"
    "ds_read_b64_tr_b16 v[20:21], v3 offset:40960\n"
    "s_waitcnt lgkmcnt(7)\n"
    "v_mfma_f32_16x16x32_bf16 v[52:55], v[172:175], a[172:175], v[52:55]\n"
    "ds_read_b64_tr_b16 v[22:23], v3 offset:41984\n"
    "v_mfma_f32_16x16x32_bf16 v[36:39], v[172:175], a[168:171], v[36:39]\n"
    "ds_read_b64_tr_b16 v[24:25], v4 offset:40960\n"
    "s_waitcnt lgkmcnt(7)\n"
    "v_mfma_f32_16x16x32_bf16 v[40:43], v[176:179], a[168:171], v[40:43]\n"
    "ds_read_b64_tr_b16 v[26:27], v4 offset:41984\n"
    "v_mfma_f32_16x16x32_bf16 v[56:59], v[176:179], a[172:175], v[56:59]\n"
    "ds_read_b64_tr_b16 v[28:29], v5 offset:40960\n"
    "s_waitcnt lgkmcnt(7)\n"
    "v_mfma_f32_16x16x32_bf16 v[60:63], v[180:183], a[172:175], v[60:63]\n"
    "ds_read_b64_tr_b16 v[30:31], v5 offset:41984\n"
    "v_mfma_f32_16x16x32_bf16 v[44:47], v[180:183], a[168:171], v[44:47]\n"
    "ds_read_b64_tr_b16 v[168:169], v2 offset:45056\n"
    "s_waitcnt lgkmcnt(7)\n"
    "v_mfma_f32_16x16x32_bf16 v[32:35], v[16:19], a[176:179], v[32:35]\n"
    "ds_read_b64_tr_b16 v[170:171], v2 offset:46080\n"
    "v_mfma_f32_16x16x32_bf16 v[48:51], v[16:19], a[180:183], v[48:51]\n"
    "ds_read_b64_tr_b16 v[172:173], v3 offset:45056\n"
    "s_waitcnt lgkmcnt(7)\n"
    "v_mfma_f32_16x16x32_bf16 v[52:55], v[20:23], a[180:183], v[52:55]\n"
    "ds_read_b64_tr_b16 v[174:175], v3 offset:46080\n"
    "v_mfma_f32_16x16x32_bf16 v[36:39], v[20:23], a[176:179], v[36:39]\n"
    "ds_read_b64_tr_b16 v[176:177], v4 offset:45056\n"
    "s_waitcnt lgkmcnt(7)\n"
    "v_mfma_f32_16x16x32_bf16 v[40:43], v[24:27], a[176:179], v[40:43]\n"
    "ds_read_b64_tr_b16 v[178:179], v4 offset:46080\n"
    "v_mfma_f32_16x16x32_bf16 v[56:59], v[24:27], a[180:183], v[56:59]\n"
    "ds_read_b64_tr_b16 v[180:181], v5 offset:45056\n"
    "s_waitcnt lgkmcnt(7)\n"
    "v_mfma_f32_16x16x32_bf16 v[60:63], v[28:31], a[180:183], v[60:63]\n"
    "ds_read_b64_tr_b16 v[182:183], v5 offset:46080\n"
    "v_mfma_f32_16x16x32_bf16 v[44:47], v[28:31], a[176:179], v[44:47]\n"
    "s_barrier\n"
    "ds_read_b64_tr_b16 v[16:17], v2 offset:49152\n"
    "s_waitcnt lgkmcnt(7)\n"
    "v_mfma_f32_16x16x32_bf16 v[32:35], v[168:171], a[184:187], v[32:35]\n"
    "ds_read_b64_tr_b16 v[18:19], v2 offset:50176\n"
    "v_mfma_f32_16x16x32_bf16 v[48:51], v[168:171], a[188:191], v[48:51]\n"
    "ds_read_b64_tr_b16 v[20:21], v3 offset:49152\n"
    "s_waitcnt lgkmcnt(7)\n"
    "v_mfma_f32_16x16x32_bf16 v[52:55], v[172:175], a[188:191], v[52:55]\n"
    "ds_read_b64_tr_b16 v[22:23], v3 offset:50176\n"
    "v_mfma_f32_16x16x32_bf16 v[36:39], v[172:175], a[184:187], v[36:39]\n"
    "ds_read_b64_tr_b16 v[24:25], v4 offset:49152\n"
    "s_waitcnt lgkmcnt(7)\n"
    "v_mfma_f32_16x16x32_bf16 v[40:43], v[176:179], a[184:187], v[40:43]\n"
    "ds_read_b64_tr_b16 v[26:27], v4 offset:50176\n"
    "v_mfma_f32_16x16x32_bf16 v[56:59], v[176:179], a[188:191], v[56:59]\n"
    "ds_read_b64_tr_b16 v[28:29], v5 offset:49152\n"
    "s_waitcnt lgkmcnt(7)\n"
    "v_mfma_f32_16x16x32_bf16 v[60:63], v[180:183], a[188:191], v[60:63]\n"
    "ds_read_b64_tr_b16 v[30:31], v5 offset:50176\n"
    "v_mfma_f32_16x16x32_bf16 v[44:47], v[180:183], a[184:187], v[44:47]\n"
    "ds_read_b64_tr_b16 v[168:169], v2 offset:53248\n"
    "s_waitcnt lgkmcnt(7)\n"
    "v_mfma_f32_16x16x32_bf16 v[32:35], v[16:19], a[192:195], v[32:35]\n"
    "ds_read_b64_tr_b16 v[170:171], v2 offset:54272\n"
    "v_mfma_f32_16x16x32_bf16 v[48:51], v[16:19], a[196:199], v[48:51]\n"
    "ds_read_b64_tr_b16 v[172:173], v3 offset:53248\n"
    "s_waitcnt lgkmcnt(7)\n"
    "v_mfma_f32_16x16x32_bf16 v[52:55], v[20:23], a[196:199], v[52:55]\n"
    "ds_read_b64_tr_b16 v[174:175], v3 offset:54272\n"
    "v_mfma_f32_16x16x32_bf16 v[36:39], v[20:23], a[192:195], v[36:39]\n"
    "ds_read_b64_tr_b16 v[176:177], v4 offset:53248\n"
    "s_waitcnt lgkmcnt(7)\n"
    "v_mfma_f32_16x16x32_bf16 v[40:43], v[24:27], a[192:195], v[40:43]\n"
    "ds_read_b64_tr_b16 v[178:179], v4 offset:54272\n"
    "v_mfma_f32_16x16x32_bf16 v[56:59], v[24:27], a[196:199], v[56:59]\n"
    "ds_read_b64_tr_b16 v[180:181], v5 offset:53248\n"
    "s_waitcnt lgkmcnt(7)\n"
    "v_mfma_f32_16x16x32_bf16 v[60:63], v[28:31], a[196:199], v[60:63]\n"
    "ds_read_b64_tr_b16 v[182:183], v5 offset:54272\n"
    "v_mfma_f32_16x16x32_bf16 v[44:47], v[28:31], a[192:195], v[44:47]\n"
    "ds_read_b64_tr_b16 v[16:17], v2 offset:57344\n"
    "s_waitcnt lgkmcnt(7)\n"
    "v_mfma_f32_16x16x32_bf16 v[32:35], v[168:171], a[200:203], v[32:35]\n"
    "ds_read_b64_tr_b16 v[18:19], v2 offset:58368\n"
    "v_mfma_f32_16x16x32_bf16 v[48:51], v[168:171], a[204:207], v[48:51]\n"
    "ds_read_b64_tr_b16 v[20:21], v3 offset:57344\n"
    "s_waitcnt lgkmcnt(7)\n"
    "v_mfma_f32_16x16x32_bf16 v[52:55], v[172:175], a[204:207], v[52:55]\n"
    "ds_read_b64_tr_b16 v[22:23], v3 offset:58368\n"
    "v_mfma_f32_16x16x32_bf16 v[36:39], v[172:175], a[200:203], v[36:39]\n"
    "ds_read_b64_tr_b16 v[24:25], v4 offset:57344\n"
    "s_waitcnt lgkmcnt(7)\n"
    "v_mfma_f32_16x16x32_bf16 v[40:43], v[176:179], a[200:203], v[40:43]\n"
    "ds_read_b64_tr_b16 v[26:27], v4 offset:58368\n"
    "v_mfma_f32_16x16x32_bf16 v[56:59], v[176:179], a[204:207], v[56:59]\n"
    "ds_read_b64_tr_b16 v[28:29], v5 offset:57344\n"
    "s_waitcnt lgkmcnt(7)\n"
    "v_mfma_f32_16x16x32_bf16 v[60:63], v[180:183], a[204:207], v[60:63]\n"
    "ds_read_b64_tr_b16 v[30:31], v5 offset:58368\n"
    "v_mfma_f32_16x16x32_bf16 v[44:47], v[180:183], a[200:203], v[44:47]\n"
    "ds_read_b64_tr_b16 v[168:169], v2 offset:61440\n"
    "s_waitcnt lgkmcnt(7)\n"
    "v_mfma_f32_16x16x32_bf16 v[32:35], v[16:19], a[208:211], v[32:35]\n"
    "ds_read_b64_tr_b16 v[170:171], v2 offset:62464\n"
    "v_mfma_f32_16x16x32_bf16 v[48:51], v[16:19], a[212:215], v[48:51]\n"
    "ds_read_b64_tr_b16 v[172:173], v3 offset:61440\n"
    "s_waitcnt lgkmcnt(7)\n"
    "v_mfma_f32_16x16x32_bf16 v[52:55], v[20:23], a[212:215], v[52:55]\n"
    "ds_read_b64_tr_b16 v[174:175], v3 offset:62464\n"
    "v_mfma_f32_16x16x32_bf16 v[36:39], v[20:23], a[208:211], v[36:39]\n"
    "ds_read_b64_tr_b16 v[176:177], v4 offset:61440\n"
    "s_waitcnt lgkmcnt(7)\n"
    "v_mfma_f32_16x16x32_bf16 v[40:43], v[24:27], a[208:211], v[40:43]\n"
    "ds_read_b64_tr_b16 v[178:179], v4 offset:62464\n"
    "v_mfma_f32_16x16x32_bf16 v[56:59], v[24:27], a[212:215], v[56:59]\n"
    "ds_read_b64_tr_b16 v[180:181], v5 offset:61440\n"
    "s_waitcnt lgkmcnt(7)\n"
    "v_mfma_f32_16x16x32_bf16 v[60:63], v[28:31], a[212:215], v[60:63]\n"
    "ds_read_b64_tr_b16 v[182:183], v5 offset:62464\n"
    "v_mfma_f32_16x16x32_bf16 v[44:47], v[28:31], a[208:211], v[44:47]\n"
    "s_waitcnt lgkmcnt(6)\n"
    "v_mfma_f32_16x16x32_bf16 v[32:35], v[168:171], a[216:219], v[32:35]\n"
    "v_mfma_f32_16x16x32_bf16 v[48:51], v[168:171], a[220:223], v[48:51]\n"
    "s_waitcnt lgkmcnt(4)\n"
    "v_mfma_f32_16x16x32_bf16 v[52:55], v[172:175], a[220:223], v[52:55]\n"
    "v_mfma_f32_16x16x32_bf16 v[36:39], v[172:175], a[216:219], v[36:39]\n"
    "s_waitcnt lgkmcnt(2)\n"
    "v_mfma_f32_16x16x32_bf16 v[40:43], v[176:179], a[216:219], v[40:43]\n"
    "v_mfma_f32_16x16x32_bf16 v[56:59], v[176:179], a[220:223], v[56:59]\n"
    "s_waitcnt lgkmcnt(0)\n"
    "v_mfma_f32_16x16x32_bf16 v[60:63], v[180:183], a[220:223], v[60:63]\n"
    "v_mfma_f32_16x16x32_bf16 v[44:47], v[180:183], a[216:219], v[44:47]\n"
    "s_cmp_eq_u32 s75, 0\n"
    "s_cbranch_scc1 LB_nosend_19%=\n"
    "s_lshl_b32 s30, s68, 1\n"
    "s_add_u32 s30, s30, s76\n"
    "s_lshl_b32 s30, s30, 13\n"
    "s_add_u32 s30, s30, 0xa58900\n"
    "s_add_u32 s31, s30, 0x1000\n"
    "v_mov_b32 v97, s72\n"
    "v_mov_b32 v99, s72\n"
    "v_mov_b32 v101, s72\n"
    "v_mov_b32 v103, s72\n"
    "v_mov_b32 v105, s72\n"
    "v_mov_b32 v107, s72\n"
    "v_mov_b32 v109, s72\n"
    "v_mov_b32 v111, s72\n"
    "v_mov_b32 v113, s72\n"
    "v_mov_b32 v115, s72\n"
    "v_mov_b32 v117, s72\n"
    "v_mov_b32 v119, s72\n"
    "v_mov_b32 v121, s72\n"
    "v_mov_b32 v123, s72\n"
    "v_mov_b32 v125, s72\n"
    "v_mov_b32 v127, s72\n"
    "v_cvt_pk_bf16_f32 v96, v32, v33\n"
    "v_cvt_pk_bf16_f32 v98, v34, v35\n"
    "buffer_store_dwordx4 v[96:99], v8, s[48:51], s30 offen sc1\n"
    "v_cvt_pk_bf16_f32 v100, v36, v37\n"
    "v_cvt_pk_bf16_f32 v102, v38, v39\n"
    "buffer_store_dwordx4 v[100:103], v8, s[48:51], s30 offen offset:1024 sc1\n"
    "v_cvt_pk_bf16_f32 v104, v40, v41\n"
    "v_cvt_pk_bf16_f32 v106, v42, v43\n"
    "buffer_store_dwordx4 v[104:107], v8, s[48:51], s30 offen offset:2048 sc1\n"
    "v_cvt_pk_bf16_f32 v108, v44, v45\n"
    "v_cvt_pk_bf16_f32 v110, v46, v47\n"
    "buffer_store_dwordx4 v[108:111], v8, s[48:51], s30 offen offset:3072 sc1\n"
    "v_cvt_pk_bf16_f32 v112, v48, v49\n"
    "v_cvt_pk_bf16_f32 v114, v50, v51\n"
    "buffer_store_dwordx4 v[112:115], v8, s[48:51], s31 offen sc1\n"
    "v_cvt_pk_bf16_f32 v116, v52, v53\n"
    "v_cvt_pk_bf16_f32 v118, v54, v55\n"
    "buffer_store_dwordx4 v[116:119], v8, s[48:51], s31 offen offset:1024 sc1\n"
    "v_cvt_pk_bf16_f32 v120, v56, v57\n"
    "v_cvt_pk_bf16_f32 v122, v58, v59\n"
    "buffer_store_dwordx4 v[120:123], v8, s[48:51], s31 offen offset:2048 sc1\n"
    "v_cvt_pk_bf16_f32 v124, v60, v61\n"
    "v_cvt_pk_bf16_f32 v126, v62, v63\n"
    "buffer_store_dwordx4 v[124:127], v8, s[48:51], s31 offen offset:3072 sc1\n"
    "s_nop 1\n"
    "LB_nosend_19%=:\n"
    "s_lshl_b32 s30, s75, 13\n"
    "v_add_u32 v2, s30, v2\n"
    "v_add_u32 v3, s30, v3\n"
    "v_add_u32 v4, s30, v4\n"
    "v_add_u32 v5, s30, v5\n"
    "s_barrier\n"
    "v_add_u32 v2, 0x8000, v2\n"
    "v_add_u32 v3, 0x8000, v3\n"
    "v_add_u32 v4, 0x8000, v4\n"
    "v_add_u32 v5, 0x8000, v5\n"
    "ds_read_b64_tr_b16 v[16:17], v2 offset:32768\n"
    "ds_read_b64_tr_b16 v[18:19], v2 offset:33792\n"
    "ds_read_b64_tr_b16 v[20:21], v3 offset:32768\n"
    "ds_read_b64_tr_b16 v[22:23], v3 offset:33792\n"
    "ds_read_b64_tr_b16 v[24:25], v4 offset:32768\n"
    "ds_read_b64_tr_b16 v[26:27], v4 offset:33792\n"
    "ds_read_b64_tr_b16 v[28:29], v5 offset:32768\n"
    "ds_read_b64_tr_b16 v[30:31], v5 offset:33792\n"
    "ds_read_b64_tr_b16 v[168:169], v2 offset:36864\n"
    "s_waitcnt vmcnt(18) lgkmcnt(7)\n"
    "v_mfma_f32_16x16x32_bf16 v[64:67], v[16:19], a[224:227], 0\n"
    "ds_read_b64_tr_b16 v[170:171], v2 offset:37888\n"
    "s_waitcnt vmcnt(17)\n"
    "v_mfma_f32_16x16x32_bf16 v[80:83], v[16:19], a[228:231], 0\n"
    "ds_read_b64_tr_b16 v[172:173], v3 offset:36864\n"
    "s_waitcnt lgkmcnt(7)\n"
    "v_mfma_f32_16x16x32_bf16 v[84:87], v[20:23], a[228:231], 0\n"
    "ds_read_b64_tr_b16 v[174:175], v3 offset:37888\n"
    "v_mfma_f32_16x16x32_bf16 v[68:71], v[20:23], a[224:227], 0\n"
    "ds_read_b64_tr_b16 v[176:177], v4 offset:36864\n"
    "s_waitcnt lgkmcnt(7)\n"
    "v_mfma_f32_16x16x32_bf16 v[72:75], v[24:27], a[224:227], 0\n"
    "ds_read_b64_tr_b16 v[178:179], v4 offset:37888\n"
    "v_mfma_f32_16x16x32_bf16 v[88:91], v[24:27], a[228:231], 0\n"
    "ds_read_b64_tr_b16 v[180:181], v5 offset:36864\n"
    "s_waitcnt lgkmcnt(7)\n"
    "v_mfma_f32_16x16x32_bf16 v[92:95], v[28:31], a[228:231], 0\n"
    "ds_read_b64_tr_b16 v[182:183], v5 offset:37888\n"
    "v_mfma_f32_16x16x32_bf16 v[76:79], v[28:31], a[224:227], 0\n"
    "v_add_u32 v2, 0xfffe8000, v2\n"
    "v_add_u32 v3, 0xfffe8000, v3\n"
    "v_add_u32 v4, 0xfffe8000, v4\n"
    "v_add_u32 v5, 0xfffe8000, v5\n"
    "ds_read_b64_tr_b16 v[16:17], v2\n"
    "s_waitcnt vmcnt(16) lgkmcnt(7)\n"
    "v_mfma_f32_16x16x32_bf16 v[64:67], v[168:171], a[232:235], v[64:67]\n"
    "ds_read_b64_tr_b16 v[18:19], v2 offset:1024\n"
    "s_waitcnt vmcnt(15)\n"
    "v_mfma_f32_16x16x32_bf16 v[80:83], v[168:171], a[236:239], v[80:83]\n"
    "ds_read_b64_tr_b16 v[20:21], v3\n"
    "s_waitcnt lgkmcnt(7)\n"
    "v_mfma_f32_16x16x32_bf16 v[84:87], v[172:175], a[236:239], v[84:87]\n"
    "ds_read_b64_tr_b16 v[22:23], v3 offset:1024\n"
    "v_mfma_f32_16x16x32_bf16 v[68:71], v[172:175], a[232:235], v[68:71]\n"
    "ds_read_b64_tr_b16 v[24:25], v4\n"
    "s_waitcnt lgkmcnt(7)\n"
    "v_mfma_f32_16x16x32_bf16 v[72:75], v[176:179], a[232:235], v[72:75]\n"
    "ds_read_b64_tr_b16 v[26:27], v4 offset:1024\n"
    "v_mfma_f32_16x16x32_bf16 v[88:91], v[176:179], a[236:239], v[88:91]\n"
    "ds_read_b64_tr_b16 v[28:29], v5\n"
    "s_waitcnt lgkmcnt(7)\n"
    "v_mfma_f32_16x16x32_bf16 v[92:95], v[180:183], a[236:239], v[92:95]\n"
    "ds_read_b64_tr_b16 v[30:31], v5 offset:1024\n"
    "v_mfma_f32_16x16x32_bf16 v[76:79], v[180:183], a[232:235], v[76:79]\n"
    "ds_read_b64_tr_b16 v[168:169], v2 offset:4096\n"
    "s_waitcnt vmcnt(14) lgkmcnt(7)\n"
    "v_mfma_f32_16x16x32_bf16 v[64:67], v[16:19], a[240:243], v[64:67]\n"
    "ds_read_b64_tr_b16 v[170:171], v2 offset:5120\n"
    "s_waitcnt vmcnt(13)\n"
    "v_mfma_f32_16x16x32_bf16 v[80:83], v[16:19], a[244:247], v[80:83]\n"
    "ds_read_b64_tr_b16 v[172:173], v3 offset:4096\n"
    "s_waitcnt lgkmcnt(7)\n"
    "v_mfma_f32_16x16x32_bf16 v[84:87], v[20:23], a[244:247], v[84:87]\n"
    "ds_read_b64_tr_b16 v[174:175], v3 offset:5120\n"
    "v_mfma_f32_16x16x32_bf16 v[68:71], v[20:23], a[240:243], v[68:71]\n"
    "ds_read_b64_tr_b16 v[176:177], v4 offset:4096\n"
    "s_waitcnt lgkmcnt(7)\n"
    "v_mfma_f32_16x16x32_bf16 v[72:75], v[24:27], a[240:243], v[72:75]\n"
    "ds_read_b64_tr_b16 v[178:179], v4 offset:5120\n"
    "v_mfma_f32_16x16x32_bf16 v[88:91], v[24:27], a[244:247], v[88:91]\n"
    "ds_read_b64_tr_b16 v[180:181], v5 offset:4096\n"
    "s_waitcnt lgkmcnt(7)\n"
    "v_mfma_f32_16x16x32_bf16 v[92:95], v[28:31], a[244:247], v[92:95]\n"
    "ds_read_b64_tr_b16 v[182:183], v5 offset:5120\n"
    "v_mfma_f32_16x16x32_bf16 v[76:79], v[28:31], a[240:243], v[76:79]\n"
    "s_barrier\n"
    "ds_read_b64_tr_b16 v[16:17], v2 offset:16384\n"
    "s_waitcnt vmcnt(12) lgkmcnt(7)\n"
    "v_mfma_f32_16x16x32_bf16 v[64:67], v[168:171], a[248:251], v[64:67]\n"
    "ds_read_b64_tr_b16 v[18:19], v2 offset:17408\n"
    "s_waitcnt vmcnt(11)\n"
    "v_mfma_f32_16x16x32_bf16 v[80:83], v[168:171], a[252:255], v[80:83]\n"
    "ds_read_b64_tr_b16 v[20:21], v3 offset:16384\n"
    "s_waitcnt lgkmcnt(7)\n"
    "v_mfma_f32_16x16x32_bf16 v[84:87], v[172:175], a[252:255], v[84:87]\n"
    "ds_read_b64_tr_b16 v[22:23], v3 offset:17408\n"
    "v_mfma_f32_16x16x32_bf16 v[68:71], v[172:175], a[248:251], v[68:71]\n"
    "ds_read_b64_tr_b16 v[24:25], v4 offset:16384\n"
    "s_waitcnt lgkmcnt(7)\n"
    "v_mfma_f32_16x16x32_bf16 v[72:75], v[176:179], a[248:251], v[72:75]\n"
    "ds_read_b64_tr_b16 v[26:27], v4 offset:17408\n"
    "v_mfma_f32_16x16x32_bf16 v[88:91], v[176:179], a[252:255], v[88:91]\n"
    "ds_read_b64_tr_b16 v[28:29], v5 offset:16384\n"
    "s_waitcnt lgkmcnt(7)\n"
    "v_mfma_f32_16x16x32_bf16 v[92:95], v[180:183], a[252:255], v[92:95]\n"
    "ds_read_b64_tr_b16 v[30:31], v5 offset:17408\n"
    "v_mfma_f32_16x16x32_bf16 v[76:79], v[180:183], a[248:251], v[76:79]\n"
    "ds_read_b64_tr_b16 v[168:169], v2 offset:20480\n"
    "s_waitcnt vmcnt(10) lgkmcnt(7)\n"
    "v_mfma_f32_16x16x32_bf16 v[64:67], v[16:19], a[0:3], v[64:67]\n"
    "ds_read_b64_tr_b16 v[170:171], v2 offset:21504\n"
    "s_waitcnt vmcnt(9)\n"
    "v_mfma_f32_16x16x32_bf16 v[80:83], v[16:19], a[4:7], v[80:83]\n"
    "ds_read_b64_tr_b16 v[172:173], v3 offset:20480\n"
    "s_waitcnt lgkmcnt(7)\n"
    "v_mfma_f32_16x16x32_bf16 v[84:87], v[20:23], a[4:7], v[84:87]\n"
    "ds_read_b64_tr_b16 v[174:175], v3 offset:21504\n"
    "v_mfma_f32_16x16x32_bf16 v[68:71], v[20:23], a[0:3], v[68:71]\n"
    "ds_read_b64_tr_b16 v[176:177], v4 offset:20480\n"
    "s_waitcnt lgkmcnt(7)\n"
    "v_mfma_f32_16x16x32_bf16 v[72:75], v[24:27], a[0:3], v[72:75]\n"
    "ds_read_b64_tr_b16 v[178:179], v4 offset:21504\n"
    "v_mfma_f32_16x16x32_bf16 v[88:91], v[24:27], a[4:7], v[88:91]\n"
    "ds_read_b64_tr_b16 v[180:181], v5 offset:20480\n"
    "s_waitcnt lgkmcnt(7)\n"
    "v_mfma_f32_16x16x32_bf16 v[92:95], v[28:31], a[4:7], v[92:95]\n"
    "ds_read_b64_tr_b16 v[182:183], v5 offset:21504\n"
    "v_mfma_f32_16x16x32_bf16 v[76:79], v[28:31], a[0:3], v[76:79]\n"
    "ds_read_b64_tr_b16 v[16:17], v2 offset:32768\n"
    "s_xor_b32 s87, s68, 1\n"
    "s_lshl_b32 s87, s87, 1\n"
    "s_add_u32 s87, s87, s76\n"
    "s_lshl_b32 s87, s87, 1\n"
    "s_add_u32 s87, s87, s75\n"
    "s_lshl_b32 s87, s87, 12\n"
    "s_add_u32 s87, s87, 0xa58900\n"
    "buffer_load_dwordx4 v[128:131], v8, s[48:51], s87 offen sc1\n"
    "buffer_load_dwordx4 v[132:135], v8, s[48:51], s87 offen offset:1024 sc1\n"
    "buffer_load_dwordx4 v[136:139], v8, s[48:51], s87 offen offset:2048 sc1\n"
    "buffer_load_dwordx4 v[140:143], v8, s[48:51], s87 offen offset:3072 sc1\n"
    "s_waitcnt vmcnt(12) lgkmcnt(7)\n"
    "v_mfma_f32_16x16x32_bf16 v[64:67], v[168:171], a[8:11], v[64:67]\n"
    "ds_read_b64_tr_b16 v[18:19], v2 offset:33792\n"
    "s_waitcnt vmcnt(11)\n"
    "v_mfma_f32_16x16x32_bf16 v[80:83], v[168:171], a[12:15], v[80:83]\n"
    "ds_read_b64_tr_b16 v[20:21], v3 offset:32768\n"
    "s_waitcnt lgkmcnt(7)\n"
    "v_mfma_f32_16x16x32_bf16 v[84:87], v[172:175], a[12:15], v[84:87]\n"
    "ds_read_b64_tr_b16 v[22:23], v3 offset:33792\n"
    "v_mfma_f32_16x16x32_bf16 v[68:71], v[172:175], a[8:11], v[68:71]\n"
    "ds_read_b64_tr_b16 v[24:25], v4 offset:32768\n"
    "s_waitcnt lgkmcnt(7)\n"
    "v_mfma_f32_16x16x32_bf16 v[72:75], v[176:179], a[8:11], v[72:75]\n"
    "ds_read_b64_tr_b16 v[26:27], v4 offset:33792\n"
    "v_mfma_f32_16x16x32_bf16 v[88:91], v[176:179], a[12:15], v[88:91]\n"
    "ds_read_b64_tr_b16 v[28:29], v5 offset:32768\n"
    "s_waitcnt lgkmcnt(7)\n"
    "v_mfma_f32_16x16x32_bf16 v[92:95], v[180:183], a[12:15], v[92:95]\n"
    "ds_read_b64_tr_b16 v[30:31], v5 offset:33792\n"
    "v_mfma_f32_16x16x32_bf16 v[76:79], v[180:183], a[8:11], v[76:79]\n"
    "ds_read_b64_tr_b16 v[168:169], v2 offset:36864\n"
    "s_waitcnt vmcnt(10) lgkmcnt(7)\n"
    "v_mfma_f32_16x16x32_bf16 v[64:67], v[16:19], a[16:19], v[64:67]\n"
    "ds_read_b64_tr_b16 v[170:171], v2 offset:37888\n"
    "s_waitcnt vmcnt(9)\n"
    "v_mfma_f32_16x16x32_bf16 v[80:83], v[16:19], a[20:23], v[80:83]\n"
    "ds_read_b64_tr_b16 v[172:173], v3 offset:36864\n"
    "s_waitcnt lgkmcnt(7)\n"
    "v_mfma_f32_16x16x32_bf16 v[84:87], v[20:23], a[20:23], v[84:87]\n"
    "ds_read_b64_tr_b16 v[174:175], v3 offset:37888\n"
    "v_mfma_f32_16x16x32_bf16 v[68:71], v[20:23], a[16:19], v[68:71]\n"
    "ds_read_b64_tr_b16 v[176:177], v4 offset:36864\n"
    "s_waitcnt lgkmcnt(7)\n"
    "v_mfma_f32_16x16x32_bf16 v[72:75], v[24:27], a[16:19], v[72:75]\n"
    "ds_read_b64_tr_b16 v[178:179], v4 offset:37888\n"
    "v_mfma_f32_16x16x32_bf16 v[88:91], v[24:27], a[20:23], v[88:91]\n"
    "ds_read_b64_tr_b16 v[180:181], v5 offset:36864\n"
    "s_waitcnt lgkmcnt(7)\n"
    "v_mfma_f32_16x16x32_bf16 v[92:95], v[28:31], a[20:23], v[92:95]\n"
    "ds_read_b64_tr_b16 v[182:183], v5 offset:37888\n"
    "v_mfma_f32_16x16x32_bf16 v[76:79], v[28:31], a[16:19], v[76:79]\n"
    "s_waitcnt vmcnt(8) lgkmcnt(6)\n"
    "v_mfma_f32_16x16x32_bf16 v[64:67], v[168:171], a[24:27], v[64:67]\n"
    "s_waitcnt vmcnt(7)\n"
    "v_mfma_f32_16x16x32_bf16 v[80:83], v[168:171], a[28:31], v[80:83]\n"
    "s_waitcnt lgkmcnt(4)\n"
    "v_mfma_f32_16x16x32_bf16 v[84:87], v[172:175], a[28:31], v[84:87]\n"
    "v_mfma_f32_16x16x32_bf16 v[68:71], v[172:175], a[24:27], v[68:71]\n"
    "s_waitcnt lgkmcnt(2)\n"
    "v_mfma_f32_16x16x32_bf16 v[72:75], v[176:179], a[24:27], v[72:75]\n"
    "v_mfma_f32_16x16x32_bf16 v[88:91], v[176:179], a[28:31], v[88:91]\n"
    "s_waitcnt lgkmcnt(0)\n"
    "v_mfma_f32_16x16x32_bf16 v[92:95], v[180:183], a[28:31], v[92:95]\n"
    "v_mfma_f32_16x16x32_bf16 v[76:79], v[180:183], a[24:27], v[76:79]\n"
    "s_mul_i32 s30, s76, 0x3000\n"
    "s_add_u32 s30, s30, 0xc000\n"
    "v_add_u32 v192, s30, v8\n"
    "s_nop 11\n"
    "s_cmp_lg_u32 s75, 0\n"
    "s_cbranch_scc1 LB_else_20%=\n"
    "ds_write_b128 v192, v[48:51]\n"
    "ds_write_b128 v192, v[52:55] offset:1024\n"
    "ds_write_b128 v192, v[56:59] offset:2048\n"
    "ds_write_b128 v192, v[60:63] offset:3072\n"
    "ds_write_b128 v192, v[80:83] offset:4096\n"
    "ds_write_b128 v192, v[84:87] offset:5120\n"
    "ds_write_b128 v192, v[88:91] offset:6144\n"
    "ds_write_b128 v192, v[92:95] offset:7168\n"
    "s_waitcnt lgkmcnt(0)\n"
    "s_barrier\n"
    "ds_read_b128 v[80:83], v192 offset:8192\n"
    "ds_read_b128 v[84:87], v192 offset:9216\n"
    "ds_read_b128 v[88:91], v192 offset:10240\n"
    "ds_read_b128 v[92:95], v192 offset:11264\n"
    "s_branch LB_join_21%=\n"
    "LB_else_20%=:\n"
    "ds_write_b128 v192, v[64:67] offset:8192\n"
    "ds_write_b128 v192, v[68:71] offset:9216\n"
    "ds_write_b128 v192, v[72:75] offset:10240\n"
    "ds_write_b128 v192, v[76:79] offset:11264\n"
    "s_waitcnt lgkmcnt(0)\n"
    "s_barrier\n"
    "ds_read_b128 v[32:35], v192\n"
    "ds_read_b128 v[36:39], v192 offset:1024\n"
    "ds_read_b128 v[40:43], v192 offset:2048\n"
    "ds_read_b128 v[44:47], v192 offset:3072\n"
    "ds_read_b128 v[64:67], v192 offset:4096\n"
    "ds_read_b128 v[68:71], v192 offset:5120\n"
    "ds_read_b128 v[72:75], v192 offset:6144\n"
    "ds_read_b128 v[76:79], v192 offset:7168\n"
    "LB_join_21%=:\n"
    "s_waitcnt vmcnt(6)\n"
    "v_and_b32 v193, 0xffff0000, v152\n"
    "v_rcp_f32 v193, v193\n"
    "s_waitcnt vmcnt(5)\n"
    "v_permlane16_swap_b32 v144, v146\n"
    "v_mul_f32 v193, v193, v193\n"
    "v_mul_f32 v194, 0xbfb8aa3b, v193\n"
    "v_permlane16_swap_b32 v145, v147\n"
    "s_waitcnt vmcnt(4)\n"
    "v_permlane16_swap_b32 v148, v150\n"
    "v_permlane16_swap_b32 v149, v151\n"
    "s_waitcnt vmcnt(3)\n"
    "v_xor_b32 v195, s72, v129\n"
    "v_bitop3_b32 v195, v195, v131, s72 bitop3:0xf6\n"
    "s_waitcnt vmcnt(2)\n"
    "v_bitop3_b32 v195, v195, v133, s72 bitop3:0xf6\n"
    "v_bitop3_b32 v195, v195, v135, s72 bitop3:0xf6\n"
    "s_waitcnt vmcnt(1)\n"
    "v_bitop3_b32 v195, v195, v137, s72 bitop3:0xf6\n"
    "v_bitop3_b32 v195, v195, v139, s72 bitop3:0xf6\n"
    "s_waitcnt vmcnt(0)\n"
    "v_bitop3_b32 v195, v195, v141, s72 bitop3:0xf6\n"
    "v_bitop3_b32 v195, v195, v143, s72 bitop3:0xf6\n"
    "v_cmp_ne_u32 s[88:89], 0, v195\n"
    "s_cmp_lg_u64 s[88:89], 0\n"
    "s_cbranch_scc1 LB_slow_22%=\n"
    "LB_back_23%=:\n"
    "s_waitcnt lgkmcnt(0)\n"
    "v_lshlrev_b32 v196, 16, v128\n"
    "v_add_f32 v32, v32, v196\n"
    "v_add_f32 v64, v64, v80\n"
    "v_mul_f32 v196, v194, v32\n"
    "v_exp_f32 v196, v196\n"
    "v_lshlrev_b32 v197, 16, v144\n"
    "v_mul_f32 v64, v64, v32\n"
    "v_add_f32 v196, 1.0, v196\n"
    "v_rcp_f32 v196, v196\n"
    "v_mul_f32 v64, v64, v193\n"
    "v_fma_f32 v32, v64, v196, v197\n"
    "v_and_b32 v198, 0xffff0000, v128\n"
    "v_add_f32 v33, v33, v198\n"
    "v_add_f32 v65, v65, v81\n"
    "v_mul_f32 v198, v194, v33\n"
    "v_exp_f32 v198, v198\n"
    "v_and_b32 v199, 0xffff0000, v144\n"
    "v_mul_f32 v65, v65, v33\n"
    "v_add_f32 v198, 1.0, v198\n"
    "v_rcp_f32 v198, v198\n"
    "v_mul_f32 v65, v65, v193\n"
    "v_fma_f32 v33, v65, v198, v199\n"
    "v_lshlrev_b32 v200, 16, v130\n"
    "v_add_f32 v34, v34, v200\n"
    "v_add_f32 v66, v66, v82\n"
    "v_mul_f32 v200, v194, v34\n"
    "v_exp_f32 v200, v200\n"
    "v_lshlrev_b32 v201, 16, v145\n"
    "v_mul_f32 v66, v66, v34\n"
    "v_add_f32 v200, 1.0, v200\n"
    "v_rcp_f32 v200, v200\n"
    "v_mul_f32 v66, v66, v193\n"
    "v_fma_f32 v34, v66, v200, v201\n"
    "v_and_b32 v202, 0xffff0000, v130\n"
    "v_add_f32 v35, v35, v202\n"
    "v_add_f32 v67, v67, v83\n"
    "v_mul_f32 v202, v194, v35\n"
    "v_exp_f32 v202, v202\n"
    "v_and_b32 v203, 0xffff0000, v145\n"
    "v_mul_f32 v67, v67, v35\n"
    "v_add_f32 v202, 1.0, v202\n"
    "v_rcp_f32 v202, v202\n"
    "v_mul_f32 v67, v67, v193\n"
    "v_fma_f32 v35, v67, v202, v203\n"
    "v_lshlrev_b32 v204, 16, v132\n"
    "v_add_f32 v36, v36, v204\n"
    "v_add_f32 v68, v68, v84\n"
    "v_mul_f32 v204, v194, v36\n"
    "v_exp_f32 v204, v204\n"
    "v_lshlrev_b32 v205, 16, v146\n"
    "v_mul_f32 v68, v68, v36\n"
    "v_add_f32 v204, 1.0, v204\n"
    "v_rcp_f32 v204, v204\n"
    "v_mul_f32 v68, v68, v193\n"
    "v_fma_f32 v36, v68, v204, v205\n"
    "v_and_b32 v206, 0xffff0000, v132\n"
    "v_add_f32 v37, v37, v206\n"
    "v_add_f32 v69, v69, v85\n"
    "v_mul_f32 v206, v194, v37\n"
    "v_exp_f32 v206, v206\n"
    "v_and_b32 v207, 0xffff0000, v146\n"
    "v_mul_f32 v69, v69, v37\n"
    "v_add_f32 v206, 1.0, v206\n"
    "v_rcp_f32 v206, v206\n"
    "v_mul_f32 v69, v69, v193\n"
    "v_fma_f32 v37, v69, v206, v207\n"
    "v_lshlrev_b32 v208, 16, v134\n"
    "v_add_f32 v38, v38, v208\n"
    "v_add_f32 v70, v70, v86\n"
    "v_mul_f32 v208, v194, v38\n"
    "v_exp_f32 v208, v208\n"
    "v_lshlrev_b32 v209, 16, v147\n"
    "v_mul_f32 v70, v70, v38\n"
    "v_add_f32 v208, 1.0, v208\n"
    "v_rcp_f32 v208, v208\n"
    "v_mul_f32 v70, v70, v193\n"
    "v_fma_f32 v38, v70, v208, v209\n"
    "v_and_b32 v210, 0xffff0000, v134\n"
    "v_add_f32 v39, v39, v210\n"
    "v_add_f32 v71, v71, v87\n"
    "v_mul_f32 v210, v194, v39\n"
    "v_exp_f32 v210, v210\n"
    "v_and_b32 v211, 0xffff0000, v147\n"
    "v_mul_f32 v71, v71, v39\n"
    "v_add_f32 v210, 1.0, v210\n"
    "v_rcp_f32 v210, v210\n"
    "v_mul_f32 v71, v71, v193\n"
    "v_fma_f32 v39, v71, v210, v211\n"
    "v_lshlrev_b32 v212, 16, v136\n"
    "v_add_f32 v40, v40, v212\n"
    "v_add_f32 v72, v72, v88\n"
    "v_mul_f32 v212, v194, v40\n"
    "v_exp_f32 v212, v212\n"
    "v_lshlrev_b32 v213, 16, v148\n"
    "v_mul_f32 v72, v72, v40\n"
    "v_add_f32 v212, 1.0, v212\n"
    "v_rcp_f32 v212, v212\n"
    "v_mul_f32 v72, v72, v193\n"
    "v_fma_f32 v40, v72, v212, v213\n"
    "v_and_b32 v214, 0xffff0000, v136\n"
    "v_add_f32 v41, v41, v214\n"
    "v_add_f32 v73, v73, v89\n"
    "v_mul_f32 v214, v194, v41\n"
    "v_exp_f32 v214, v214\n"
    "v_and_b32 v215, 0xffff0000, v148\n"
    "v_mul_f32 v73, v73, v41\n"
    "v_add_f32 v214, 1.0, v214\n"
    "v_rcp_f32 v214, v214\n"
    "v_mul_f32 v73, v73, v193\n"
    "v_fma_f32 v41, v73, v214, v215\n"
    "v_lshlrev_b32 v216, 16, v138\n"
    "v_add_f32 v42, v42, v216\n"
    "v_add_f32 v74, v74, v90\n"
    "v_mul_f32 v216, v194, v42\n"
    "v_exp_f32 v216, v216\n"
    "v_lshlrev_b32 v217, 16, v149\n"
    "v_mul_f32 v74, v74, v42\n"
    "v_add_f32 v216, 1.0, v216\n"
    "v_rcp_f32 v216, v216\n"
    "v_mul_f32 v74, v74, v193\n"
    "v_fma_f32 v42, v74, v216, v217\n"
    "v_and_b32 v218, 0xffff0000, v138\n"
    "v_add_f32 v43, v43, v218\n"
    "v_add_f32 v75, v75, v91\n"
    "v_mul_f32 v218, v194, v43\n"
    "v_exp_f32 v218, v218\n"
    "v_and_b32 v219, 0xffff0000, v149\n"
    "v_mul_f32 v75, v75, v43\n"
    "v_add_f32 v218, 1.0, v218\n"
    "v_rcp_f32 v218, v218\n"
    "v_mul_f32 v75, v75, v193\n"
    "v_fma_f32 v43, v75, v218, v219\n"
    "v_lshlrev_b32 v220, 16, v140\n"
    "v_add_f32 v44, v44, v220\n"
    "v_add_f32 v76, v76, v92\n"
    "v_mul_f32 v220, v194, v44\n"
    "v_exp_f32 v220, v220\n"
    "v_lshlrev_b32 v221, 16, v150\n"
    "v_mul_f32 v76, v76, v44\n"
    "v_add_f32 v220, 1.0, v220\n"
    "v_rcp_f32 v220, v220\n"
    "v_mul_f32 v76, v76, v193\n"
    "v_fma_f32 v44, v76, v220, v221\n"
    "v_and_b32 v222, 0xffff0000, v140\n"
    "v_add_f32 v45, v45, v222\n"
    "v_add_f32 v77, v77, v93\n"
    "v_mul_f32 v222, v194, v45\n"
    "v_exp_f32 v222, v222\n"
    "v_and_b32 v223, 0xffff0000, v150\n"
    "v_mul_f32 v77, v77, v45\n"
    "v_add_f32 v222, 1.0, v222\n"
    "v_rcp_f32 v222, v222\n"
    "v_mul_f32 v77, v77, v193\n"
    "v_fma_f32 v45, v77, v222, v223\n"
    "v_lshlrev_b32 v224, 16, v142\n"
    "v_add_f32 v46, v46, v224\n"
    "v_add_f32 v78, v78, v94\n"
    "v_mul_f32 v224, v194, v46\n"
    "v_exp_f32 v224, v224\n"
    "v_lshlrev_b32 v225, 16, v151\n"
    "v_mul_f32 v78, v78, v46\n"
    "v_add_f32 v224, 1.0, v224\n"
    "v_rcp_f32 v224, v224\n"
    "v_mul_f32 v78, v78, v193\n"
    "v_fma_f32 v46, v78, v224, v225\n"
    "v_and_b32 v226, 0xffff0000, v142\n"
    "v_add_f32 v47, v47, v226\n"
    "v_add_f32 v79, v79, v95\n"
    "v_mul_f32 v226, v194, v47\n"
    "v_exp_f32 v226, v226\n"
    "v_and_b32 v227, 0xffff0000, v151\n"
    "v_mul_f32 v79, v79, v47\n"
    "v_add_f32 v226, 1.0, v226\n"
    "v_rcp_f32 v226, v226\n"
    "v_mul_f32 v79, v79, v193\n"
    "v_fma_f32 v47, v79, v226, v227\n"
    "v_cvt_pk_bf16_f32 v228, v32, v33\n"
    "v_cvt_pk_bf16_f32 v229, v34, v35\n"
    "v_cvt_pk_bf16_f32 v230, v36, v37\n"
    "v_cvt_pk_bf16_f32 v231, v38, v39\n"
    "v_cvt_pk_bf16_f32 v232, v40, v41\n"
    "v_permlane16_swap_b32 v228, v230\n"
    "v_permlane16_swap_b32 v229, v231\n"
    "buffer_store_dwordx4 v[228:231], v154, s[56:59], 0 offen\n"
    "v_cvt_pk_bf16_f32 v233, v42, v43\n"
    "v_cvt_pk_bf16_f32 v234, v44, v45\n"
    "v_cvt_pk_bf16_f32 v235, v46, v47\n"
    "s_nop 0\n"
    "v_permlane16_swap_b32 v232, v234\n"
    "v_permlane16_swap_b32 v233, v235\n"
    "buffer_store_dwordx4 v[232:235], v154, s[56:59], 0 offen offset:64\n"
    "s_or_b32 s30, s68, s69\n"
    "s_cbranch_scc1 LB_nogen_24%=\n"
    "v_mov_b32 v249, 0\n"
    "v_mov_b32 v250, s72\n"
    "s_mov_b32 s31, 0xa58800\n"
    "buffer_store_dword v250, v249, s[48:51], s31 offen sc1\n"
    "LB_nogen_24%=:\n"
    "s_branch L_end%=\n"
    "LA_slow_1%=:\n"
    "s_sub_u32 s73, s73, 1\n"
    "s_cbranch_scc1 L_end%=\n"
    "s_sleep 1\n"
    "buffer_load_dwordx2 v[192:193], v196, s[48:51], s34 offen sc1\n"
    "buffer_load_dwordx2 v[194:195], v196, s[48:51], s34 offen offset:256 sc1\n"
    "s_waitcnt vmcnt(0)\n"
    "v_xor_b32 v197, s72, v193\n"
    "v_bitop3_b32 v197, v197, v195, s72 bitop3:0xf6\n"
    "v_cmp_ne_u32 s[88:89], 0, v197\n"
    "s_cmp_lg_u64 s[88:89], 0\n"
    "s_cbranch_scc1 LA_slow_1%=\n"
    "s_branch LA_back_2%=\n"
    "LA_slow_3%=:\n"
    "s_sub_u32 s73, s73, 1\n"
    "s_cbranch_scc1 L_end%=\n"
    "s_sleep 1\n"
    "buffer_load_dwordx2 v[168:169], v15, s[48:51], s34 offen sc1\n"
    "buffer_load_dwordx2 v[170:171], v15, s[48:51], s34 offen offset:256 sc1\n"
    "buffer_load_dwordx2 v[172:173], v15, s[48:51], s34 offen offset:512 sc1\n"
    "buffer_load_dwordx2 v[174:175], v15, s[48:51], s34 offen offset:768 sc1\n"
    "buffer_load_dwordx2 v[176:177], v15, s[48:51], s34 offen offset:1024 sc1\n"
    "buffer_load_dwordx2 v[178:179], v15, s[48:51], s34 offen offset:1280 sc1\n"
    "buffer_load_dwordx2 v[180:181], v15, s[48:51], s34 offen offset:1536 sc1\n"
    "buffer_load_dwordx2 v[182:183], v15, s[48:51], s34 offen offset:1792 sc1\n"
    "s_waitcnt vmcnt(0)\n"
    "v_xor_b32 v185, s72, v169\n"
    "v_bitop3_b32 v185, v185, v171, s72 bitop3:0xf6\n"
    "v_bitop3_b32 v185, v185, v173, s72 bitop3:0xf6\n"
    "v_bitop3_b32 v185, v185, v175, s72 bitop3:0xf6\n"
    "v_bitop3_b32 v185, v185, v177, s72 bitop3:0xf6\n"
    "v_bitop3_b32 v185, v185, v179, s72 bitop3:0xf6\n"
    "v_bitop3_b32 v185, v185, v181, s72 bitop3:0xf6\n"
    "v_bitop3_b32 v185, v185, v183, s72 bitop3:0xf6\n"
    "v_cmp_ne_u32 s[88:89], 0, v185\n"
    "s_cmp_lg_u64 s[88:89], 0\n"
    "s_cbranch_scc1 LA_slow_3%=\n"
    "s_branch LA_back_4%=\n"
    "LA_slow_5%=:\n"
    "s_sub_u32 s73, s73, 1\n"
    "s_cbranch_scc1 L_end%=\n"
    "s_sleep 1\n"
    "buffer_load_dwordx2 v[168:169], v15, s[48:51], s34 offen sc1\n"
    "buffer_load_dwordx2 v[170:171], v15, s[48:51], s34 offen offset:256 sc1\n"
    "buffer_load_dwordx2 v[172:173], v15, s[48:51], s34 offen offset:512 sc1\n"
    "buffer_load_dwordx2 v[174:175], v15, s[48:51], s34 offen offset:768 sc1\n"
    "buffer_load_dwordx2 v[176:177], v15, s[48:51], s34 offen offset:1024 sc1\n"
    "buffer_load_dwordx2 v[178:179], v15, s[48:51], s34 offen offset:1280 sc1\n"
    "buffer_load_dwordx2 v[180:181], v15, s[48:51], s34 offen offset:1536 sc1\n"
    "buffer_load_dwordx2 v[182:183], v15, s[48:51], s34 offen offset:1792 sc1\n"
    "s_waitcnt vmcnt(0)\n"
    "v_xor_b32 v185, s72, v169\n"
    "v_bitop3_b32 v185, v185, v171, s72 bitop3:0xf6\n"
    "v_bitop3_b32 v185, v185, v173, s72 bitop3:0xf6\n"
    "v_bitop3_b32 v185, v185, v175, s72 bitop3:0xf6\n"
    "v_bitop3_b32 v185, v185, v177, s72 bitop3:0xf6\n"
    "v_bitop3_b32 v185, v185, v179, s72 bitop3:0xf6\n"
    "v_bitop3_b32 v185, v185, v181, s72 bitop3:0xf6\n"
    "v_bitop3_b32 v185, v185, v183, s72 bitop3:0xf6\n"
    "v_cmp_ne_u32 s[88:89], 0, v185\n"
    "s_cmp_lg_u64 s[88:89], 0\n"
    "s_cbranch_scc1 LA_slow_5%=\n"
    "s_branch LA_back_6%=\n"
    "LA_slow_7%=:\n"
    "s_sub_u32 s73, s73, 1\n"
    "s_cbranch_scc1 L_end%=\n"
    "s_sleep 1\n"
    "buffer_load_dwordx2 v[168:169], v15, s[48:51], s34 offen sc1\n"
    "buffer_load_dwordx2 v[170:171], v15, s[48:51], s34 offen offset:256 sc1\n"
    "buffer_load_dwordx2 v[172:173], v15, s[48:51], s34 offen offset:512 sc1\n"
    "buffer_load_dwordx2 v[174:175], v15, s[48:51], s34 offen offset:768 sc1\n"
    "buffer_load_dwordx2 v[176:177], v15, s[48:51], s34 offen offset:1024 sc1\n"
    "buffer_load_dwordx2 v[178:179], v15, s[48:51], s34 offen offset:1280 sc1\n"
    "buffer_load_dwordx2 v[180:181], v15, s[48:51], s34 offen offset:1536 sc1\n"
    "buffer_load_dwordx2 v[182:183], v15, s[48:51], s34 offen offset:1792 sc1\n"
    "s_waitcnt vmcnt(0)\n"
    "v_xor_b32 v185, s72, v169\n"
    "v_bitop3_b32 v185, v185, v171, s72 bitop3:0xf6\n"
    "v_bitop3_b32 v185, v185, v173, s72 bitop3:0xf6\n"
    "v_bitop3_b32 v185, v185, v175, s72 bitop3:0xf6\n"
    "v_bitop3_b32 v185, v185, v177, s72 bitop3:0xf6\n"
    "v_bitop3_b32 v185, v185, v179, s72 bitop3:0xf6\n"
    "v_bitop3_b32 v185, v185, v181, s72 bitop3:0xf6\n"
    "v_bitop3_b32 v185, v185, v183, s72 bitop3:0xf6\n"
    "v_cmp_ne_u32 s[88:89], 0, v185\n"
    "s_cmp_lg_u64 s[88:89], 0\n"
    "s_cbranch_scc1 LA_slow_7%=\n"
    "s_branch LA_back_8%=\n"
    "LA_slow_9%=:\n"
    "s_sub_u32 s73, s73, 1\n"
    "s_cbranch_scc1 L_end%=\n"
    "s_sleep 1\n"
    "buffer_load_dword v247, v248, s[48:51], s86 offen sc1\n"
    "s_waitcnt vmcnt(0)\n"
    "v_cmp_ne_u32 s[88:89], s72, v247\n"
    "s_cmp_lg_u64 s[88:89], 0\n"
    "s_cbranch_scc1 LA_slow_9%=\n"
    "s_branch LA_back_10%=\n"
    "LA_slow_11%=:\n"
    "s_sub_u32 s73, s73, 1\n"
    "s_cbranch_scc1 L_end%=\n"
    "s_sleep 1\n"
    "buffer_load_dwordx2 v[244:245], v246, s[48:51], s8 offen sc1\n"
    "s_waitcnt vmcnt(0)\n"
    "v_cmp_ne_u32 s[88:89], s72, v244\n"
    "v_cmp_ne_u32 s[90:91], s72, v245\n"
    "s_or_b64 s[88:89], s[88:89], s[90:91]\n"
    "s_and_b64 s[88:89], s[88:89], s[94:95]\n"
    "s_cbranch_scc1 LA_slow_11%=\n"
    "s_branch LA_back_12%=\n"
    "LA_slow_13%=:\n"
    "s_sub_u32 s73, s73, 1\n"
    "s_cbranch_scc1 L_end%=\n"
    "s_sleep 1\n"
    "buffer_load_dwordx2 v[244:245], v246, s[48:51], s8 offen sc1\n"
    "s_waitcnt vmcnt(0)\n"
    "v_cmp_ne_u32 s[88:89], s72, v244\n"
    "v_cmp_ne_u32 s[90:91], s72, v245\n"
    "s_or_b64 s[88:89], s[88:89], s[90:91]\n"
    "s_and_b64 s[88:89], s[88:89], s[94:95]\n"
    "s_cbranch_scc1 LA_slow_13%=\n"
    "s_branch LA_back_14%=\n"
    "LA_slow_15%=:\n"
    "s_sub_u32 s73, s73, 1\n"
    "s_cbranch_scc1 L_end%=\n"
    "s_sleep 1\n"
    "buffer_load_dwordx2 v[244:245], v246, s[48:51], s8 offen sc1\n"
    "s_waitcnt vmcnt(0)\n"
    "v_cmp_ne_u32 s[88:89], s72, v244\n"
    "v_cmp_ne_u32 s[90:91], s72, v245\n"
    "s_or_b64 s[88:89], s[88:89], s[90:91]\n"
    "s_and_b64 s[88:89], s[88:89], s[94:95]\n"
    "s_cbranch_scc1 LA_slow_15%=\n"
    "s_branch LA_back_16%=\n"
    "LA_slow_17%=:\n"
    "s_sub_u32 s73, s73, 1\n"
    "s_cbranch_scc1 L_end%=\n"
    "s_sleep 1\n"
    "buffer_load_dwordx2 v[244:245], v246, s[48:51], s8 offen sc1\n"
    "s_waitcnt vmcnt(0)\n"
    "v_cmp_ne_u32 s[88:89], s72, v244\n"
    "v_cmp_ne_u32 s[90:91], s72, v245\n"
    "s_or_b64 s[88:89], s[88:89], s[90:91]\n"
    "s_and_b64 s[88:89], s[88:89], s[94:95]\n"
    "s_cbranch_scc1 LA_slow_17%=\n"
    "s_branch LA_back_18%=\n"
    "LA_slow_19%=:\n"
    "s_sub_u32 s73, s73, 1\n"
    "s_cbranch_scc1 L_end%=\n"
    "s_sleep 1\n"
    "buffer_load_dwordx2 v[244:245], v246, s[48:51], s9 offen sc1\n"
    "s_waitcnt vmcnt(0)\n"
    "v_cmp_ne_u32 s[88:89], s72, v244\n"
    "v_cmp_ne_u32 s[90:91], s72, v245\n"
    "s_or_b64 s[88:89], s[88:89], s[90:91]\n"
    "s_and_b64 s[88:89], s[88:89], s[94:95]\n"
    "s_cbranch_scc1 LA_slow_19%=\n"
    "s_branch LA_back_20%=\n"
    "LA_slow_24%=:\n"
    "s_sub_u32 s73, s73, 1\n"
    "s_cbranch_scc1 L_end%=\n"
    "s_sleep 1\n"
    "buffer_load_dwordx4 v[128:131], v8, s[48:51], s87 offen sc1\n"
    "buffer_load_dwordx4 v[132:135], v8, s[48:51], s87 offen offset:1024 sc1\n"
    "buffer_load_dwordx4 v[136:139], v8, s[48:51], s87 offen offset:2048 sc1\n"
    "buffer_load_dwordx4 v[140:143], v8, s[48:51], s87 offen offset:3072 sc1\n"
    "s_waitcnt vmcnt(0)\n"
    "v_xor_b32 v195, s72, v129\n"
    "v_bitop3_b32 v195, v195, v131, s72 bitop3:0xf6\n"
    "v_bitop3_b32 v195, v195, v133, s72 bitop3:0xf6\n"
    "v_bitop3_b32 v195, v195, v135, s72 bitop3:0xf6\n"
    "v_bitop3_b32 v195, v195, v137, s72 bitop3:0xf6\n"
    "v_bitop3_b32 v195, v195, v139, s72 bitop3:0xf6\n"
    "v_bitop3_b32 v195, v195, v141, s72 bitop3:0xf6\n"
    "v_bitop3_b32 v195, v195, v143, s72 bitop3:0xf6\n"
    "v_cmp_ne_u32 s[88:89], 0, v195\n"
    "s_cmp_lg_u64 s[88:89], 0\n"
    "s_cbranch_scc1 LA_slow_24%=\n"
    "s_branch LA_back_25%=\n"
    "LB_slow_1%=:\n"
    "s_sub_u32 s73, s73, 1\n"
    "s_cbranch_scc1 L_end%=\n"
    "s_sleep 1\n"
    "buffer_load_dwordx2 v[192:193], v196, s[48:51], s34 offen sc1\n"
    "buffer_load_dwordx2 v[194:195], v196, s[48:51], s34 offen offset:256 sc1\n"
    "s_waitcnt vmcnt(0)\n"
    "v_xor_b32 v197, s72, v193\n"
    "v_bitop3_b32 v197, v197, v195, s72 bitop3:0xf6\n"
    "v_cmp_ne_u32 s[88:89], 0, v197\n"
    "s_cmp_lg_u64 s[88:89], 0\n"
    "s_cbranch_scc1 LB_slow_1%=\n"
    "s_branch LB_back_2%=\n"
    "LB_slow_3%=:\n"
    "s_sub_u32 s73, s73, 1\n"
    "s_cbranch_scc1 L_end%=\n"
    "s_sleep 1\n"
    "buffer_load_dwordx2 v[168:169], v15, s[48:51], s34 offen sc1\n"
    "buffer_load_dwordx2 v[170:171], v15, s[48:51], s34 offen offset:256 sc1\n"
    "buffer_load_dwordx2 v[172:173], v15, s[48:51], s34 offen offset:512 sc1\n"
    "buffer_load_dwordx2 v[174:175], v15, s[48:51], s34 offen offset:768 sc1\n"
    "buffer_load_dwordx2 v[176:177], v15, s[48:51], s34 offen offset:1024 sc1\n"
    "buffer_load_dwordx2 v[178:179], v15, s[48:51], s34 offen offset:1280 sc1\n"
    "buffer_load_dwordx2 v[180:181], v15, s[48:51], s34 offen offset:1536 sc1\n"
    "buffer_load_dwordx2 v[182:183], v15, s[48:51], s34 offen offset:1792 sc1\n"
    "s_waitcnt vmcnt(0)\n"
    "v_xor_b32 v185, s72, v169\n"
    "v_bitop3_b32 v185, v185, v171, s72 bitop3:0xf6\n"
    "v_bitop3_b32 v185, v185, v173, s72 bitop3:0xf6\n"
    "v_bitop3_b32 v185, v185, v175, s72 bitop3:0xf6\n"
    "v_bitop3_b32 v185, v185, v177, s72 bitop3:0xf6\n"
    "v_bitop3_b32 v185, v185, v179, s72 bitop3:0xf6\n"
    "v_bitop3_b32 v185, v185, v181, s72 bitop3:0xf6\n"
    "v_bitop3_b32 v185, v185, v183, s72 bitop3:0xf6\n"
    "v_cmp_ne_u32 s[88:89], 0, v185\n"
    "s_cmp_lg_u64 s[88:89], 0\n"
    "s_cbranch_scc1 LB_slow_3%=\n"
    "s_branch LB_back_4%=\n"
    "LB_slow_5%=:\n"
    "s_sub_u32 s73, s73, 1\n"
    "s_cbranch_scc1 L_end%=\n"
    "s_sleep 1\n"
    "buffer_load_dwordx2 v[168:169], v15, s[48:51], s34 offen sc1\n"
    "buffer_load_dwordx2 v[170:171], v15, s[48:51], s34 offen offset:256 sc1\n"
    "buffer_load_dwordx2 v[172:173], v15, s[48:51], s34 offen offset:512 sc1\n"
    "buffer_load_dwordx2 v[174:175], v15, s[48:51], s34 offen offset:768 sc1\n"
    "buffer_load_dwordx2 v[176:177], v15, s[48:51], s34 offen offset:1024 sc1\n"
    "buffer_load_dwordx2 v[178:179], v15, s[48:51], s34 offen offset:1280 sc1\n"
    "buffer_load_dwordx2 v[180:181], v15, s[48:51], s34 offen offset:1536 sc1\n"
    "buffer_load_dwordx2 v[182:183], v15, s[48:51], s34 offen offset:1792 sc1\n"
    "s_waitcnt vmcnt(0)\n"
    "v_xor_b32 v185, s72, v169\n"
    "v_bitop3_b32 v185, v185, v171, s72 bitop3:0xf6\n"
    "v_bitop3_b32 v185, v185, v173, s72 bitop3:0xf6\n"
    "v_bitop3_b32 v185, v185, v175, s72 bitop3:0xf6\n"
    "v_bitop3_b32 v185, v185, v177, s72 bitop3:0xf6\n"
    "v_bitop3_b32 v185, v185, v179, s72 bitop3:0xf6\n"
    "v_bitop3_b32 v185, v185, v181, s72 bitop3:0xf6\n"
    "v_bitop3_b32 v185, v185, v183, s72 bitop3:0xf6\n"
    "v_cmp_ne_u32 s[88:89], 0, v185\n"
    "s_cmp_lg_u64 s[88:89], 0\n"
    "s_cbranch_scc1 LB_slow_5%=\n"
    "s_branch LB_back_6%=\n"
    "LB_slow_7%=:\n"
    "s_sub_u32 s73, s73, 1\n"
    "s_cbranch_scc1 L_end%=\n"
    "s_sleep 1\n"
    "buffer_load_dwordx2 v[168:169], v15, s[48:51], s34 offen sc1\n"
    "buffer_load_dwordx2 v[170:171], v15, s[48:51], s34 offen offset:256 sc1\n"
    "buffer_load_dwordx2 v[172:173], v15, s[48:51], s34 offen offset:512 sc1\n"
    "buffer_load_dwordx2 v[174:175], v15, s[48:51], s34 offen offset:768 sc1\n"
    "buffer_load_dwordx2 v[176:177], v15, s[48:51], s34 offen offset:1024 sc1\n"
    "buffer_load_dwordx2 v[178:179], v15, s[48:51], s34 offen offset:1280 sc1\n"
    "buffer_load_dwordx2 v[180:181], v15, s[48:51], s34 offen offset:1536 sc1\n"
    "buffer_load_dwordx2 v[182:183], v15, s[48:51], s34 offen offset:1792 sc1\n"
    "s_waitcnt vmcnt(0)\n"
    "v_xor_b32 v185, s72, v169\n"
    "v_bitop3_b32 v185, v185, v171, s72 bitop3:0xf6\n"
    "v_bitop3_b32 v185, v185, v173, s72 bitop3:0xf6\n"
    "v_bitop3_b32 v185, v185, v175, s72 bitop3:0xf6\n"
    "v_bitop3_b32 v185, v185, v177, s72 bitop3:0xf6\n"
    "v_bitop3_b32 v185, v185, v179, s72 bitop3:0xf6\n"
    "v_bitop3_b32 v185, v185, v181, s72 bitop3:0xf6\n"
    "v_bitop3_b32 v185, v185, v183, s72 bitop3:0xf6\n"
    "v_cmp_ne_u32 s[88:89], 0, v185\n"
    "s_cmp_lg_u64 s[88:89], 0\n"
    "s_cbranch_scc1 LB_slow_7%=\n"
    "s_branch LB_back_8%=\n"
    "LB_slow_9%=:\n"
    "s_sub_u32 s73, s73, 1\n"
    "s_cbranch_scc1 L_end%=\n"
    "s_sleep 1\n"
    "buffer_load_dwordx2 v[244:245], v246, s[48:51], s8 offen sc1\n"
    "s_waitcnt vmcnt(0)\n"
    "v_cmp_ne_u32 s[88:89], s72, v244\n"
    "v_cmp_ne_u32 s[90:91], s72, v245\n"
    "s_or_b64 s[88:89], s[88:89], s[90:91]\n"
    "s_and_b64 s[88:89], s[88:89], s[94:95]\n"
    "s_cbranch_scc1 LB_slow_9%=\n"
    "s_branch LB_back_10%=\n"
    "LB_slow_11%=:\n"
    "s_sub_u32 s73, s73, 1\n"
    "s_cbranch_scc1 L_end%=\n"
    "s_sleep 1\n"
    "buffer_load_dwordx2 v[244:245], v246, s[48:51], s8 offen sc1\n"
    "s_waitcnt vmcnt(0)\n"
    "v_cmp_ne_u32 s[88:89], s72, v244\n"
    "v_cmp_ne_u32 s[90:91], s72, v245\n"
    "s_or_b64 s[88:89], s[88:89], s[90:91]\n"
    "s_and_b64 s[88:89], s[88:89], s[94:95]\n"
    "s_cbranch_scc1 LB_slow_11%=\n"
    "s_branch LB_back_12%=\n"
    "LB_slow_13%=:\n"
    "s_sub_u32 s73, s73, 1\n"
    "s_cbranch_scc1 L_end%=\n"
    "s_sleep 1\n"
    "buffer_load_dwordx2 v[244:245], v246, s[48:51], s8 offen sc1\n"
    "s_waitcnt vmcnt(0)\n"
    "v_cmp_ne_u32 s[88:89], s72, v244\n"
    "v_cmp_ne_u32 s[90:91], s72, v245\n"
    "s_or_b64 s[88:89], s[88:89], s[90:91]\n"
    "s_and_b64 s[88:89], s[88:89], s[94:95]\n"
    "s_cbranch_scc1 LB_slow_13%=\n"
    "s_branch LB_back_14%=\n"
    "LB_slow_15%=:\n"
    "s_sub_u32 s73, s73, 1\n"
    "s_cbranch_scc1 L_end%=\n"
    "s_sleep 1\n"
    "buffer_load_dwordx2 v[244:245], v246, s[48:51], s8 offen sc1\n"
    "s_waitcnt vmcnt(0)\n"
    "v_cmp_ne_u32 s[88:89], s72, v244\n"
    "v_cmp_ne_u32 s[90:91], s72, v245\n"
    "s_or_b64 s[88:89], s[88:89], s[90:91]\n"
    "s_and_b64 s[88:89], s[88:89], s[94:95]\n"
    "s_cbranch_scc1 LB_slow_15%=\n"
    "s_branch LB_back_16%=\n"
    "LB_slow_17%=:\n"
    "s_sub_u32 s73, s73, 1\n"
    "s_cbranch_scc1 L_end%=\n"
    "s_sleep 1\n"
    "buffer_load_dwordx2 v[244:245], v246, s[48:51], s9 offen sc1\n"
    "s_waitcnt vmcnt(0)\n"
    "v_cmp_ne_u32 s[88:89], s72, v244\n"
    "v_cmp_ne_u32 s[90:91], s72, v245\n"
    "s_or_b64 s[88:89], s[88:89], s[90:91]\n"
    "s_and_b64 s[88:89], s[88:89], s[94:95]\n"
    "s_cbranch_scc1 LB_slow_17%=\n"
    "s_branch LB_back_18%=\n"
    "LB_slow_22%=:\n"
    "s_sub_u32 s73, s73, 1\n"
    "s_cbranch_scc1 L_end%=\n"
    "s_sleep 1\n"
    "buffer_load_dwordx4 v[128:131], v8, s[48:51], s87 offen sc1\n"
    "buffer_load_dwordx4 v[132:135], v8, s[48:51], s87 offen offset:1024 sc1\n"
    "buffer_load_dwordx4 v[136:139], v8, s[48:51], s87 offen offset:2048 sc1\n"
    "buffer_load_dwordx4 v[140:143], v8, s[48:51], s87 offen offset:3072 sc1\n"
    "s_waitcnt vmcnt(0)\n"
    "v_xor_b32 v195, s72, v129\n"
    "v_bitop3_b32 v195, v195, v131, s72 bitop3:0xf6\n"
    "v_bitop3_b32 v195, v195, v133, s72 bitop3:0xf6\n"
    "v_bitop3_b32 v195, v195, v135, s72 bitop3:0xf6\n"
    "v_bitop3_b32 v195, v195, v137, s72 bitop3:0xf6\n"
    "v_bitop3_b32 v195, v195, v139, s72 bitop3:0xf6\n"
    "v_bitop3_b32 v195, v195, v141, s72 bitop3:0xf6\n"
    "v_bitop3_b32 v195, v195, v143, s72 bitop3:0xf6\n"
    "v_cmp_ne_u32 s[88:89], 0, v195\n"
    "s_cmp_lg_u64 s[88:89], 0\n"
    "s_cbranch_scc1 LB_slow_22%=\n"
    "s_branch LB_back_23%=\n"
    "L_end%=:\n"
    :
    : "s"(__builtin_amdgcn_kernarg_segment_ptr()), "s"(blk), "s"(wave), "s"((unsigned)(size_t)lds)
    : "v0", "v1", "v2", "v3", "v4", "v5", "v6", "v7", "v8", "v9", "v10", "v11", "v12", "v13", "v14", "v15", "v16", "v17", "v18", "v19", "v20", "v21", "v22", "v23",
    "v24", "v25", "v26", "v27", "v28", "v29", "v30", "v31", "v32", "v33", "v34", "v35", "v36", "v37", "v38", "v39", "v40", "v41", "v42", "v43", "v44", "v45", "v46", "v47",
    "v48", "v49", "v50", "v51", "v52", "v53", "v54", "v55", "v56", "v57", "v58", "v59", "v60", "v61", "v62", "v63", "v64", "v65", "v66", "v67", "v68", "v69", "v70", "v71",
    "v72", "v73", "v74", "v75", "v76", "v77", "v78", "v79", "v80", "v81", "v82", "v83", "v84", "v85", "v86", "v87", "v88", "v89", "v90", "v91", "v92", "v93", "v94", "v95",
    "v96", "v97", "v98", "v99", "v100", "v101", "v102", "v103", "v104", "v105", "v106", "v107", "v108", "v109", "v110", "v111", "v112", "v113", "v114", "v115", "v116", "v117", "v118", "v119",
    "v120", "v121", "v122", "v123", "v124", "v125", "v126", "v127", "v128", "v129", "v130", "v131", "v132", "v133", "v134", "v135", "v136", "v137", "v138", "v139", "v140", "v141", "v142", "v143",
    "v144", "v145", "v146", "v147", "v148", "v149", "v150", "v151", "v152", "v153", "v154", "v155", "v156", "v157", "v158", "v159", "v160", "v161", "v162", "v163", "v164", "v165", "v166", "v167",
    "v168", "v169", "v170", "v171", "v172", "v173", "v174", "v175", "v176", "v177", "v178", "v179", "v180", "v181", "v182", "v183", "v184", "v185", "v186", "v187", "v188", "v189", "v190", "v191",
    "v192", "v193", "v194", "v195", "v196", "v197", "v198", "v199", "v200", "v201", "v202", "v203", "v204", "v205", "v206", "v207", "v208", "v209", "v210", "v211", "v212", "v213", "v214", "v215",
    "v216", "v217", "v218", "v219", "v220", "v221", "v222", "v223", "v224", "v225", "v226", "v227", "v228", "v229", "v230", "v231", "v232", "v233", "v234", "v235", "v236", "v237", "v238", "v239",
    "v240", "v241", "v242", "v243", "v244", "v245", "v246", "v247", "v248", "v249", "v250", "v251", "v252", "v253", "v254", "v255", "a0", "a1", "a2", "a3", "a4", "a5", "a6", "a7",
    "a8", "a9", "a10", "a11", "a12", "a13", "a14", "a15", "a16", "a17", "a18", "a19", "a20", "a21", "a22", "a23", "a24", "a25", "a26", "a27", "a28", "a29", "a30", "a31",
    "a32", "a33", "a34", "a35", "a36", "a37", "a38", "a39", "a40", "a41", "a42", "a43", "a44", "a45", "a46", "a47", "a48", "a49", "a50", "a51", "a52", "a53", "a54", "a55",
    "a56", "a57", "a58", "a59", "a60", "a61", "a62", "a63", "a64", "a65", "a66", "a67", "a68", "a69", "a70", "a71", "a72", "a73", "a74", "a75", "a76", "a77", "a78", "a79",
    "a80", "a81", "a82", "a83", "a84", "a85", "a86", "a87", "a88", "a89", "a90", "a91", "a92", "a93", "a94", "a95", "a96", "a97", "a98", "a99", "a100", "a101", "a102", "a103",
    "a104", "a105", "a106", "a107", "a108", "a109", "a110", "a111", "a112", "a113", "a114", "a115", "a116", "a117", "a118", "a119", "a120", "a121", "a122", "a123", "a124", "a125", "a126", "a127",
    "a128", "a129", "a130", "a131", "a132", "a133", "a134", "a135", "a136", "a137", "a138", "a139", "a140", "a141", "a142", "a143", "a144", "a145", "a146", "a147", "a148", "a149", "a150", "a151",
    "a152", "a153", "a154", "a155", "a156", "a157", "a158", "a159", "a160", "a161", "a162", "a163", "a164", "a165", "a166", "a167", "a168", "a169", "a170", "a171", "a172", "a173", "a174", "a175",
    "a176", "a177", "a178", "a179", "a180", "a181", "a182", "a183", "a184", "a185", "a186", "a187", "a188", "a189", "a190", "a191", "a192", "a193", "a194", "a195", "a196", "a197", "a198", "a199",
    "a200", "a201", "a202", "a203", "a204", "a205", "a206", "a207", "a208", "a209", "a210", "a211", "a212", "a213", "a214", "a215", "a216", "a217", "a218", "a219", "a220", "a221", "a222", "a223",
    "a224", "a225", "a226", "a227", "a228", "a229", "a230", "a231", "a232", "a233", "a234", "a235", "a236", "a237", "a238", "a239", "a240", "a241", "a242", "a243", "a244", "a245", "a246", "a247",
    "a248", "a249", "a250", "a251", "a252", "a253", "a254", "a255", "s8", "s9", "s10", "s11", "s12", "s13", "s14", "s15", "s16", "s17", "s18", "s19", "s20", "s21", "s22", "s23",
    "s24", "s25", "s26", "s27", "s28", "s29", "s30", "s31", "s34", "s35", "s36", "s37", "s38", "s39", "s40", "s41", "s42", "s43", "s44", "s45", "s46", "s47", "s48", "s49",
    "s50", "s51", "s52", "s53", "s54", "s55", "s56", "s57", "s58", "s59", "s60", "s61", "s62", "s63", "s64", "s65", "s66", "s67", "s68", "s69", "s70", "s71", "s72", "s73",
    "s74", "s75", "s76", "s77", "s78", "s79", "s80", "s81", "s82", "s83", "s84", "s85", "s86", "s87", "s88", "s89", "s90", "s91", "s92", "s93", "s94", "s95", "s96", "s97",
    "s98", "s99", "vcc", "scc", "memory");
}

extern "C" void kernel_launch(void* const* d_in, const int* in_sizes, int n_in, void* d_out, int out_size, void* d_ws, size_t ws_size,
                              hipStream_t stream) {
  hedgehog_glu_block<<<256, 256, 0, stream>>>(d_in[0], d_in[1], d_in[2], d_in[3], d_in[5], d_in[6], d_in[7], d_out, d_ws);
}
```
